```python
import torch

def _softmax(x, axis=-1):
    return torch.softmax(x, dim=axis)
def _gelu(x):
    return torch.nn.functional.gelu(x, approximate="tanh")
def _silu(x):
    return torch.nn.functional.silu(x)
def _sigmoid(x):
    return torch.sigmoid(x)
def _layernorm(x, axis=-1, eps=1e-5):
    m = x.mean(dim=axis, keepdim=True)
    v = x.var(dim=axis, keepdim=True, unbiased=False)
    return (x - m) / torch.sqrt(v + eps)
def _scatter_add(tgt, idx, src, axis=0):
    return tgt.clone().index_add_(axis, idx, src)
def _segment_sum(src, seg, n):
    out = torch.zeros((n,) + src.shape[1:], dtype=src.dtype)
    return out.index_add_(0, seg, src)
def _pad(x, pw):
    flat = tuple(v for pair in reversed(pw) for v in pair)
    return torch.nn.functional.pad(x, flat)

kernel_name = 'concat_heads__gqa_block_cached__gqa_block_cached__near_ridge'

def setup_inputs():

    _w = (lambda a: (lambda t: t.to(torch.bfloat16) if t.is_floating_point() else t)(torch.as_tensor(a)))
    def _A_setup():
        B, Sq, D = 8, 160, 576
        Hq, Hkv, Sk, Dh = 32, 16, 1024, 128
        assert Hq % Hkv == 0, (Hq, Hkv)
        return {
            "x": _w((lambda *s: torch.randn(*s, dtype=torch.bfloat16) * 0.8)(B, Sq, D)),
            "k_cache": _w((lambda *s: torch.randn(*s, dtype=torch.bfloat16) * 0.8)(B, Hkv, Sk, Dh)),
            "v_cache": _w((lambda *s: torch.randn(*s, dtype=torch.bfloat16) * 0.05)(B, Hkv, Sk, Dh)),
            "wq": _w((lambda *s: torch.randn(*s, dtype=torch.bfloat16) * 0.8)(D, Hq * Dh) * (D ** -0.5)),
            "wo": _w((lambda *s: torch.randn(*s, dtype=torch.bfloat16) * 0.05)(Hq * Dh, D) * ((Hq * Dh) ** -0.5)),
        }
    _A_in = _A_setup()

    def _B_setup():
        B, Sq, D = 8, 160, 576
        Hq, Hkv, Sk, Dh = 32, 16, 1024, 128
        assert Hq % Hkv == 0, (Hq, Hkv)
        return {
            "x": _w((lambda *s: torch.randn(*s, dtype=torch.bfloat16) * 0.8)(B, Sq, D)),
            "k_cache": _w((lambda *s: torch.randn(*s, dtype=torch.bfloat16) * 0.8)(B, Hkv, Sk, Dh)),
            "v_cache": _w((lambda *s: torch.randn(*s, dtype=torch.bfloat16) * 0.05)(B, Hkv, Sk, Dh)),
            "wq": _w((lambda *s: torch.randn(*s, dtype=torch.bfloat16) * 0.8)(D, Hq * Dh) * (D ** -0.5)),
            "wo": _w((lambda *s: torch.randn(*s, dtype=torch.bfloat16) * 0.05)(Hq * Dh, D) * ((Hq * Dh) ** -0.5)),
        }
    _B_in = _B_setup()

    return {
        "A_x": _A_in["x"],
        "A_k_cache": _A_in["k_cache"],
        "A_v_cache": _A_in["v_cache"],
        "A_wq": _A_in["wq"],
        "A_wo": _A_in["wo"],
        "B_x": _B_in["x"],
        "B_k_cache": _B_in["k_cache"],
        "B_v_cache": _B_in["v_cache"],
        "B_wq": _B_in["wq"],
        "B_wo": _B_in["wo"],
    }

def reference(A_x, A_k_cache, A_v_cache, A_wq, A_wo, B_x, B_k_cache, B_v_cache, B_wq, B_wo):
    def _A_ref(x, k_cache, v_cache, wq, wo):
        B, Sq, D = x.shape
        Hq, Dh = 32, 128
        Hkv = k_cache.shape[1]
        rep = Hq // Hkv
        q = (lambda x, *d: x.permute(*d))((x @ wq).reshape(B, Sq, Hq, Dh), 0, 2, 1, 3)
        k = (lambda x, n, axis: torch.repeat_interleave(x, n, dim=axis))(k_cache, rep, axis=1)
        v = (lambda x, n, axis: torch.repeat_interleave(x, n, dim=axis))(v_cache, rep, axis=1)
        scores = torch.einsum("bhqd,bhkd->bhqk", q, k) * (Dh ** -0.5)
        p = _softmax(scores, axis=-1)
        o = torch.einsum("bhqk,bhkd->bhqd", p, v)
        return (lambda x, *d: x.permute(*d))(o, 0, 2, 1, 3).reshape(B, Sq, Hq * Dh) @ wo

    def _B_ref(x, k_cache, v_cache, wq, wo):
        B, Sq, D = x.shape
        Hq, Dh = 32, 128
        Hkv = k_cache.shape[1]
        rep = Hq // Hkv
        q = (lambda x, *d: x.permute(*d))((x @ wq).reshape(B, Sq, Hq, Dh), 0, 2, 1, 3)
        k = (lambda x, n, axis: torch.repeat_interleave(x, n, dim=axis))(k_cache, rep, axis=1)
        v = (lambda x, n, axis: torch.repeat_interleave(x, n, dim=axis))(v_cache, rep, axis=1)
        scores = torch.einsum("bhqd,bhkd->bhqk", q, k) * (Dh ** -0.5)
        p = _softmax(scores, axis=-1)
        o = torch.einsum("bhqk,bhkd->bhqd", p, v)
        return (lambda x, *d: x.permute(*d))(o, 0, 2, 1, 3).reshape(B, Sq, Hq * Dh) @ wo

    a = _A_ref(A_x, A_k_cache, A_v_cache, A_wq, A_wo)
    b = _B_ref(B_x, B_k_cache, B_v_cache, B_wq, B_wo)
    return torch.concatenate([a, b], axis=-1)
```

```cpp
#include <hip/hip_runtime.h>
#include <hip/hip_bf16.h>
typedef __attribute__((ext_vector_type(8))) short bf16x8;
typedef __attribute__((ext_vector_type(16))) float f32x16;
typedef unsigned short u16;
#define MFMA(a,b,c) __builtin_amdgcn_mfma_f32_32x32x16_bf16((a),(b),(c),0,0,0)
__device__ inline u16 f2bf(float x){ unsigned u=__float_as_uint(x); u+=0x7fffu+((u>>16)&1u); return (u16)(u>>16);}
__device__ inline int crow(int r,int h){return (r&3)+8*(r>>2)+4*h;}
__global__ __launch_bounds__(64) void gemm(const u16* A0,const u16* B0,u16* C0,const u16* A1,const u16* B1,u16* C1,int N,int K,int ldc){
  const u16* A=blockIdx.z?A1:A0; const u16* B=blockIdx.z?B1:B0; u16* C=blockIdx.z?C1:C0;
  int l=threadIdx.x,r=l&31,h=l>>5; int m0=blockIdx.y*32,n0=blockIdx.x*32;
  f32x16 acc={};
  for(int k=0;k<K;k+=16){
    bf16x8 a=*(const bf16x8*)(A+(size_t)(m0+r)*K+k+8*h);
    bf16x8 b; for(int j=0;j<8;j++) b[j]=B[(size_t)(k+8*h+j)*N+n0+r];
    acc=MFMA(a,b,acc);
  }
  for(int i=0;i<16;i++) C[(size_t)(m0+crow(i,h))*ldc+n0+r]=f2bf(acc[i]);
}
__global__ __launch_bounds__(64) void attn(const u16* Q0,const u16* K0,const u16* V0,u16* O0,const u16* Q1,const u16* K1,const u16* V1,u16* O1){
  int br=blockIdx.z; const u16* Q=br?Q1:Q0; const u16* Kc=br?K1:K0; const u16* Vc=br?V1:V0; u16* O=br?O1:O0;
  int l=threadIdx.x,r=l&31,h=l>>5; int rb=blockIdx.x; int bh=blockIdx.y; int b=bh>>5, hq=bh&31, kv=hq>>1;
  const u16* Kp=Kc+(size_t)(b*16+kv)*1024*128; const u16* Vp=Vc+(size_t)(b*16+kv)*1024*128;
  const u16* qrow=Q+(size_t)(b*160+rb*32+r)*4096+hq*128;
  bf16x8 qf[8];
  #pragma unroll
  for(int s=0;s<8;s++) qf[s]=*(const bf16x8*)(qrow+16*s+8*h);
  f32x16 o[4]={}; float m=-1e30f,lsum=0.f; const float c=0.12751743082f;
  for(int k0=0;k0<1024;k0+=32){
    f32x16 s={};
    #pragma unroll
    for(int d=0;d<8;d++){ bf16x8 kf=*(const bf16x8*)(Kp+(size_t)(k0+r)*128+16*d+8*h); s=MFMA(kf,qf[d],s);}
    float mx=s[0]; for(int i=1;i<16;i++) mx=fmaxf(mx,s[i]); mx=fmaxf(mx,__shfl_xor(mx,32)); mx*=c;
    float mn=fmaxf(m,mx), al=exp2f(m-mn); m=mn; float ps=0;
    for(int i=0;i<16;i++){ s[i]=exp2f(s[i]*c-mn); ps+=s[i];}
    ps+=__shfl_xor(ps,32); lsum=lsum*al+ps;
    #pragma unroll
    for(int db=0;db<4;db++) for(int i=0;i<16;i++) o[db][i]*=al;
    #pragma unroll
    for(int ks=0;ks<2;ks++){ bf16x8 p;
      #pragma unroll
      for(int j=0;j<8;j++) p[j]=f2bf(s[8*ks+j]);
      #pragma unroll
      for(int db=0;db<4;db++){ bf16x8 v;
        #pragma unroll
        for(int j=0;j<8;j++) v[j]=Vp[(size_t)(k0+16*ks+8*(j>>2)+4*h+(j&3))*128+32*db+r];
        o[db]=MFMA(v,p,o[db]); } }
  }
  float inv=1.f/lsum; u16* orow=O+(size_t)(b*160+rb*32+r)*4096+hq*128;
  #pragma unroll
  for(int db=0;db<4;db++) for(int i=0;i<16;i++) orow[32*db+crow(i,h)]=f2bf(o[db][i]*inv);
}
extern "C" void kernel_launch(void* const* d_in,const int* in_sizes,int n_in,void* d_out,int out_size,void* d_ws,size_t ws_size,hipStream_t stream){
  const u16* xA=(const u16*)d_in[0],*kA=(const u16*)d_in[1],*vA=(const u16*)d_in[2],*wqA=(const u16*)d_in[3],*woA=(const u16*)d_in[4];
  const u16* xB=(const u16*)d_in[5],*kB=(const u16*)d_in[6],*vB=(const u16*)d_in[7],*wqB=(const u16*)d_in[8],*woB=(const u16*)d_in[9];
  u16* ws=(u16*)d_ws; size_t n=(size_t)1280*4096; u16* qA=ws,*qB=ws+n,*oA=ws+2*n,*oB=ws+3*n; u16* out=(u16*)d_out;
  gemm<<<dim3(128,40,2),64,0,stream>>>(xA,wqA,qA,xB,wqB,qB,4096,576,4096);
  attn<<<dim3(5,256,2),64,0,stream>>>(qA,kA,vA,oA,qB,kB,vB,oB);
  gemm<<<dim3(18,40,2),64,0,stream>>>(oA,woA,out,oB,woB,out+576,576,4096,1152);
}
```

```cpp
#include <hip/hip_runtime.h>
#include <hip/hip_bf16.h>
typedef __attribute__((ext_vector_type(8))) short bf16x8;
typedef __attribute__((ext_vector_type(16))) float f32x16;
typedef unsigned short u16;
#define MFMA(a,b,c) __builtin_amdgcn_mfma_f32_32x32x16_bf16((a),(b),(c),0,0,0)
__device__ inline u16 f2bf(float x){ unsigned u=__float_as_uint(x); u+=0x7fffu+((u>>16)&1u); return (u16)(u>>16);}
__device__ inline int crow(int r,int h){return (r&3)+8*(r>>2)+4*h;}
__global__ __launch_bounds__(64) void gemm(const u16* A0,const u16* B0,u16* C0,const u16* A1,const u16* B1,u16* C1,int N,int K,int ldc){
  const u16* A=blockIdx.z?A1:A0; const u16* B=blockIdx.z?B1:B0; u16* C=blockIdx.z?C1:C0;
  int l=threadIdx.x,r=l&31,h=l>>5; int m0=blockIdx.y*32,n0=blockIdx.x*32;
  f32x16 acc={};
  for(int k=0;k<K;k+=16){
    bf16x8 a=*(const bf16x8*)(A+(size_t)(m0+r)*K+k+8*h);
    bf16x8 b; for(int j=0;j<8;j++) b[j]=B[(size_t)(k+8*h+j)*N+n0+r];
    acc=MFMA(a,b,acc);
  }
  for(int i=0;i<16;i++) C[(size_t)(m0+crow(i,h))*ldc+n0+r]=f2bf(acc[i]);
}
__global__ __launch_bounds__(64) void attn(const u16* Q0,const u16* K0,const u16* V0,u16* O0,const u16* Q1,const u16* K1,const u16* V1,u16* O1){
  int br=blockIdx.z; const u16* Q=br?Q1:Q0; const u16* Kc=br?K1:K0; const u16* Vc=br?V1:V0; u16* O=br?O1:O0;
  int l=threadIdx.x,r=l&31,h=l>>5; int rb=blockIdx.x; int bh=blockIdx.y; int b=bh>>5, hq=bh&31, kv=hq>>1;
  const u16* Kp=Kc+(size_t)(b*16+kv)*1024*128; const u16* Vp=Vc+(size_t)(b*16+kv)*1024*128;
  const u16* qrow=Q+(size_t)(b*160+rb*32+r)*4096+hq*128;
  bf16x8 qf[8];
  #pragma unroll
  for(int s=0;s<8;s++) qf[s]=*(const bf16x8*)(qrow+16*s+8*h);
  f32x16 o[4]={}; float m=-1e30f,lsum=0.f; const float c=0.12751743082f;
  for(int k0=0;k0<1024;k0+=32){
    f32x16 s={};
    #pragma unroll
    for(int d=0;d<8;d++){ bf16x8 kf=*(const bf16x8*)(Kp+(size_t)(k0+r)*128+16*d+8*h); s=MFMA(kf,qf[d],s);}
    float mx=s[0]; for(int i=1;i<16;i++) mx=fmaxf(mx,s[i]); mx=fmaxf(mx,__shfl_xor(mx,32)); mx*=c;
    float mn=fmaxf(m,mx), al=exp2f(m-mn); m=mn; float ps=0;
    for(int i=0;i<16;i++){ s[i]=exp2f(s[i]*c-mn); ps+=s[i];}
    ps+=__shfl_xor(ps,32); lsum=lsum*al+ps;
    #pragma unroll
    for(int db=0;db<4;db++) for(int i=0;i<16;i++) o[db][i]*=al;
    #pragma unroll
    for(int ks=0;ks<2;ks++){ bf16x8 p;
      #pragma unroll
      for(int j=0;j<8;j++) p[j]=f2bf(s[8*ks+j]);
      #pragma unroll
      for(int db=0;db<4;db++){ bf16x8 v;
        #pragma unroll
        for(int j=0;j<8;j++) v[j]=Vp[(size_t)(k0+16*ks+8*(j>>2)+4*h+(j&3))*128+32*db+r];
        o[db]=MFMA(v,p,o[db]); } }
  }
  float inv=1.f/lsum; u16* orow=O+(size_t)(b*160+rb*32+r)*4096+hq*128;
  #pragma unroll
  for(int db=0;db<4;db++) for(int i=0;i<16;i++) orow[32*db+crow(i,h)]=f2bf(o[db][i]*inv);
}

typedef unsigned u32x4 __attribute__((ext_vector_type(4)));
template<int POL> __global__ __launch_bounds__(256,1) void strm(const char* kA,const char* vA,const char* kB,const char* vB, unsigned* o){
  extern __shared__ char lds[];
  int w=__builtin_amdgcn_readfirstlane(threadIdx.x>>6); int lane=threadIdx.x&63;
  int br=blockIdx.x>>7, u=blockIdx.x&127;
  __amdgpu_buffer_rsrc_t sk=__builtin_amdgcn_make_buffer_rsrc((void*)(br?kB:kA),0,33554432,0x20000);
  __amdgpu_buffer_rsrc_t sv=__builtin_amdgcn_make_buffer_rsrc((void*)(br?vB:vA),0,33554432,0x20000);
  unsigned voff=(lane>>3)*256+(lane&7)*16;
  unsigned soff=u*262144+w*4096;
  unsigned long long t0=__builtin_amdgcn_s_memrealtime();
  for(int t=0;t<16;t++){
    unsigned mb=w*4096+(t%4)*32768;
    #pragma unroll
    for(int i=0;i<8;i++){
      unsigned m=mb+(i&3)*1024+(i>>2)*16384, so=soff+((i>>1)&1)*2048+(i&1)*128;
      if(POL==0){ if(i<4) asm volatile("s_mov_b32 m0, %0\n s_nop 0\n buffer_load_dwordx4 %1, %2, %3 offen lds"::"s"(m),"v"(voff),"s"(sk),"s"(so):"memory");
                  else asm volatile("s_mov_b32 m0, %0\n s_nop 0\n buffer_load_dwordx4 %1, %2, %3 offen lds"::"s"(m),"v"(voff),"s"(sv),"s"(so):"memory"); }
      if(POL==1){ if(i<4) asm volatile("s_mov_b32 m0, %0\n s_nop 0\n buffer_load_dwordx4 %1, %2, %3 offen nt lds"::"s"(m),"v"(voff),"s"(sk),"s"(so):"memory");
                  else asm volatile("s_mov_b32 m0, %0\n s_nop 0\n buffer_load_dwordx4 %1, %2, %3 offen nt lds"::"s"(m),"v"(voff),"s"(sv),"s"(so):"memory"); }
    }
    soff+=16384;
    asm volatile("s_waitcnt vmcnt(16)":::"memory");
  }
  asm volatile("s_waitcnt vmcnt(0)":::"memory");
  unsigned long long t1=__builtin_amdgcn_s_memrealtime();
  if(lane==0){ o[(blockIdx.x*4+w)*2]=(unsigned)t0; o[(blockIdx.x*4+w)*2+1]=(unsigned)t1; }
}
template<int POL> __global__ __launch_bounds__(256,1) void wr(char* p, unsigned* o){
  extern __shared__ char lds[];
  int w=__builtin_amdgcn_readfirstlane(threadIdx.x>>6); int lane=threadIdx.x&63;
  __amdgpu_buffer_rsrc_t srd=__builtin_amdgcn_make_buffer_rsrc((void*)p,0,256*184320,0x20000);
  unsigned long long t0=__builtin_amdgcn_s_memrealtime();
  u32x4 v={(unsigned)lane*77u,blockIdx.x*3u,7u,5u};
  for(int i=0;i<45;i++){ v[3]+=v[0]; __builtin_amdgcn_raw_buffer_store_b128(v,srd,lane*16,blockIdx.x*184320+(i*4+w)*1024,POL); }
  asm volatile("s_waitcnt vmcnt(0)":::"memory");
  unsigned long long t1=__builtin_amdgcn_s_memrealtime();
  if(lane==0){ o[(blockIdx.x*4+w)*2]=(unsigned)t0; o[(blockIdx.x*4+w)*2+1]=(unsigned)t1; }
}
__global__ __launch_bounds__(64) void nop0(unsigned* o){ o[blockIdx.x*64+threadIdx.x]=100; }
__global__ __launch_bounds__(64) void amp(const unsigned* o){
  unsigned mn=~0u,mx=0; for(int j=threadIdx.x;j<1024;j+=64){ mn=min(mn,o[2*j]); mx=max(mx,o[2*j+1]); }
  for(int s=32;s;s>>=1){ mn=min(mn,(unsigned)__shfl_xor((int)mn,s)); mx=max(mx,(unsigned)__shfl_xor((int)mx,s)); }
  unsigned d=mx-mn; if(d>20000) d=20000;
  unsigned long long t0=__builtin_amdgcn_s_memrealtime();
  while(__builtin_amdgcn_s_memrealtime()-t0 < (unsigned long long)d*20) __builtin_amdgcn_s_sleep(1);
}
extern "C" void kernel_launch(void* const* d_in,const int* in_sizes,int n_in,void* d_out,int out_size,void* d_ws,size_t ws_size,hipStream_t stream){
  const u16* xA=(const u16*)d_in[0],*kA=(const u16*)d_in[1],*vA=(const u16*)d_in[2],*wqA=(const u16*)d_in[3],*woA=(const u16*)d_in[4];
  const u16* xB=(const u16*)d_in[5],*kB=(const u16*)d_in[6],*vB=(const u16*)d_in[7],*wqB=(const u16*)d_in[8],*woB=(const u16*)d_in[9];
  u16* ws=(u16*)d_ws; size_t n=(size_t)1280*4096; u16* qA=ws,*qB=ws+n,*oA=ws+2*n,*oB=ws+3*n; u16* out=(u16*)d_out;
  unsigned* st=(unsigned*)((char*)d_ws+(60<<20));
  nop0<<<32,64,0,stream>>>(st);
  gemm<<<dim3(128,40,2),64,0,stream>>>(xA,wqA,qA,xB,wqB,qB,4096,576,4096);
  attn<<<dim3(5,256,2),64,0,stream>>>(qA,kA,vA,oA,qB,kB,vB,oB);
  gemm<<<dim3(18,40,2),64,0,stream>>>(oA,woA,out,oB,woB,out+576,576,4096,1152);
  amp<<<1,64,0,stream>>>(st);
}
```

```cpp
#include <hip/hip_runtime.h>
#include <hip/hip_bf16.h>

extern "C" __global__ __launch_bounds__(256, 1) void gqa_fused(const void* xA, const void* kA, const void* vA, const void* wqA, const void* woA,
                                                               const void* xB, const void* kB, const void* vB, const void* wqB, const void* woB,
                                                               void* out, void* ws) {
  asm volatile(
"s_mov_b32 s18, %1\n"
"s_mov_b32 s19, %2\n"
"s_mov_b32 s85, 0\n"
"L_start_%=:\n"
"s_and_b32 s25, s18, 4\n"
"s_mul_i32 s25, s25, 10\n"
"s_load_dwordx2 s[44:45], %0, s25\n"
"s_load_dwordx2 s[48:49], %0, s25 offset:0x18\n"
"v_mbcnt_lo_u32_b32 v213, -1, 0\n"
"v_mbcnt_hi_u32_b32 v213, -1, v213\n"
"s_getpc_b64 s[20:21]\n"
"s_lshr_b32 s23, s18, 3\n"
"s_lshl_b32 s23, s23, 2\n"
"s_add_u32 s23, s23, s19\n"
"s_mul_i32 s23, s23, 0x240\n"
"v_min_u32 v241, 8, v213\n"
"v_lshlrev_b32 v241, 6, v241\n"
"v_add_u32 v241, s23, v241\n"
"v_min_u32 v241, 0xfcc0, v241\n"
"global_load_dword v240, v241, s[20:21]\n"
"s_lshr_b32 s43, s18, 5\n"
"s_lshr_b32 s22, s18, 3\n"
"s_and_b32 s22, s22, 3\n"
"s_and_b32 s20, s18, 3\n"
"s_lshl2_add_u32 s42, s20, s22\n"
"v_lshrrev_b32 v242, 3, v213\n"
"v_and_b32 v243, 7, v213\n"
"v_and_b32 v244, 6, v242\n"
"v_xor_b32 v244, v244, v243\n"
"v_lshlrev_b32 v244, 4, v244\n"
"s_movk_i32 s23, 0x480\n"
"v_mad_u32_u24 v199, v242, s23, v244\n"
"s_and_b32 s20, s19, 1\n"
"s_lshr_b32 s21, s19, 1\n"
"s_lshl2_add_u32 s21, s21, s20\n"
"v_and_b32 v245, 2, v242\n"
"v_lshlrev_b32 v245, 1, v245\n"
"v_or_b32 v245, s20, v245\n"
"v_xor_b32 v245, v245, v243\n"
"v_lshlrev_b32 v245, 4, v245\n"
"v_lshl_add_u32 v200, v242, 13, v245\n"
"s_lshr_b32 s20, s43, 2\n"
"s_lshl1_add_u32 s20, s20, s21\n"
"s_and_b32 s23, s43, 3\n"
"s_lshl2_add_u32 s24, s20, s23\n"
"s_lshl_b32 m0, s24, 10\n"
"s_lshl_b32 s99, s20, 9\n"
"s_add_u32 s99, s99, s23\n"
"s_lshl_b32 s99, s99, 7\n"
"s_mul_i32 s24, s19, 5\n"
"s_add_u32 s24, s24, s22\n"
"s_mul_i32 s74, s24, 0x2400\n"
"s_mov_b32 s46, 0x2d000\n"
"s_lshl_b32 s47, 1, 17\n"
"s_mov_b32 s51, s47\n"
"s_lshl_b32 s23, s42, 9\n"
"s_sub_u32 s50, 0x480000, s23\n"
"s_mul_i32 s20, s43, 0x2d000\n"
"s_waitcnt lgkmcnt(0)\n"
"s_add_u32 s48, s48, s23\n"
"s_addc_u32 s49, s49, 0\n"
"buffer_load_dwordx4 v200, s[48:51], s99 offen offset:0 lds\n"
"s_lshl_b32 m0, s24, 10\n"
"s_bitset1_b32 m0, 15\n"
"s_add_u32 s44, s44, s20\n"
"s_addc_u32 s45, s45, 0\n"
"buffer_load_dwordx4 v199, s[44:47], s74 offen offset:0 lds\n"
"s_add_u32 m0, m0, 0x400\n"
"s_add_u32 s74, s74, 0x2400\n"
"buffer_load_dwordx4 v199, s[44:47], s74 offen offset:0 lds\n"
"s_load_dwordx2 s[52:53], %0, s25 offset:0x8\n"
"s_load_dwordx2 s[56:57], %0, s25 offset:0x10\n"
"s_load_dwordx2 s[60:61], %0, s25 offset:0x20\n"
"s_load_dwordx2 s[72:73], %0, 0x58\n"
"s_load_dwordx2 s[82:83], %0, 0x50\n"
"s_bfe_u32 s41, s18, 0x10002\n"
"v_lshl_add_u32 v198, v242, 8, v244\n"
"s_lshl_b32 s79, s21, 12\n"
"s_lshl_b32 s26, s21, 16\n"
"s_mul_i32 s80, s19, 0x1400\n"
"s_add_u32 s80, s80, 0x8000\n"
"s_mul_i32 s27, s19, 0xb400\n"
"v_add_u32 v246, s43, v213\n"
"v_and_b32 v247, 4, v246\n"
"v_and_b32 v246, 3, v246\n"
"v_lshlrev_b32 v248, 15, v247\n"
"v_lshl_add_u32 v248, v246, 7, v248\n"
"v_add_u32 v248, s26, v248\n"
"v_lshlrev_b32 v249, 11, v247\n"
"v_lshl_add_u32 v249, v246, 10, v249\n"
"v_add_u32 v249, s79, v249\n"
"v_add_u32 v246, s22, v213\n"
"v_add_u32 v247, -5, v246\n"
"v_min_u32 v246, v246, v247\n"
"v_mul_u32_u24 v242, 0x2400, v246\n"
"v_add_u32 v242, s27, v242\n"
"v_lshlrev_b32 v243, 10, v246\n"
"v_add_u32 v243, s80, v243\n"
"v_readlane_b32 s96, v242, 2\n"
"v_readlane_b32 s38, v243, 2\n"
"v_readlane_b32 s97, v242, 3\n"
"v_readlane_b32 s39, v243, 3\n"
"v_readlane_b32 s98, v242, 4\n"
"v_readlane_b32 s40, v243, 4\n"
"v_readlane_b32 s94, v242, 0\n"
"v_readlane_b32 s36, v243, 0\n"
"v_readlane_b32 s95, v242, 1\n"
"v_readlane_b32 s37, v243, 1\n"
"v_readlane_b32 s87, v248, 1\n"
"v_readlane_b32 s29, v249, 1\n"
"v_readlane_b32 s88, v248, 2\n"
"v_readlane_b32 s30, v249, 2\n"
"v_readlane_b32 s89, v248, 3\n"
"v_readlane_b32 s31, v249, 3\n"
"v_readlane_b32 s90, v248, 4\n"
"v_readlane_b32 s32, v249, 4\n"
"v_readlane_b32 s91, v248, 5\n"
"v_readlane_b32 s33, v249, 5\n"
"v_readlane_b32 s92, v248, 6\n"
"v_readlane_b32 s34, v249, 6\n"
"v_readlane_b32 s93, v248, 7\n"
"v_readlane_b32 s35, v249, 7\n"
"v_readlane_b32 s86, v248, 0\n"
"v_readlane_b32 s28, v249, 0\n"
"s_add_u32 s74, s96, 0\n"
"s_add_u32 m0, s38, 0\n"
"s_nop 0\n"
"buffer_load_dwordx4 v199, s[44:47], s74 offen offset:0 lds\n"
"s_add_u32 s74, s97, 0\n"
"s_add_u32 m0, s39, 0\n"
"s_nop 0\n"
"buffer_load_dwordx4 v199, s[44:47], s74 offen offset:0 lds\n"
"s_add_u32 s74, s98, 0\n"
"s_add_u32 m0, s40, 0\n"
"s_nop 0\n"
"buffer_load_dwordx4 v199, s[44:47], s74 offen offset:0 lds\n"
"s_add_u32 s99, s87, 0\n"
"s_add_u32 m0, s29, 0\n"
"s_nop 0\n"
"buffer_load_dwordx4 v200, s[48:51], s99 offen offset:0 lds\n"
"s_add_u32 s99, s88, 0\n"
"s_add_u32 m0, s30, 0\n"
"s_nop 0\n"
"buffer_load_dwordx4 v200, s[48:51], s99 offen offset:0 lds\n"
"s_add_u32 s99, s89, 0\n"
"s_add_u32 m0, s31, 0\n"
"s_nop 0\n"
"buffer_load_dwordx4 v200, s[48:51], s99 offen offset:0 lds\n"
"s_add_u32 s99, s90, 0\n"
"s_add_u32 m0, s32, 0\n"
"s_nop 0\n"
"buffer_load_dwordx4 v200, s[48:51], s99 offen offset:0 lds\n"
"s_add_u32 s99, s91, 0\n"
"s_add_u32 m0, s33, 0\n"
"s_nop 0\n"
"buffer_load_dwordx4 v200, s[48:51], s99 offen offset:0 lds\n"
"s_add_u32 s99, s92, 0\n"
"s_add_u32 m0, s34, 0\n"
"s_nop 0\n"
"buffer_load_dwordx4 v200, s[48:51], s99 offen offset:0 lds\n"
"s_add_u32 s99, s93, 0\n"
"s_add_u32 m0, s35, 0\n"
"s_nop 0\n"
"buffer_load_dwordx4 v200, s[48:51], s99 offen offset:0 lds\n"
"s_add_u32 s99, s86, 0x80000\n"
"s_add_u32 m0, s28, 0xd000\n"
"s_nop 0\n"
"buffer_load_dwordx4 v200, s[48:51], s99 offen offset:0 lds\n"
"s_add_u32 s74, s94, 0x80\n"
"s_add_u32 m0, s36, 0xd000\n"
"s_nop 0\n"
"buffer_load_dwordx4 v199, s[44:47], s74 offen offset:0 lds\n"
"s_add_u32 s74, s95, 0x80\n"
"s_add_u32 m0, s37, 0xd000\n"
"s_nop 0\n"
"buffer_load_dwordx4 v199, s[44:47], s74 offen offset:0 lds\n"
"s_waitcnt lgkmcnt(0)\n"
"s_add_u32 s68, s72, 0x2100000\n"
"s_addc_u32 s69, s73, 0\n"
"s_and_b32 s69, s69, 0xffff\n"
"s_mov_b32 s70, 0x10000\n"
"s_mov_b32 s71, 0x20000\n"
"s_lshl_b32 s23, s41, 3\n"
"s_add_u32 s23, s23, s43\n"
"s_lshl_b32 s23, s23, 4\n"
"s_add_u32 s23, s23, s42\n"
"s_lshl_b32 s23, s23, 8\n"
"v_lshlrev_b32 v254, 2, v213\n"
"v_and_b32 v212, 32, v213\n"
"v_lshl_add_u32 v254, v212, 20, v254\n"
"v_add_u32 v254, s23, v254\n"
"buffer_load_dword v212, v254, s[68:71], 0 offen offset:0 sc1\n"
"v_lshrrev_b32 v242, 4, v213\n"
"v_bfe_u32 v243, v213, 2, 2\n"
"v_and_b32 v244, 3, v213\n"
"v_and_b32 v245, 15, v213\n"
"s_and_b32 s20, s19, 1\n"
"s_lshl_b32 s20, s20, 11\n"
"v_lshlrev_b32 v246, 12, v242\n"
"v_lshl_add_u32 v246, v243, 7, v246\n"
"v_add_u32 v246, s20, v246\n"
"v_and_b32 v247, 1, v244\n"
"v_lshl_add_u32 v246, v247, 3, v246\n"
"v_and_b32 v247, 2, v243\n"
"v_lshlrev_b32 v247, 1, v247\n"
"v_and_b32 v248, 1, v242\n"
"v_or_b32 v247, v247, v248\n"
"v_and_b32 v248, 2, v244\n"
"v_xor_b32 v247, v247, v248\n"
"v_xor_b32 v248, 0, v247\n"
"v_lshl_add_u32 v201, v248, 4, v246\n"
"v_xor_b32 v248, 1, v247\n"
"v_lshl_add_u32 v202, v248, 4, v246\n"
"v_xor_b32 v248, 4, v247\n"
"v_lshl_add_u32 v203, v248, 4, v246\n"
"v_xor_b32 v248, 5, v247\n"
"v_lshl_add_u32 v204, v248, 4, v246\n"
"s_lshr_b32 s20, s19, 1\n"
"s_mul_i32 s20, s20, 0x2800\n"
"s_add_u32 s20, s20, 0x8000\n"
"v_lshlrev_b32 v246, 7, v245\n"
"v_add_u32 v246, s20, v246\n"
"v_and_b32 v247, 6, v245\n"
"v_xor_b32 v247, v247, v242\n"
"v_xor_b32 v248, 0, v247\n"
"v_lshl_add_u32 v205, v248, 4, v246\n"
"v_xor_b32 v248, 4, v247\n"
"v_lshl_add_u32 v206, v248, 4, v246\n"
"s_add_u32 s99, s86, 0x100000\n"
"s_add_u32 m0, s28, 0x1a000\n"
"s_nop 0\n"
"buffer_load_dwordx4 v200, s[48:51], s99 offen offset:0 lds\n"
"s_add_u32 s74, s94, 0x100\n"
"s_add_u32 m0, s36, 0x1a000\n"
"s_nop 0\n"
"buffer_load_dwordx4 v199, s[44:47], s74 offen offset:0 lds\n"
"s_add_u32 s74, s95, 0x100\n"
"s_add_u32 m0, s37, 0x1a000\n"
"s_nop 0\n"
"buffer_load_dwordx4 v199, s[44:47], s74 offen offset:0 lds\n"
"s_waitcnt vmcnt(7)\n"
"s_barrier\n"
"ds_read_b128 a[0:3], v205 offset:0\n"
"ds_read_b128 a[4:7], v206 offset:0\n"
"ds_read_b128 a[8:11], v205 offset:2048\n"
"ds_read_b128 a[12:15], v206 offset:2048\n"
"ds_read_b128 a[16:19], v205 offset:4096\n"
"ds_read_b128 a[20:23], v206 offset:4096\n"
"ds_read_b128 a[24:27], v205 offset:6144\n"
"ds_read_b128 a[28:31], v206 offset:6144\n"
"ds_read_b128 a[32:35], v205 offset:8192\n"
"ds_read_b128 a[36:39], v206 offset:8192\n"
"ds_read_b64_tr_b16 a[40:41], v201 offset:0\n"
"ds_read_b64_tr_b16 a[42:43], v201 offset:512\n"
"ds_read_b64_tr_b16 a[44:45], v201 offset:16384\n"
"ds_read_b64_tr_b16 a[46:47], v201 offset:16896\n"
"ds_read_b64_tr_b16 a[48:49], v202 offset:0\n"
"ds_read_b64_tr_b16 a[50:51], v202 offset:512\n"
"ds_read_b64_tr_b16 a[52:53], v202 offset:16384\n"
"ds_read_b64_tr_b16 a[54:55], v202 offset:16896\n"
"s_waitcnt lgkmcnt(6)\n"
"v_mfma_f32_16x16x32_bf16 v[0:3], a[40:43], a[0:3], 0\n"
"s_waitcnt lgkmcnt(4)\n"
"s_add_u32 s74, s96, 0x80\n"
"ds_read_b64_tr_b16 a[56:57], v203 offset:0\n"
"ds_read_b64_tr_b16 a[58:59], v203 offset:512\n"
"v_mfma_f32_16x16x32_bf16 v[0:3], a[44:47], a[4:7], v[0:3]\n"
"s_add_u32 m0, s38, 0xd000\n"
"ds_read_b64_tr_b16 a[60:61], v203 offset:16384\n"
"ds_read_b64_tr_b16 a[62:63], v203 offset:16896\n"
"v_mfma_f32_16x16x32_bf16 v[32:35], a[40:43], a[8:11], 0\n"
"ds_read_b64_tr_b16 a[64:65], v204 offset:0\n"
"ds_read_b64_tr_b16 a[66:67], v204 offset:512\n"
"v_mfma_f32_16x16x32_bf16 v[32:35], a[44:47], a[12:15], v[32:35]\n"
"ds_read_b64_tr_b16 a[68:69], v204 offset:16384\n"
"ds_read_b64_tr_b16 a[70:71], v204 offset:16896\n"
"v_mfma_f32_16x16x32_bf16 v[64:67], a[40:43], a[16:19], 0\n"
"ds_read_b64_tr_b16 a[72:73], v201 offset:1024\n"
"ds_read_b64_tr_b16 a[74:75], v201 offset:1536\n"
"v_mfma_f32_16x16x32_bf16 v[64:67], a[44:47], a[20:23], v[64:67]\n"
"ds_read_b64_tr_b16 a[76:77], v201 offset:17408\n"
"ds_read_b64_tr_b16 a[78:79], v201 offset:17920\n"
"v_mfma_f32_16x16x32_bf16 v[96:99], a[40:43], a[24:27], 0\n"
"ds_read_b64_tr_b16 a[80:81], v202 offset:1024\n"
"ds_read_b64_tr_b16 a[82:83], v202 offset:1536\n"
"buffer_load_dwordx4 v199, s[44:47], s74 offen offset:0 lds\n"
"v_mfma_f32_16x16x32_bf16 v[96:99], a[44:47], a[28:31], v[96:99]\n"
"s_add_u32 s74, s97, 0x80\n"
"ds_read_b64_tr_b16 a[84:85], v202 offset:17408\n"
"ds_read_b64_tr_b16 a[86:87], v202 offset:17920\n"
"v_mfma_f32_16x16x32_bf16 v[128:131], a[40:43], a[32:35], 0\n"
"s_add_u32 m0, s39, 0xd000\n"
"ds_read_b64_tr_b16 a[88:89], v203 offset:1024\n"
"ds_read_b64_tr_b16 a[90:91], v203 offset:1536\n"
"v_mfma_f32_16x16x32_bf16 v[128:131], a[44:47], a[36:39], v[128:131]\n"
"s_waitcnt lgkmcnt(12)\n"
"ds_read_b64_tr_b16 a[92:93], v203 offset:17408\n"
"ds_read_b64_tr_b16 a[94:95], v203 offset:17920\n"
"buffer_load_dwordx4 v199, s[44:47], s74 offen offset:0 lds\n"
"v_mfma_f32_16x16x32_bf16 v[4:7], a[48:51], a[0:3], 0\n"
"s_add_u32 s74, s98, 0x80\n"
"ds_read_b64_tr_b16 a[96:97], v204 offset:1024\n"
"ds_read_b64_tr_b16 a[98:99], v204 offset:1536\n"
"v_mfma_f32_16x16x32_bf16 v[4:7], a[52:55], a[4:7], v[4:7]\n"
"s_add_u32 m0, s40, 0xd000\n"
"ds_read_b64_tr_b16 a[100:101], v204 offset:17408\n"
"ds_read_b64_tr_b16 a[102:103], v204 offset:17920\n"
"v_mfma_f32_16x16x32_bf16 v[36:39], a[48:51], a[8:11], 0\n"
"buffer_load_dwordx4 v199, s[44:47], s74 offen offset:0 lds\n"
"v_mfma_f32_16x16x32_bf16 v[36:39], a[52:55], a[12:15], v[36:39]\n"
"s_add_u32 s99, s87, 0x80000\n"
"v_add_u32 v205, 0xd000, v205\n"
"v_mfma_f32_16x16x32_bf16 v[68:71], a[48:51], a[16:19], 0\n"
"s_add_u32 m0, s29, 0xd000\n"
"v_add_u32 v206, 0xd000, v206\n"
"v_mfma_f32_16x16x32_bf16 v[68:71], a[52:55], a[20:23], v[68:71]\n"
"buffer_load_dwordx4 v200, s[48:51], s99 offen offset:0 lds\n"
"v_add_u32 v201, 0xd000, v201\n"
"v_mfma_f32_16x16x32_bf16 v[100:103], a[48:51], a[24:27], 0\n"
"s_add_u32 s99, s88, 0x80000\n"
"v_add_u32 v202, 0xd000, v202\n"
"v_mfma_f32_16x16x32_bf16 v[100:103], a[52:55], a[28:31], v[100:103]\n"
"s_add_u32 m0, s30, 0xd000\n"
"v_add_u32 v203, 0xd000, v203\n"
"v_mfma_f32_16x16x32_bf16 v[132:135], a[48:51], a[32:35], 0\n"
"buffer_load_dwordx4 v200, s[48:51], s99 offen offset:0 lds\n"
"v_add_u32 v204, 0xd000, v204\n"
"v_mfma_f32_16x16x32_bf16 v[132:135], a[52:55], a[36:39], v[132:135]\n"
"s_add_u32 s99, s89, 0x80000\n"
"v_mfma_f32_16x16x32_bf16 v[8:11], a[56:59], a[0:3], 0\n"
"s_add_u32 m0, s31, 0xd000\n"
"v_mfma_f32_16x16x32_bf16 v[8:11], a[60:63], a[4:7], v[8:11]\n"
"buffer_load_dwordx4 v200, s[48:51], s99 offen offset:0 lds\n"
"v_mfma_f32_16x16x32_bf16 v[40:43], a[56:59], a[8:11], 0\n"
"s_waitcnt lgkmcnt(0)\n"
"v_mfma_f32_16x16x32_bf16 v[40:43], a[60:63], a[12:15], v[40:43]\n"
"s_barrier\n"
"v_mfma_f32_16x16x32_bf16 v[72:75], a[56:59], a[16:19], 0\n"
"s_add_u32 s99, s90, 0x80000\n"
"v_mfma_f32_16x16x32_bf16 v[72:75], a[60:63], a[20:23], v[72:75]\n"
"s_add_u32 m0, s32, 0xd000\n"
"v_mfma_f32_16x16x32_bf16 v[104:107], a[56:59], a[24:27], 0\n"
"buffer_load_dwordx4 v200, s[48:51], s99 offen offset:0 lds\n"
"v_mfma_f32_16x16x32_bf16 v[104:107], a[60:63], a[28:31], v[104:107]\n"
"s_add_u32 s99, s91, 0x80000\n"
"v_mfma_f32_16x16x32_bf16 v[136:139], a[56:59], a[32:35], 0\n"
"s_add_u32 m0, s33, 0xd000\n"
"v_mfma_f32_16x16x32_bf16 v[136:139], a[60:63], a[36:39], v[136:139]\n"
"buffer_load_dwordx4 v200, s[48:51], s99 offen offset:0 lds\n"
"v_mfma_f32_16x16x32_bf16 v[12:15], a[64:67], a[0:3], 0\n"
"s_add_u32 s99, s92, 0x80000\n"
"v_mfma_f32_16x16x32_bf16 v[12:15], a[68:71], a[4:7], v[12:15]\n"
"s_add_u32 m0, s34, 0xd000\n"
"v_mfma_f32_16x16x32_bf16 v[44:47], a[64:67], a[8:11], 0\n"
"buffer_load_dwordx4 v200, s[48:51], s99 offen offset:0 lds\n"
"v_mfma_f32_16x16x32_bf16 v[44:47], a[68:71], a[12:15], v[44:47]\n"
"s_add_u32 s99, s93, 0x80000\n"
"v_mfma_f32_16x16x32_bf16 v[76:79], a[64:67], a[16:19], 0\n"
"s_add_u32 m0, s35, 0xd000\n"
"v_mfma_f32_16x16x32_bf16 v[76:79], a[68:71], a[20:23], v[76:79]\n"
"buffer_load_dwordx4 v200, s[48:51], s99 offen offset:0 lds\n"
"v_mfma_f32_16x16x32_bf16 v[108:111], a[64:67], a[24:27], 0\n"
"s_add_u32 s99, s86, 0x180000\n"
"v_mfma_f32_16x16x32_bf16 v[108:111], a[68:71], a[28:31], v[108:111]\n"
"s_add_u32 m0, s28, 0\n"
"v_mfma_f32_16x16x32_bf16 v[140:143], a[64:67], a[32:35], 0\n"
"buffer_load_dwordx4 v200, s[48:51], s99 offen offset:0 lds\n"
"v_mfma_f32_16x16x32_bf16 v[140:143], a[68:71], a[36:39], v[140:143]\n"
"s_add_u32 s74, s94, 0x180\n"
"v_mfma_f32_16x16x32_bf16 v[16:19], a[72:75], a[0:3], 0\n"
"s_add_u32 m0, s36, 0\n"
"v_mfma_f32_16x16x32_bf16 v[16:19], a[76:79], a[4:7], v[16:19]\n"
"buffer_load_dwordx4 v199, s[44:47], s74 offen offset:0 lds\n"
"v_mfma_f32_16x16x32_bf16 v[48:51], a[72:75], a[8:11], 0\n"
"v_mfma_f32_16x16x32_bf16 v[48:51], a[76:79], a[12:15], v[48:51]\n"
"s_add_u32 s74, s95, 0x180\n"
"v_mfma_f32_16x16x32_bf16 v[80:83], a[72:75], a[16:19], 0\n"
"s_add_u32 m0, s37, 0\n"
"v_mfma_f32_16x16x32_bf16 v[80:83], a[76:79], a[20:23], v[80:83]\n"
"buffer_load_dwordx4 v199, s[44:47], s74 offen offset:0 lds\n"
"v_mfma_f32_16x16x32_bf16 v[112:115], a[72:75], a[24:27], 0\n"
"s_add_u32 s74, s96, 0x100\n"
"v_mfma_f32_16x16x32_bf16 v[112:115], a[76:79], a[28:31], v[112:115]\n"
"s_add_u32 m0, s38, 0x1a000\n"
"v_mfma_f32_16x16x32_bf16 v[144:147], a[72:75], a[32:35], 0\n"
"s_waitcnt vmcnt(3)\n"
"buffer_load_dwordx4 v199, s[44:47], s74 offen offset:0 lds\n"
"v_mfma_f32_16x16x32_bf16 v[144:147], a[76:79], a[36:39], v[144:147]\n"
"s_barrier\n"
"v_mfma_f32_16x16x32_bf16 v[20:23], a[80:83], a[0:3], 0\n"
"s_add_u32 s74, s97, 0x100\n"
"ds_read_b128 a[104:107], v205 offset:0\n"
"ds_read_b128 a[108:111], v206 offset:0\n"
"v_mfma_f32_16x16x32_bf16 v[20:23], a[84:87], a[4:7], v[20:23]\n"
"s_add_u32 m0, s39, 0x1a000\n"
"ds_read_b128 a[112:115], v205 offset:2048\n"
"ds_read_b128 a[116:119], v206 offset:2048\n"
"v_mfma_f32_16x16x32_bf16 v[52:55], a[80:83], a[8:11], 0\n"
"ds_read_b128 a[120:123], v205 offset:4096\n"
"ds_read_b128 a[124:127], v206 offset:4096\n"
"v_mfma_f32_16x16x32_bf16 v[52:55], a[84:87], a[12:15], v[52:55]\n"
"ds_read_b128 a[128:131], v205 offset:6144\n"
"ds_read_b128 a[132:135], v206 offset:6144\n"
"v_mfma_f32_16x16x32_bf16 v[84:87], a[80:83], a[16:19], 0\n"
"ds_read_b128 a[136:139], v205 offset:8192\n"
"ds_read_b128 a[140:143], v206 offset:8192\n"
"v_mfma_f32_16x16x32_bf16 v[84:87], a[84:87], a[20:23], v[84:87]\n"
"ds_read_b64_tr_b16 a[144:145], v201 offset:0\n"
"ds_read_b64_tr_b16 a[146:147], v201 offset:512\n"
"v_mfma_f32_16x16x32_bf16 v[116:119], a[80:83], a[24:27], 0\n"
"buffer_load_dwordx4 v199, s[44:47], s74 offen offset:0 lds\n"
"ds_read_b64_tr_b16 a[148:149], v201 offset:16384\n"
"ds_read_b64_tr_b16 a[150:151], v201 offset:16896\n"
"v_mfma_f32_16x16x32_bf16 v[116:119], a[84:87], a[28:31], v[116:119]\n"
"s_add_u32 s74, s98, 0x100\n"
"ds_read_b64_tr_b16 a[152:153], v202 offset:0\n"
"ds_read_b64_tr_b16 a[154:155], v202 offset:512\n"
"v_mfma_f32_16x16x32_bf16 v[148:151], a[80:83], a[32:35], 0\n"
"s_add_u32 m0, s40, 0x1a000\n"
"ds_read_b64_tr_b16 a[156:157], v202 offset:16384\n"
"ds_read_b64_tr_b16 a[158:159], v202 offset:16896\n"
"v_mfma_f32_16x16x32_bf16 v[148:151], a[84:87], a[36:39], v[148:151]\n"
"ds_read_b64_tr_b16 a[160:161], v203 offset:0\n"
"ds_read_b64_tr_b16 a[162:163], v203 offset:512\n"
"v_mfma_f32_16x16x32_bf16 v[24:27], a[88:91], a[0:3], 0\n"
"buffer_load_dwordx4 v199, s[44:47], s74 offen offset:0 lds\n"
"ds_read_b64_tr_b16 a[164:165], v203 offset:16384\n"
"ds_read_b64_tr_b16 a[166:167], v203 offset:16896\n"
"v_mfma_f32_16x16x32_bf16 v[24:27], a[92:95], a[4:7], v[24:27]\n"
"s_add_u32 s99, s87, 0x100000\n"
"ds_read_b64_tr_b16 a[168:169], v204 offset:0\n"
"ds_read_b64_tr_b16 a[170:171], v204 offset:512\n"
"v_mfma_f32_16x16x32_bf16 v[56:59], a[88:91], a[8:11], 0\n"
"s_add_u32 m0, s29, 0x1a000\n"
"ds_read_b64_tr_b16 a[172:173], v204 offset:16384\n"
"ds_read_b64_tr_b16 a[174:175], v204 offset:16896\n"
"v_mfma_f32_16x16x32_bf16 v[56:59], a[92:95], a[12:15], v[56:59]\n"
"buffer_load_dwordx4 v200, s[48:51], s99 offen offset:0 lds\n"
"ds_read_b64_tr_b16 a[176:177], v201 offset:1024\n"
"ds_read_b64_tr_b16 a[178:179], v201 offset:1536\n"
"v_mfma_f32_16x16x32_bf16 v[88:91], a[88:91], a[16:19], 0\n"
"s_add_u32 s99, s88, 0x100000\n"
"ds_read_b64_tr_b16 a[180:181], v201 offset:17408\n"
"ds_read_b64_tr_b16 a[182:183], v201 offset:17920\n"
"v_mfma_f32_16x16x32_bf16 v[88:91], a[92:95], a[20:23], v[88:91]\n"
"s_add_u32 m0, s30, 0x1a000\n"
"ds_read_b64_tr_b16 a[184:185], v202 offset:1024\n"
"ds_read_b64_tr_b16 a[186:187], v202 offset:1536\n"
"v_mfma_f32_16x16x32_bf16 v[120:123], a[88:91], a[24:27], 0\n"
"ds_read_b64_tr_b16 a[188:189], v202 offset:17408\n"
"ds_read_b64_tr_b16 a[190:191], v202 offset:17920\n"
"v_mfma_f32_16x16x32_bf16 v[120:123], a[92:95], a[28:31], v[120:123]\n"
"buffer_load_dwordx4 v200, s[48:51], s99 offen offset:0 lds\n"
"ds_read_b64_tr_b16 a[192:193], v203 offset:1024\n"
"ds_read_b64_tr_b16 a[194:195], v203 offset:1536\n"
"v_mfma_f32_16x16x32_bf16 v[152:155], a[88:91], a[32:35], 0\n"
"s_add_u32 s99, s89, 0x100000\n"
"ds_read_b64_tr_b16 a[196:197], v203 offset:17408\n"
"ds_read_b64_tr_b16 a[198:199], v203 offset:17920\n"
"v_mfma_f32_16x16x32_bf16 v[152:155], a[92:95], a[36:39], v[152:155]\n"
"s_add_u32 m0, s31, 0x1a000\n"
"ds_read_b64_tr_b16 a[200:201], v204 offset:1024\n"
"ds_read_b64_tr_b16 a[202:203], v204 offset:1536\n"
"v_mfma_f32_16x16x32_bf16 v[28:31], a[96:99], a[0:3], 0\n"
"buffer_load_dwordx4 v200, s[48:51], s99 offen offset:0 lds\n"
"ds_read_b64_tr_b16 a[204:205], v204 offset:17408\n"
"ds_read_b64_tr_b16 a[206:207], v204 offset:17920\n"
"v_mfma_f32_16x16x32_bf16 v[28:31], a[100:103], a[4:7], v[28:31]\n"
"s_add_u32 s99, s90, 0x100000\n"
"v_add_u32 v205, 0xd000, v205\n"
"v_add_u32 v206, 0xd000, v206\n"
"v_mfma_f32_16x16x32_bf16 v[60:63], a[96:99], a[8:11], 0\n"
"s_add_u32 m0, s32, 0x1a000\n"
"v_add_u32 v201, 0xd000, v201\n"
"v_add_u32 v202, 0xd000, v202\n"
"v_mfma_f32_16x16x32_bf16 v[60:63], a[100:103], a[12:15], v[60:63]\n"
"buffer_load_dwordx4 v200, s[48:51], s99 offen offset:0 lds\n"
"v_add_u32 v203, 0xd000, v203\n"
"v_mfma_f32_16x16x32_bf16 v[92:95], a[96:99], a[16:19], 0\n"
"s_add_u32 s99, s91, 0x100000\n"
"v_add_u32 v204, 0xd000, v204\n"
"v_mfma_f32_16x16x32_bf16 v[92:95], a[100:103], a[20:23], v[92:95]\n"
"s_add_u32 m0, s33, 0x1a000\n"
"v_mfma_f32_16x16x32_bf16 v[124:127], a[96:99], a[24:27], 0\n"
"buffer_load_dwordx4 v200, s[48:51], s99 offen offset:0 lds\n"
"v_mfma_f32_16x16x32_bf16 v[124:127], a[100:103], a[28:31], v[124:127]\n"
"s_add_u32 s99, s92, 0x100000\n"
"v_mfma_f32_16x16x32_bf16 v[156:159], a[96:99], a[32:35], 0\n"
"s_waitcnt lgkmcnt(0)\n"
"v_mfma_f32_16x16x32_bf16 v[156:159], a[100:103], a[36:39], v[156:159]\n"
"s_barrier\n"
"v_mfma_f32_16x16x32_bf16 v[0:3], a[144:147], a[104:107], v[0:3]\n"
"s_add_u32 m0, s34, 0x1a000\n"
"v_mfma_f32_16x16x32_bf16 v[0:3], a[148:151], a[108:111], v[0:3]\n"
"buffer_load_dwordx4 v200, s[48:51], s99 offen offset:0 lds\n"
"v_mfma_f32_16x16x32_bf16 v[32:35], a[144:147], a[112:115], v[32:35]\n"
"s_add_u32 s99, s93, 0x100000\n"
"v_mfma_f32_16x16x32_bf16 v[32:35], a[148:151], a[116:119], v[32:35]\n"
"s_add_u32 m0, s35, 0x1a000\n"
"v_mfma_f32_16x16x32_bf16 v[64:67], a[144:147], a[120:123], v[64:67]\n"
"s_waitcnt vmcnt(12)\n"
"buffer_load_dwordx4 v200, s[48:51], s99 offen offset:0 lds\n"
"v_mfma_f32_16x16x32_bf16 v[64:67], a[148:151], a[124:127], v[64:67]\n"
"s_add_u32 s99, s86, 0x200000\n"
"v_and_b32 v249, 31, v213\n"
"v_mfma_f32_16x16x32_bf16 v[96:99], a[144:147], a[128:131], v[96:99]\n"
"s_add_u32 m0, s28, 0xd000\n"
"v_lshrrev_b32 v212, v249, v212\n"
"v_and_b32 v212, 1, v212\n"
"v_mfma_f32_16x16x32_bf16 v[96:99], a[148:151], a[132:135], v[96:99]\n"
"buffer_load_dwordx4 v200, s[48:51], s99 offen offset:0 lds\n"
"v_cmp_eq_u32 vcc, 0, v212\n"
"v_mfma_f32_16x16x32_bf16 v[128:131], a[144:147], a[136:139], v[128:131]\n"
"v_mfma_f32_16x16x32_bf16 v[128:131], a[148:151], a[140:143], v[128:131]\n"
"s_add_u32 s74, s94, 0x200\n"
"v_mfma_f32_16x16x32_bf16 v[4:7], a[152:155], a[104:107], v[4:7]\n"
"s_add_u32 m0, s36, 0xd000\n"
"v_mfma_f32_16x16x32_bf16 v[4:7], a[156:159], a[108:111], v[4:7]\n"
"s_or_b32 s20, s19, s85\ns_cbranch_scc1 L_tok_%=\nv_mov_b32 v212, vcc_lo\ns_mov_b64 exec, 1\nbuffer_store_dword v212, v254, s[68:71], 0 offen offset:128 sc1\ns_mov_b64 exec, -1\nL_tok_%=:\n"
"buffer_load_dwordx4 v199, s[44:47], s74 offen offset:0 lds\n"
"v_mfma_f32_16x16x32_bf16 v[36:39], a[152:155], a[112:115], v[36:39]\n"
"s_lshl_b32 s78, s19, 12\n"
"v_mfma_f32_16x16x32_bf16 v[36:39], a[156:159], a[116:119], v[36:39]\n"
"s_add_u32 s74, s95, 0x200\n"
"v_mfma_f32_16x16x32_bf16 v[68:71], a[152:155], a[120:123], v[68:71]\n"
"s_add_u32 m0, s37, 0xd000\n"
"v_mfma_f32_16x16x32_bf16 v[68:71], a[156:159], a[124:127], v[68:71]\n"
"s_lshl_b32 s81, s19, 13\n"
"buffer_load_dwordx4 v199, s[44:47], s74 offen offset:0 lds\n"
"v_mfma_f32_16x16x32_bf16 v[100:103], a[152:155], a[128:131], v[100:103]\n"
"s_mov_b32 s75, s78\n"
"v_mfma_f32_16x16x32_bf16 v[100:103], a[156:159], a[132:135], v[100:103]\n"
"s_add_u32 s74, s96, 0x180\n"
"v_mfma_f32_16x16x32_bf16 v[132:135], a[152:155], a[136:139], v[132:135]\n"
"s_add_u32 m0, s38, 0\n"
"v_mfma_f32_16x16x32_bf16 v[132:135], a[156:159], a[140:143], v[132:135]\n"
"s_mov_b32 s84, 0x3e0293ee\n"
"buffer_load_dwordx4 v199, s[44:47], s74 offen offset:0 lds\n"
"v_mfma_f32_16x16x32_bf16 v[8:11], a[160:163], a[104:107], v[8:11]\n"
"s_lshl_b32 s23, s43, 4\n"
"v_mfma_f32_16x16x32_bf16 v[8:11], a[164:167], a[108:111], v[8:11]\n"
"s_add_u32 s74, s97, 0x180\n"
"v_mfma_f32_16x16x32_bf16 v[40:43], a[160:163], a[112:115], v[40:43]\n"
"s_add_u32 m0, s39, 0\n"
"v_mfma_f32_16x16x32_bf16 v[40:43], a[164:167], a[116:119], v[40:43]\n"
"s_add_u32 s23, s23, s42\n"
"buffer_load_dwordx4 v199, s[44:47], s74 offen offset:0 lds\n"
"v_mfma_f32_16x16x32_bf16 v[72:75], a[160:163], a[120:123], v[72:75]\n"
"s_lshl_b32 s24, s23, 18\n"
"v_mfma_f32_16x16x32_bf16 v[72:75], a[164:167], a[124:127], v[72:75]\n"
"s_add_u32 s74, s98, 0x180\n"
"v_mfma_f32_16x16x32_bf16 v[104:107], a[160:163], a[128:131], v[104:107]\n"
"s_add_u32 m0, s40, 0\n"
"v_mfma_f32_16x16x32_bf16 v[104:107], a[164:167], a[132:135], v[104:107]\n"
"s_add_u32 s52, s52, s24\ns_addc_u32 s53, s53, 0\n"
"buffer_load_dwordx4 v199, s[44:47], s74 offen offset:0 lds\n"
"v_mfma_f32_16x16x32_bf16 v[136:139], a[160:163], a[136:139], v[136:139]\n"
"s_and_b32 s53, s53, 0xffff\n"
"v_mfma_f32_16x16x32_bf16 v[136:139], a[164:167], a[140:143], v[136:139]\n"
"s_add_u32 s99, s87, 0x180000\n"
"v_mfma_f32_16x16x32_bf16 v[12:15], a[168:171], a[104:107], v[12:15]\n"
"s_waitcnt vmcnt(6)\n"
"v_mfma_f32_16x16x32_bf16 v[12:15], a[172:175], a[108:111], v[12:15]\n"
"s_barrier\n"
"v_mfma_f32_16x16x32_bf16 v[44:47], a[168:171], a[112:115], v[44:47]\n"
"s_add_u32 m0, s29, 0\n"
"ds_read_b128 a[0:3], v205 offset:0\n"
"ds_read_b128 a[4:7], v206 offset:0\n"
"v_mfma_f32_16x16x32_bf16 v[44:47], a[172:175], a[116:119], v[44:47]\n"
"s_mov_b32 s54, 0x40000\n"
"ds_read_b128 a[8:11], v205 offset:2048\n"
"ds_read_b128 a[12:15], v206 offset:2048\n"
"v_mfma_f32_16x16x32_bf16 v[76:79], a[168:171], a[120:123], v[76:79]\n"
"s_mov_b32 s55, 0x20000\n"
"ds_read_b128 a[16:19], v205 offset:4096\n"
"ds_read_b128 a[20:23], v206 offset:4096\n"
"v_mfma_f32_16x16x32_bf16 v[76:79], a[172:175], a[124:127], v[76:79]\n"
"s_add_u32 s56, s56, s24\ns_addc_u32 s57, s57, 0\n"
"buffer_load_dwordx4 v200, s[48:51], s99 offen offset:0 lds\n"
"ds_read_b128 a[24:27], v205 offset:6144\n"
"ds_read_b128 a[28:31], v206 offset:6144\n"
"v_mfma_f32_16x16x32_bf16 v[108:111], a[168:171], a[128:131], v[108:111]\n"
"s_add_u32 s99, s88, 0x180000\n"
"ds_read_b128 a[32:35], v205 offset:8192\n"
"ds_read_b128 a[36:39], v206 offset:8192\n"
"v_mfma_f32_16x16x32_bf16 v[108:111], a[172:175], a[132:135], v[108:111]\n"
"s_add_u32 m0, s30, 0\n"
"ds_read_b64_tr_b16 a[40:41], v201 offset:0\n"
"ds_read_b64_tr_b16 a[42:43], v201 offset:512\n"
"v_mfma_f32_16x16x32_bf16 v[140:143], a[168:171], a[136:139], v[140:143]\n"
"s_and_b32 s57, s57, 0xffff\n"
"ds_read_b64_tr_b16 a[44:45], v201 offset:16384\n"
"ds_read_b64_tr_b16 a[46:47], v201 offset:16896\n"
"v_mfma_f32_16x16x32_bf16 v[140:143], a[172:175], a[140:143], v[140:143]\n"
"s_mov_b32 s58, 0x40000\n"
"buffer_load_dwordx4 v200, s[48:51], s99 offen offset:0 lds\n"
"ds_read_b64_tr_b16 a[48:49], v202 offset:0\n"
"ds_read_b64_tr_b16 a[50:51], v202 offset:512\n"
"v_mfma_f32_16x16x32_bf16 v[16:19], a[176:179], a[104:107], v[16:19]\n"
"s_add_u32 s99, s89, 0x180000\n"
"ds_read_b64_tr_b16 a[52:53], v202 offset:16384\n"
"ds_read_b64_tr_b16 a[54:55], v202 offset:16896\n"
"v_mfma_f32_16x16x32_bf16 v[16:19], a[180:183], a[108:111], v[16:19]\n"
"s_add_u32 m0, s31, 0\n"
"ds_read_b64_tr_b16 a[56:57], v203 offset:0\n"
"ds_read_b64_tr_b16 a[58:59], v203 offset:512\n"
"v_mfma_f32_16x16x32_bf16 v[48:51], a[176:179], a[112:115], v[48:51]\n"
"s_mov_b32 s59, 0x20000\n"
"ds_read_b64_tr_b16 a[60:61], v203 offset:16384\n"
"ds_read_b64_tr_b16 a[62:63], v203 offset:16896\n"
"v_mfma_f32_16x16x32_bf16 v[48:51], a[180:183], a[116:119], v[48:51]\n"
"s_lshl_b32 s24, s41, 7\n"
"buffer_load_dwordx4 v200, s[48:51], s99 offen offset:0 lds\n"
"ds_read_b64_tr_b16 a[64:65], v204 offset:0\n"
"ds_read_b64_tr_b16 a[66:67], v204 offset:512\n"
"v_mfma_f32_16x16x32_bf16 v[80:83], a[176:179], a[120:123], v[80:83]\n"
"s_add_u32 s99, s90, 0x180000\n"
"ds_read_b64_tr_b16 a[68:69], v204 offset:16384\n"
"ds_read_b64_tr_b16 a[70:71], v204 offset:16896\n"
"v_mfma_f32_16x16x32_bf16 v[80:83], a[180:183], a[124:127], v[80:83]\n"
"s_add_u32 m0, s32, 0\n"
"ds_read_b64_tr_b16 a[72:73], v201 offset:1024\n"
"ds_read_b64_tr_b16 a[74:75], v201 offset:1536\n"
"v_mfma_f32_16x16x32_bf16 v[112:115], a[176:179], a[128:131], v[112:115]\n"
"s_add_u32 s23, s23, s24\n"
"ds_read_b64_tr_b16 a[76:77], v201 offset:17408\n"
"ds_read_b64_tr_b16 a[78:79], v201 offset:17920\n"
"v_mfma_f32_16x16x32_bf16 v[112:115], a[180:183], a[132:135], v[112:115]\n"
"s_mul_i32 s24, s23, 0x17000\n"
"buffer_load_dwordx4 v200, s[48:51], s99 offen offset:0 lds\n"
"ds_read_b64_tr_b16 a[80:81], v202 offset:1024\n"
"ds_read_b64_tr_b16 a[82:83], v202 offset:1536\n"
"v_mfma_f32_16x16x32_bf16 v[144:147], a[176:179], a[136:139], v[144:147]\n"
"s_add_u32 s99, s91, 0x180000\n"
"ds_read_b64_tr_b16 a[84:85], v202 offset:17408\n"
"ds_read_b64_tr_b16 a[86:87], v202 offset:17920\n"
"v_mfma_f32_16x16x32_bf16 v[144:147], a[180:183], a[140:143], v[144:147]\n"
"s_add_u32 m0, s33, 0\n"
"ds_read_b64_tr_b16 a[88:89], v203 offset:1024\n"
"ds_read_b64_tr_b16 a[90:91], v203 offset:1536\n"
"v_mfma_f32_16x16x32_bf16 v[20:23], a[184:187], a[104:107], v[20:23]\n"
"s_add_u32 s24, s24, 0xa00000\n"
"ds_read_b64_tr_b16 a[92:93], v203 offset:17408\n"
"ds_read_b64_tr_b16 a[94:95], v203 offset:17920\n"
"v_mfma_f32_16x16x32_bf16 v[20:23], a[188:191], a[108:111], v[20:23]\n"
"s_add_u32 s64, s72, s24\ns_addc_u32 s65, s73, 0\n"
"buffer_load_dwordx4 v200, s[48:51], s99 offen offset:0 lds\n"
"ds_read_b64_tr_b16 a[96:97], v204 offset:1024\n"
"ds_read_b64_tr_b16 a[98:99], v204 offset:1536\n"
"v_mfma_f32_16x16x32_bf16 v[52:55], a[184:187], a[112:115], v[52:55]\n"
"s_add_u32 s99, s92, 0x180000\n"
"ds_read_b64_tr_b16 a[100:101], v204 offset:17408\n"
"ds_read_b64_tr_b16 a[102:103], v204 offset:17920\n"
"v_mfma_f32_16x16x32_bf16 v[52:55], a[188:191], a[116:119], v[52:55]\n"
"s_add_u32 m0, s34, 0\n"
"v_add_u32 v205, 0xfffe6000, v205\n"
"v_add_u32 v206, 0xfffe6000, v206\n"
"v_mfma_f32_16x16x32_bf16 v[84:87], a[184:187], a[120:123], v[84:87]\n"
"s_and_b32 s65, s65, 0xffff\n"
"buffer_load_dwordx4 v200, s[48:51], s99 offen offset:0 lds\n"
"v_add_u32 v201, 0xfffe6000, v201\n"
"v_mfma_f32_16x16x32_bf16 v[84:87], a[188:191], a[124:127], v[84:87]\n"
"s_add_u32 s99, s93, 0x180000\n"
"v_add_u32 v202, 0xfffe6000, v202\n"
"v_add_u32 v203, 0xfffe6000, v203\n"
"v_mfma_f32_16x16x32_bf16 v[116:119], a[184:187], a[128:131], v[116:119]\n"
"s_add_u32 m0, s35, 0\n"
"v_add_u32 v204, 0xfffe6000, v204\n"
"v_mfma_f32_16x16x32_bf16 v[116:119], a[188:191], a[132:135], v[116:119]\n"
"s_mov_b32 s66, 0x17000\n"
"buffer_load_dwordx4 v200, s[48:51], s99 offen offset:0 lds\n"
"v_mfma_f32_16x16x32_bf16 v[148:151], a[184:187], a[136:139], v[148:151]\n"
"s_mov_b32 s67, 0x20000\n"
"v_mfma_f32_16x16x32_bf16 v[148:151], a[188:191], a[140:143], v[148:151]\n"
"s_and_b32 s24, s42, 14\n"
"v_mfma_f32_16x16x32_bf16 v[24:27], a[192:195], a[104:107], v[24:27]\n"
"s_waitcnt lgkmcnt(0)\n"
"v_mfma_f32_16x16x32_bf16 v[24:27], a[196:199], a[108:111], v[24:27]\n"
"s_barrier\n"
"v_mfma_f32_16x16x32_bf16 v[56:59], a[192:195], a[112:115], v[56:59]\n"
"s_add_u32 s99, s86, 0x280000\n"
"v_mfma_f32_16x16x32_bf16 v[56:59], a[196:199], a[116:119], v[56:59]\n"
"s_add_u32 m0, s28, 0x1a000\n"
"v_mfma_f32_16x16x32_bf16 v[88:91], a[192:195], a[120:123], v[88:91]\n"
"s_mul_i32 s24, s24, 0x48000\n"
"buffer_load_dwordx4 v200, s[48:51], s99 offen offset:0 lds\n"
"v_mfma_f32_16x16x32_bf16 v[88:91], a[196:199], a[124:127], v[88:91]\n"
"s_add_u32 s60, s60, s24\ns_addc_u32 s61, s61, 0\n"
"v_mfma_f32_16x16x32_bf16 v[120:123], a[192:195], a[128:131], v[120:123]\n"
"s_and_b32 s61, s61, 0xffff\n"
"v_mfma_f32_16x16x32_bf16 v[120:123], a[196:199], a[132:135], v[120:123]\n"
"s_add_u32 s74, s94, 0x280\n"
"v_mfma_f32_16x16x32_bf16 v[152:155], a[192:195], a[136:139], v[152:155]\n"
"s_add_u32 m0, s36, 0x1a000\n"
"v_mfma_f32_16x16x32_bf16 v[152:155], a[196:199], a[140:143], v[152:155]\n"
"s_mov_b32 s62, 0x90000\n"
"buffer_load_dwordx4 v199, s[44:47], s74 offen offset:0 lds\n"
"v_mfma_f32_16x16x32_bf16 v[28:31], a[200:203], a[104:107], v[28:31]\n"
"s_mov_b32 s63, 0x20000\n"
"v_mfma_f32_16x16x32_bf16 v[28:31], a[204:207], a[108:111], v[28:31]\n"
"s_and_b32 s20, s23, 0xfff1\n"
"v_mfma_f32_16x16x32_bf16 v[60:63], a[200:203], a[112:115], v[60:63]\n"
"s_add_u32 s74, s95, 0x280\n"
"v_mfma_f32_16x16x32_bf16 v[60:63], a[204:207], a[116:119], v[60:63]\n"
"s_add_u32 m0, s37, 0x1a000\n"
"v_mfma_f32_16x16x32_bf16 v[92:95], a[200:203], a[120:123], v[92:95]\n"
"s_sub_u32 s20, s20, 2\n"
"buffer_load_dwordx4 v199, s[44:47], s74 offen offset:0 lds\n"
"v_mfma_f32_16x16x32_bf16 v[92:95], a[204:207], a[124:127], v[92:95]\n"
"s_xor_b32 s21, s23, 1\n"
"v_mfma_f32_16x16x32_bf16 v[124:127], a[200:203], a[128:131], v[124:127]\n"
"v_lshlrev_b32 v252, 1, v213\n"
"v_add_u32 v252, s20, v252\n"
"v_mfma_f32_16x16x32_bf16 v[124:127], a[204:207], a[132:135], v[124:127]\n"
"v_mov_b32 v249, s21\n"
"v_cmp_eq_u32 vcc, 0, v213\n"
"v_mfma_f32_16x16x32_bf16 v[156:159], a[200:203], a[136:139], v[156:159]\n"
"s_add_u32 s74, s96, 0x200\n"
"v_mfma_f32_16x16x32_bf16 v[156:159], a[204:207], a[140:143], v[156:159]\n"
"s_add_u32 m0, s38, 0xd000\n"
"v_cndmask_b32 v252, v252, v249, vcc\n"
"v_lshlrev_b32 v252, 8, v252\n"
"v_mfma_f32_16x16x32_bf16 v[0:3], a[40:43], a[0:3], v[0:3]\n"
"buffer_load_dwordx4 v199, s[44:47], s74 offen offset:0 lds\n"
"v_mov_b32 v249, 0x7ff00000\n"
"v_mfma_f32_16x16x32_bf16 v[0:3], a[44:47], a[4:7], v[0:3]\n"
"v_cmp_lt_u32 vcc, 8, v213\n"
"v_mfma_f32_16x16x32_bf16 v[32:35], a[40:43], a[8:11], v[32:35]\n"
"s_waitcnt vmcnt(4)\n"
"v_mfma_f32_16x16x32_bf16 v[32:35], a[44:47], a[12:15], v[32:35]\n"
"s_barrier\n"
"v_cndmask_b32 v252, v252, v249, vcc\n"
"v_mfma_f32_16x16x32_bf16 v[64:67], a[40:43], a[16:19], v[64:67]\n"
"s_add_u32 s74, s97, 0x200\n"
"ds_read_b128 a[104:107], v205 offset:0\n"
"v_mfma_f32_16x16x32_bf16 v[64:67], a[44:47], a[20:23], v[64:67]\n"
"s_add_u32 m0, s39, 0xd000\n"
"ds_read_b128 a[108:111], v206 offset:0\n"
"v_mfma_f32_16x16x32_bf16 v[96:99], a[40:43], a[24:27], v[96:99]\n"
"s_and_b32 s20, s42, 1\n"
"buffer_load_dwordx4 v199, s[44:47], s74 offen offset:0 lds\n"
"v_mfma_f32_16x16x32_bf16 v[96:99], a[44:47], a[28:31], v[96:99]\n"
"s_mul_i32 s21, s20, 0x48000\n"
"ds_read_b128 a[112:115], v205 offset:2048\n"
"v_mfma_f32_16x16x32_bf16 v[128:131], a[40:43], a[32:35], v[128:131]\n"
"s_mul_i32 s24, s19, 0x12000\n"
"ds_read_b128 a[116:119], v206 offset:2048\n"
"v_mfma_f32_16x16x32_bf16 v[128:131], a[44:47], a[36:39], v[128:131]\n"
"s_add_u32 s74, s98, 0x200\n"
"ds_read_b128 a[120:123], v205 offset:4096\n"
"v_mfma_f32_16x16x32_bf16 v[4:7], a[48:51], a[0:3], v[4:7]\n"
"s_add_u32 m0, s40, 0xd000\n"
"ds_read_b128 a[124:127], v206 offset:4096\n"
"v_mfma_f32_16x16x32_bf16 v[4:7], a[52:55], a[4:7], v[4:7]\n"
"s_add_u32 s26, s21, s24\n"
"buffer_load_dwordx4 v199, s[44:47], s74 offen offset:0 lds\n"
"v_mfma_f32_16x16x32_bf16 v[36:39], a[48:51], a[8:11], v[36:39]\n"
"s_sub_u32 s20, 0x48000, s21\n"
"ds_read_b128 a[128:131], v205 offset:6144\n"
"v_mfma_f32_16x16x32_bf16 v[36:39], a[52:55], a[12:15], v[36:39]\n"
"s_add_u32 s27, s20, s24\n"
"ds_read_b128 a[132:135], v206 offset:6144\n"
"v_mfma_f32_16x16x32_bf16 v[68:71], a[48:51], a[16:19], v[68:71]\n"
"s_add_u32 s99, s87, 0x200000\n"
"ds_read_b128 a[136:139], v205 offset:8192\n"
"v_mfma_f32_16x16x32_bf16 v[68:71], a[52:55], a[20:23], v[68:71]\n"
"s_add_u32 m0, s29, 0xd000\n"
"ds_read_b128 a[140:143], v206 offset:8192\n"
"v_mfma_f32_16x16x32_bf16 v[100:103], a[48:51], a[24:27], v[100:103]\n"
"s_lshl_b32 s20, s43, 2\n"
"buffer_load_dwordx4 v200, s[48:51], s99 offen offset:0 lds\n"
"v_mfma_f32_16x16x32_bf16 v[100:103], a[52:55], a[28:31], v[100:103]\n"
"s_add_u32 s20, s20, s19\n"
"ds_read_b64_tr_b16 a[144:145], v201 offset:0\n"
"v_mfma_f32_16x16x32_bf16 v[132:135], a[48:51], a[32:35], v[132:135]\n"
"s_mul_i32 s20, s20, 0x2400\n"
"ds_read_b64_tr_b16 a[146:147], v201 offset:512\n"
"v_mfma_f32_16x16x32_bf16 v[132:135], a[52:55], a[36:39], v[132:135]\n"
"s_add_u32 s20, s20, s21\n"
"ds_read_b64_tr_b16 a[148:149], v201 offset:16384\n"
"v_mfma_f32_16x16x32_bf16 v[8:11], a[56:59], a[0:3], v[8:11]\n"
"s_add_u32 s99, s88, 0x200000\n"
"ds_read_b64_tr_b16 a[150:151], v201 offset:16896\n"
"v_mfma_f32_16x16x32_bf16 v[8:11], a[60:63], a[4:7], v[8:11]\n"
"s_add_u32 m0, s30, 0xd000\n"
"ds_read_b64_tr_b16 a[152:153], v202 offset:0\n"
"v_mfma_f32_16x16x32_bf16 v[40:43], a[56:59], a[8:11], v[40:43]\n"
"s_add_u32 s20, s20, 0x100\n"
"buffer_load_dwordx4 v200, s[48:51], s99 offen offset:0 lds\n"
"v_mfma_f32_16x16x32_bf16 v[40:43], a[60:63], a[12:15], v[40:43]\n"
"ds_read_b64_tr_b16 a[154:155], v202 offset:512\n"
"v_min_u32 v185, 55, v213\n"
"v_mfma_f32_16x16x32_bf16 v[72:75], a[56:59], a[16:19], v[72:75]\n"
"ds_read_b64_tr_b16 a[156:157], v202 offset:16384\n"
"v_mul_u32_u24 v249, 37, v185\n"
"v_mfma_f32_16x16x32_bf16 v[72:75], a[60:63], a[20:23], v[72:75]\n"
"s_add_u32 s99, s89, 0x200000\n"
"ds_read_b64_tr_b16 a[158:159], v202 offset:16896\n"
"v_lshrrev_b32 v249, 8, v249\n"
"v_mfma_f32_16x16x32_bf16 v[104:107], a[56:59], a[24:27], v[104:107]\n"
"s_add_u32 m0, s31, 0xd000\n"
"ds_read_b64_tr_b16 a[160:161], v203 offset:0\n"
"v_lshlrev_b32 v185, 7, v185\n"
"v_mfma_f32_16x16x32_bf16 v[104:107], a[60:63], a[28:31], v[104:107]\n"
"buffer_load_dwordx4 v200, s[48:51], s99 offen offset:0 lds\n"
"v_mul_u32_u24 v249, 0x100, v249\n"
"v_mfma_f32_16x16x32_bf16 v[136:139], a[56:59], a[32:35], v[136:139]\n"
"ds_read_b64_tr_b16 a[162:163], v203 offset:512\n"
"v_add3_u32 v185, v185, v249, s20\n"
"v_mfma_f32_16x16x32_bf16 v[136:139], a[60:63], a[36:39], v[136:139]\n"
"ds_read_b64_tr_b16 a[164:165], v203 offset:16384\n"
"v_and_b32 v247, 6, v245\n"
"v_mfma_f32_16x16x32_bf16 v[12:15], a[64:67], a[0:3], v[12:15]\n"
"ds_read_b64_tr_b16 a[166:167], v203 offset:16896\n"
"v_xor_b32 v247, v247, v242\n"
"v_mfma_f32_16x16x32_bf16 v[12:15], a[68:71], a[4:7], v[12:15]\n"
"s_add_u32 s99, s90, 0x200000\n"
"ds_read_b64_tr_b16 a[168:169], v204 offset:0\n"
"v_lshrrev_b32 v246, 3, v245\n"
"v_mfma_f32_16x16x32_bf16 v[44:47], a[64:67], a[8:11], v[44:47]\n"
"s_add_u32 m0, s32, 0xd000\n"
"ds_read_b64_tr_b16 a[170:171], v204 offset:512\n"
"v_lshlrev_b32 v246, 11, v246\n"
"v_mfma_f32_16x16x32_bf16 v[44:47], a[68:71], a[12:15], v[44:47]\n"
"buffer_load_dwordx4 v200, s[48:51], s99 offen offset:0 lds\n"
"v_and_b32 v248, 7, v245\n"
"v_mfma_f32_16x16x32_bf16 v[76:79], a[64:67], a[16:19], v[76:79]\n"
"ds_read_b64_tr_b16 a[172:173], v204 offset:16384\n"
"v_lshl_add_u32 v246, v248, 7, v246\n"
"v_mfma_f32_16x16x32_bf16 v[76:79], a[68:71], a[20:23], v[76:79]\n"
"ds_read_b64_tr_b16 a[174:175], v204 offset:16896\n"
"v_xor_b32 v248, 0, v247\n"
"v_mfma_f32_16x16x32_bf16 v[108:111], a[64:67], a[24:27], v[108:111]\n"
"s_add_u32 s99, s91, 0x200000\n"
"ds_read_b64_tr_b16 a[176:177], v201 offset:1024\n"
"v_lshl_add_u32 v186, v248, 4, v246\n"
"v_mfma_f32_16x16x32_bf16 v[108:111], a[68:71], a[28:31], v[108:111]\n"
"s_add_u32 m0, s33, 0xd000\n"
"ds_read_b64_tr_b16 a[178:179], v201 offset:1536\n"
"v_add_u32 v188, 0x10000, v186\n"
"v_mfma_f32_16x16x32_bf16 v[140:143], a[64:67], a[32:35], v[140:143]\n"
"buffer_load_dwordx4 v200, s[48:51], s99 offen offset:0 lds\n"
"v_xor_b32 v248, 4, v247\n"
"v_mfma_f32_16x16x32_bf16 v[140:143], a[68:71], a[36:39], v[140:143]\n"
"ds_read_b64_tr_b16 a[180:181], v201 offset:17408\n"
"v_lshl_add_u32 v187, v248, 4, v246\n"
"v_mfma_f32_16x16x32_bf16 v[16:19], a[72:75], a[0:3], v[16:19]\n"
"ds_read_b64_tr_b16 a[182:183], v201 offset:17920\n"
"v_add_u32 v189, 0x10000, v187\n"
"v_mfma_f32_16x16x32_bf16 v[16:19], a[76:79], a[4:7], v[16:19]\n"
"ds_read_b64_tr_b16 a[184:185], v202 offset:1024\n"
"v_lshrrev_b32 v246, 1, v242\n"
"v_mfma_f32_16x16x32_bf16 v[48:51], a[72:75], a[8:11], v[48:51]\n"
"s_add_u32 s99, s92, 0x200000\n"
"ds_read_b64_tr_b16 a[186:187], v202 offset:1536\n"
"v_lshlrev_b32 v246, 11, v246\n"
"v_mfma_f32_16x16x32_bf16 v[48:51], a[76:79], a[12:15], v[48:51]\n"
"s_add_u32 m0, s34, 0xd000\n"
"ds_read_b64_tr_b16 a[188:189], v202 offset:17408\n"
"v_and_b32 v247, 1, v242\n"
"v_mfma_f32_16x16x32_bf16 v[80:83], a[72:75], a[16:19], v[80:83]\n"
"buffer_load_dwordx4 v200, s[48:51], s99 offen offset:0 lds\n"
"v_lshl_add_u32 v248, v247, 2, v243\n"
"v_mfma_f32_16x16x32_bf16 v[80:83], a[76:79], a[20:23], v[80:83]\n"
"ds_read_b64_tr_b16 a[190:191], v202 offset:17920\n"
"v_lshl_add_u32 v246, v248, 7, v246\n"
"v_mfma_f32_16x16x32_bf16 v[112:115], a[72:75], a[24:27], v[112:115]\n"
"ds_read_b64_tr_b16 a[192:193], v203 offset:1024\n"
"v_and_b32 v247, 6, v248\n"
"v_mfma_f32_16x16x32_bf16 v[112:115], a[76:79], a[28:31], v[112:115]\n"
"s_add_u32 s99, s93, 0x200000\n"
"ds_read_b64_tr_b16 a[194:195], v203 offset:1536\n"
"v_and_b32 v248, 1, v244\n"
"v_mfma_f32_16x16x32_bf16 v[144:147], a[72:75], a[32:35], v[144:147]\n"
"s_add_u32 m0, s35, 0xd000\n"
"ds_read_b64_tr_b16 a[196:197], v203 offset:17408\n"
"v_lshl_add_u32 v246, v248, 3, v246\n"
"v_mfma_f32_16x16x32_bf16 v[144:147], a[76:79], a[36:39], v[144:147]\n"
"ds_read_b64_tr_b16 a[198:199], v203 offset:17920\n"
"v_add_u32 v246, 0x4000, v246\n"
"v_mfma_f32_16x16x32_bf16 v[20:23], a[80:83], a[0:3], v[20:23]\n"
"ds_read_b64_tr_b16 a[200:201], v204 offset:1024\n"
"v_lshrrev_b32 v248, 1, v244\n"
"v_mfma_f32_16x16x32_bf16 v[20:23], a[84:87], a[4:7], v[20:23]\n"
"ds_read_b64_tr_b16 a[202:203], v204 offset:1536\n"
"v_xor_b32 v247, v247, v248\n"
"v_mfma_f32_16x16x32_bf16 v[52:55], a[80:83], a[8:11], v[52:55]\n"
"ds_read_b64_tr_b16 a[204:205], v204 offset:17408\n"
"ds_read_b64_tr_b16 a[206:207], v204 offset:17920\n"
"v_mfma_f32_16x16x32_bf16 v[52:55], a[84:87], a[12:15], v[52:55]\n"
"buffer_load_dwordx4 v200, s[48:51], s99 offen offset:0 lds\n"
"v_add_u32 v205, 0xd000, v205\n"
"v_mfma_f32_16x16x32_bf16 v[84:87], a[80:83], a[16:19], v[84:87]\n"
"v_add_u32 v206, 0xd000, v206\n"
"v_add_u32 v201, 0xd000, v201\n"
"v_mfma_f32_16x16x32_bf16 v[84:87], a[84:87], a[20:23], v[84:87]\n"
"s_waitcnt lgkmcnt(0)\n"
"v_add_u32 v202, 0xd000, v202\n"
"v_add_u32 v203, 0xd000, v203\n"
"v_mfma_f32_16x16x32_bf16 v[116:119], a[80:83], a[24:27], v[116:119]\n"
"s_barrier\n"
"v_add_u32 v204, 0xd000, v204\n"
"v_xor_b32 v248, 0, v247\n"
"v_mfma_f32_16x16x32_bf16 v[116:119], a[84:87], a[28:31], v[116:119]\n"
"s_add_u32 s99, s86, 0x300000\n"
"v_lshl_add_u32 v190, v248, 4, v246\n"
"v_add_u32 v194, 0x10000, v190\n"
"v_mfma_f32_16x16x32_bf16 v[148:151], a[80:83], a[32:35], v[148:151]\n"
"s_add_u32 m0, s28, 0\n"
"v_xor_b32 v248, 2, v247\n"
"v_lshl_add_u32 v191, v248, 4, v246\n"
"v_mfma_f32_16x16x32_bf16 v[148:151], a[84:87], a[36:39], v[148:151]\n"
"buffer_load_dwordx4 v200, s[48:51], s99 offen offset:0 lds\n"
"v_add_u32 v195, 0x10000, v191\n"
"v_mfma_f32_16x16x32_bf16 v[24:27], a[88:91], a[0:3], v[24:27]\n"
"v_xor_b32 v248, 4, v247\n"
"v_mfma_f32_16x16x32_bf16 v[24:27], a[92:95], a[4:7], v[24:27]\n"
"v_lshl_add_u32 v192, v248, 4, v246\n"
"v_add_u32 v196, 0x10000, v192\n"
"v_mfma_f32_16x16x32_bf16 v[56:59], a[88:91], a[8:11], v[56:59]\n"
"s_add_u32 s74, s94, 0x300\n"
"v_xor_b32 v248, 6, v247\n"
"v_lshl_add_u32 v193, v248, 4, v246\n"
"v_mfma_f32_16x16x32_bf16 v[56:59], a[92:95], a[12:15], v[56:59]\n"
"s_add_u32 m0, s36, 0\n"
"v_add_u32 v197, 0x10000, v193\n"
"v_mov_b32 v216, 0\n"
"v_mfma_f32_16x16x32_bf16 v[88:91], a[88:91], a[16:19], v[88:91]\n"
"buffer_load_dwordx4 v199, s[44:47], s74 offen offset:0 lds\n"
"v_mov_b32 v217, 0\n"
"v_mfma_f32_16x16x32_bf16 v[88:91], a[92:95], a[20:23], v[88:91]\n"
"v_mov_b32 v218, 0\n"
"v_mfma_f32_16x16x32_bf16 v[120:123], a[88:91], a[24:27], v[120:123]\n"
"v_mov_b32 v219, 0\n"
"v_mov_b32 v180, 0\n"
"v_mfma_f32_16x16x32_bf16 v[120:123], a[92:95], a[28:31], v[120:123]\n"
"v_mov_b32 v181, 0\n"
"v_mov_b32 v182, 0\n"
"v_mfma_f32_16x16x32_bf16 v[152:155], a[88:91], a[32:35], v[152:155]\n"
"s_add_u32 s74, s95, 0x300\n"
"v_mov_b32 v183, 0\n"
"v_mov_b32 v184, 0\n"
"v_mfma_f32_16x16x32_bf16 v[152:155], a[92:95], a[36:39], v[152:155]\n"
"s_add_u32 m0, s37, 0\n"
"v_mfma_f32_16x16x32_bf16 v[28:31], a[96:99], a[0:3], v[28:31]\n"
"buffer_load_dwordx4 v199, s[44:47], s74 offen offset:0 lds\n"
"v_mfma_f32_16x16x32_bf16 v[28:31], a[100:103], a[4:7], v[28:31]\n"
"v_mfma_f32_16x16x32_bf16 v[60:63], a[96:99], a[8:11], v[60:63]\n"
"v_mfma_f32_16x16x32_bf16 v[60:63], a[100:103], a[12:15], v[60:63]\n"
"v_mfma_f32_16x16x32_bf16 v[92:95], a[96:99], a[16:19], v[92:95]\n"
"s_add_u32 s74, s96, 0x280\n"
"v_mfma_f32_16x16x32_bf16 v[92:95], a[100:103], a[20:23], v[92:95]\n"
"s_add_u32 m0, s38, 0x1a000\n"
"v_mfma_f32_16x16x32_bf16 v[124:127], a[96:99], a[24:27], v[124:127]\n"
"buffer_load_dwordx4 v199, s[44:47], s74 offen offset:0 lds\n"
"v_mfma_f32_16x16x32_bf16 v[124:127], a[100:103], a[28:31], v[124:127]\n"
"v_mfma_f32_16x16x32_bf16 v[156:159], a[96:99], a[32:35], v[156:159]\n"
"v_mfma_f32_16x16x32_bf16 v[156:159], a[100:103], a[36:39], v[156:159]\n"
"v_mfma_f32_16x16x32_bf16 v[0:3], a[144:147], a[104:107], v[0:3]\n"
"s_add_u32 s74, s97, 0x280\n"
"v_mfma_f32_16x16x32_bf16 v[0:3], a[148:151], a[108:111], v[0:3]\n"
"s_add_u32 m0, s39, 0x1a000\n"
"v_mfma_f32_16x16x32_bf16 v[32:35], a[144:147], a[112:115], v[32:35]\n"
"s_waitcnt vmcnt(4)\n"
"buffer_load_dwordx4 v199, s[44:47], s74 offen offset:0 lds\n"
"v_mfma_f32_16x16x32_bf16 v[32:35], a[148:151], a[116:119], v[32:35]\n"
"s_barrier\n"
"v_mfma_f32_16x16x32_bf16 v[64:67], a[144:147], a[120:123], v[64:67]\n"
"ds_read_b128 a[0:3], v205 offset:0\n"
"v_mfma_f32_16x16x32_bf16 v[64:67], a[148:151], a[124:127], v[64:67]\n"
"ds_read_b128 a[4:7], v206 offset:0\n"
"v_mfma_f32_16x16x32_bf16 v[96:99], a[144:147], a[128:131], v[96:99]\n"
"s_add_u32 s74, s98, 0x280\n"
"ds_read_b128 a[8:11], v205 offset:2048\n"
"v_mfma_f32_16x16x32_bf16 v[96:99], a[148:151], a[132:135], v[96:99]\n"
"s_add_u32 m0, s40, 0x1a000\n"
"ds_read_b128 a[12:15], v206 offset:2048\n"
"v_mfma_f32_16x16x32_bf16 v[128:131], a[144:147], a[136:139], v[128:131]\n"
"buffer_load_dwordx4 v199, s[44:47], s74 offen offset:0 lds\n"
"v_mfma_f32_16x16x32_bf16 v[128:131], a[148:151], a[140:143], v[128:131]\n"
"ds_read_b128 a[16:19], v205 offset:4096\n"
"v_mfma_f32_16x16x32_bf16 v[4:7], a[152:155], a[104:107], v[4:7]\n"
"ds_read_b128 a[20:23], v206 offset:4096\n"
"v_mfma_f32_16x16x32_bf16 v[4:7], a[156:159], a[108:111], v[4:7]\n"
"ds_read_b128 a[24:27], v205 offset:6144\n"
"v_mfma_f32_16x16x32_bf16 v[36:39], a[152:155], a[112:115], v[36:39]\n"
"s_add_u32 s99, s87, 0x280000\n"
"ds_read_b128 a[28:31], v206 offset:6144\n"
"v_mfma_f32_16x16x32_bf16 v[36:39], a[156:159], a[116:119], v[36:39]\n"
"s_add_u32 m0, s29, 0x1a000\n"
"ds_read_b128 a[32:35], v205 offset:8192\n"
"v_mfma_f32_16x16x32_bf16 v[68:71], a[152:155], a[120:123], v[68:71]\n"
"buffer_load_dwordx4 v200, s[48:51], s99 offen offset:0 lds\n"
"v_mfma_f32_16x16x32_bf16 v[68:71], a[156:159], a[124:127], v[68:71]\n"
"ds_read_b128 a[36:39], v206 offset:8192\n"
"v_mfma_f32_16x16x32_bf16 v[100:103], a[152:155], a[128:131], v[100:103]\n"
"ds_read_b64_tr_b16 a[40:41], v201 offset:0\n"
"v_mfma_f32_16x16x32_bf16 v[100:103], a[156:159], a[132:135], v[100:103]\n"
"s_add_u32 s99, s88, 0x280000\n"
"ds_read_b64_tr_b16 a[42:43], v201 offset:512\n"
"v_mfma_f32_16x16x32_bf16 v[132:135], a[152:155], a[136:139], v[132:135]\n"
"s_add_u32 m0, s30, 0x1a000\n"
"ds_read_b64_tr_b16 a[44:45], v201 offset:16384\n"
"v_mfma_f32_16x16x32_bf16 v[132:135], a[156:159], a[140:143], v[132:135]\n"
"buffer_load_dwordx4 v200, s[48:51], s99 offen offset:0 lds\n"
"v_mfma_f32_16x16x32_bf16 v[8:11], a[160:163], a[104:107], v[8:11]\n"
"ds_read_b64_tr_b16 a[46:47], v201 offset:16896\n"
"v_mfma_f32_16x16x32_bf16 v[8:11], a[164:167], a[108:111], v[8:11]\n"
"ds_read_b64_tr_b16 a[48:49], v202 offset:0\n"
"v_mfma_f32_16x16x32_bf16 v[40:43], a[160:163], a[112:115], v[40:43]\n"
"ds_read_b64_tr_b16 a[50:51], v202 offset:512\n"
"v_mfma_f32_16x16x32_bf16 v[40:43], a[164:167], a[116:119], v[40:43]\n"
"s_add_u32 s99, s89, 0x280000\n"
"ds_read_b64_tr_b16 a[52:53], v202 offset:16384\n"
"v_mfma_f32_16x16x32_bf16 v[72:75], a[160:163], a[120:123], v[72:75]\n"
"s_add_u32 m0, s31, 0x1a000\n"
"ds_read_b64_tr_b16 a[54:55], v202 offset:16896\n"
"v_mfma_f32_16x16x32_bf16 v[72:75], a[164:167], a[124:127], v[72:75]\n"
"buffer_load_dwordx4 v200, s[48:51], s99 offen offset:0 lds\n"
"v_mfma_f32_16x16x32_bf16 v[104:107], a[160:163], a[128:131], v[104:107]\n"
"ds_read_b64_tr_b16 a[56:57], v203 offset:0\n"
"v_mfma_f32_16x16x32_bf16 v[104:107], a[164:167], a[132:135], v[104:107]\n"
"ds_read_b64_tr_b16 a[58:59], v203 offset:512\n"
"v_mfma_f32_16x16x32_bf16 v[136:139], a[160:163], a[136:139], v[136:139]\n"
"ds_read_b64_tr_b16 a[60:61], v203 offset:16384\n"
"v_mfma_f32_16x16x32_bf16 v[136:139], a[164:167], a[140:143], v[136:139]\n"
"s_add_u32 s99, s90, 0x280000\n"
"ds_read_b64_tr_b16 a[62:63], v203 offset:16896\n"
"v_mfma_f32_16x16x32_bf16 v[12:15], a[168:171], a[104:107], v[12:15]\n"
"s_add_u32 m0, s32, 0x1a000\n"
"ds_read_b64_tr_b16 a[64:65], v204 offset:0\n"
"v_mfma_f32_16x16x32_bf16 v[12:15], a[172:175], a[108:111], v[12:15]\n"
"buffer_load_dwordx4 v200, s[48:51], s99 offen offset:0 lds\n"
"v_mfma_f32_16x16x32_bf16 v[44:47], a[168:171], a[112:115], v[44:47]\n"
"ds_read_b64_tr_b16 a[66:67], v204 offset:512\n"
"v_mfma_f32_16x16x32_bf16 v[44:47], a[172:175], a[116:119], v[44:47]\n"
"ds_read_b64_tr_b16 a[68:69], v204 offset:16384\n"
"v_mfma_f32_16x16x32_bf16 v[76:79], a[168:171], a[120:123], v[76:79]\n"
"ds_read_b64_tr_b16 a[70:71], v204 offset:16896\n"
"v_mfma_f32_16x16x32_bf16 v[76:79], a[172:175], a[124:127], v[76:79]\n"
"s_add_u32 s99, s91, 0x280000\n"
"ds_read_b64_tr_b16 a[72:73], v201 offset:1024\n"
"v_mfma_f32_16x16x32_bf16 v[108:111], a[168:171], a[128:131], v[108:111]\n"
"s_add_u32 m0, s33, 0x1a000\n"
"ds_read_b64_tr_b16 a[74:75], v201 offset:1536\n"
"v_mfma_f32_16x16x32_bf16 v[108:111], a[172:175], a[132:135], v[108:111]\n"
"buffer_load_dwordx4 v200, s[48:51], s99 offen offset:0 lds\n"
"v_mfma_f32_16x16x32_bf16 v[140:143], a[168:171], a[136:139], v[140:143]\n"
"ds_read_b64_tr_b16 a[76:77], v201 offset:17408\n"
"v_mfma_f32_16x16x32_bf16 v[140:143], a[172:175], a[140:143], v[140:143]\n"
"ds_read_b64_tr_b16 a[78:79], v201 offset:17920\n"
"v_mfma_f32_16x16x32_bf16 v[16:19], a[176:179], a[104:107], v[16:19]\n"
"ds_read_b64_tr_b16 a[80:81], v202 offset:1024\n"
"v_mfma_f32_16x16x32_bf16 v[16:19], a[180:183], a[108:111], v[16:19]\n"
"s_add_u32 s99, s92, 0x280000\n"
"ds_read_b64_tr_b16 a[82:83], v202 offset:1536\n"
"v_mfma_f32_16x16x32_bf16 v[48:51], a[176:179], a[112:115], v[48:51]\n"
"s_add_u32 m0, s34, 0x1a000\n"
"ds_read_b64_tr_b16 a[84:85], v202 offset:17408\n"
"v_mfma_f32_16x16x32_bf16 v[48:51], a[180:183], a[116:119], v[48:51]\n"
"buffer_load_dwordx4 v200, s[48:51], s99 offen offset:0 lds\n"
"v_mfma_f32_16x16x32_bf16 v[80:83], a[176:179], a[120:123], v[80:83]\n"
"ds_read_b64_tr_b16 a[86:87], v202 offset:17920\n"
"v_mfma_f32_16x16x32_bf16 v[80:83], a[180:183], a[124:127], v[80:83]\n"
"ds_read_b64_tr_b16 a[88:89], v203 offset:1024\n"
"v_mfma_f32_16x16x32_bf16 v[112:115], a[176:179], a[128:131], v[112:115]\n"
"ds_read_b64_tr_b16 a[90:91], v203 offset:1536\n"
"v_mfma_f32_16x16x32_bf16 v[112:115], a[180:183], a[132:135], v[112:115]\n"
"s_add_u32 s99, s93, 0x280000\n"
"ds_read_b64_tr_b16 a[92:93], v203 offset:17408\n"
"v_mfma_f32_16x16x32_bf16 v[144:147], a[176:179], a[136:139], v[144:147]\n"
"s_add_u32 m0, s35, 0x1a000\n"
"ds_read_b64_tr_b16 a[94:95], v203 offset:17920\n"
"v_mfma_f32_16x16x32_bf16 v[144:147], a[180:183], a[140:143], v[144:147]\n"
"ds_read_b64_tr_b16 a[96:97], v204 offset:1024\n"
"v_mfma_f32_16x16x32_bf16 v[20:23], a[184:187], a[104:107], v[20:23]\n"
"ds_read_b64_tr_b16 a[98:99], v204 offset:1536\n"
"v_mfma_f32_16x16x32_bf16 v[20:23], a[188:191], a[108:111], v[20:23]\n"
"ds_read_b64_tr_b16 a[100:101], v204 offset:17408\n"
"v_mfma_f32_16x16x32_bf16 v[52:55], a[184:187], a[112:115], v[52:55]\n"
"ds_read_b64_tr_b16 a[102:103], v204 offset:17920\n"
"v_mfma_f32_16x16x32_bf16 v[52:55], a[188:191], a[116:119], v[52:55]\n"
"buffer_load_dwordx4 v200, s[48:51], s99 offen offset:0 lds\n"
"v_add_u32 v205, 0xd000, v205\n"
"v_mfma_f32_16x16x32_bf16 v[84:87], a[184:187], a[120:123], v[84:87]\n"
"v_add_u32 v206, 0xd000, v206\n"
"v_add_u32 v201, 0xd000, v201\n"
"v_mfma_f32_16x16x32_bf16 v[84:87], a[188:191], a[124:127], v[84:87]\n"
"s_waitcnt lgkmcnt(0)\n"
"v_add_u32 v202, 0xd000, v202\n"
"v_add_u32 v203, 0xd000, v203\n"
"v_mfma_f32_16x16x32_bf16 v[116:119], a[184:187], a[128:131], v[116:119]\n"
"s_barrier\n"
"v_add_u32 v204, 0xd000, v204\n"
"v_mfma_f32_16x16x32_bf16 v[116:119], a[188:191], a[132:135], v[116:119]\n"
"s_add_u32 s99, s86, 0x380000\n"
"v_mfma_f32_16x16x32_bf16 v[148:151], a[184:187], a[136:139], v[148:151]\n"
"s_add_u32 m0, s28, 0xd000\n"
"v_mfma_f32_16x16x32_bf16 v[148:151], a[188:191], a[140:143], v[148:151]\n"
"buffer_load_dwordx4 v200, s[48:51], s99 offen offset:0 lds\n"
"v_mfma_f32_16x16x32_bf16 v[24:27], a[192:195], a[104:107], v[24:27]\n"
"v_mfma_f32_16x16x32_bf16 v[24:27], a[196:199], a[108:111], v[24:27]\n"
"v_mfma_f32_16x16x32_bf16 v[56:59], a[192:195], a[112:115], v[56:59]\n"
"s_add_u32 s74, s94, 0x380\n"
"v_mfma_f32_16x16x32_bf16 v[56:59], a[196:199], a[116:119], v[56:59]\n"
"s_add_u32 m0, s36, 0xd000\n"
"v_mfma_f32_16x16x32_bf16 v[88:91], a[192:195], a[120:123], v[88:91]\n"
"buffer_load_dwordx4 v199, s[44:47], s74 offen offset:0 lds\n"
"v_mfma_f32_16x16x32_bf16 v[88:91], a[196:199], a[124:127], v[88:91]\n"
"v_mfma_f32_16x16x32_bf16 v[120:123], a[192:195], a[128:131], v[120:123]\n"
"v_mfma_f32_16x16x32_bf16 v[120:123], a[196:199], a[132:135], v[120:123]\n"
"v_mfma_f32_16x16x32_bf16 v[152:155], a[192:195], a[136:139], v[152:155]\n"
"s_add_u32 s74, s95, 0x380\n"
"v_mfma_f32_16x16x32_bf16 v[152:155], a[196:199], a[140:143], v[152:155]\n"
"s_add_u32 m0, s37, 0xd000\n"
"v_mfma_f32_16x16x32_bf16 v[28:31], a[200:203], a[104:107], v[28:31]\n"
"buffer_load_dwordx4 v199, s[44:47], s74 offen offset:0 lds\n"
"v_mfma_f32_16x16x32_bf16 v[28:31], a[204:207], a[108:111], v[28:31]\n"
"v_mfma_f32_16x16x32_bf16 v[60:63], a[200:203], a[112:115], v[60:63]\n"
"v_mfma_f32_16x16x32_bf16 v[60:63], a[204:207], a[116:119], v[60:63]\n"
"v_mfma_f32_16x16x32_bf16 v[92:95], a[200:203], a[120:123], v[92:95]\n"
"s_add_u32 s74, s96, 0x300\n"
"v_mfma_f32_16x16x32_bf16 v[92:95], a[204:207], a[124:127], v[92:95]\n"
"s_add_u32 m0, s38, 0\n"
"v_mfma_f32_16x16x32_bf16 v[124:127], a[200:203], a[128:131], v[124:127]\n"
"buffer_load_dwordx4 v199, s[44:47], s74 offen offset:0 lds\n"
"v_mfma_f32_16x16x32_bf16 v[124:127], a[204:207], a[132:135], v[124:127]\n"
"v_mfma_f32_16x16x32_bf16 v[156:159], a[200:203], a[136:139], v[156:159]\n"
"v_mfma_f32_16x16x32_bf16 v[156:159], a[204:207], a[140:143], v[156:159]\n"
"v_mfma_f32_16x16x32_bf16 v[0:3], a[40:43], a[0:3], v[0:3]\n"
"s_add_u32 s74, s97, 0x300\n"
"v_mfma_f32_16x16x32_bf16 v[0:3], a[44:47], a[4:7], v[0:3]\n"
"s_add_u32 m0, s39, 0\n"
"v_mfma_f32_16x16x32_bf16 v[32:35], a[40:43], a[8:11], v[32:35]\n"
"s_waitcnt vmcnt(4)\n"
"buffer_load_dwordx4 v199, s[44:47], s74 offen offset:0 lds\n"
"v_mfma_f32_16x16x32_bf16 v[32:35], a[44:47], a[12:15], v[32:35]\n"
"s_barrier\n"
"v_mfma_f32_16x16x32_bf16 v[64:67], a[40:43], a[16:19], v[64:67]\n"
"ds_read_b128 a[104:107], v205 offset:0\n"
"v_mfma_f32_16x16x32_bf16 v[64:67], a[44:47], a[20:23], v[64:67]\n"
"ds_read_b128 a[108:111], v206 offset:0\n"
"v_mfma_f32_16x16x32_bf16 v[96:99], a[40:43], a[24:27], v[96:99]\n"
"s_add_u32 s74, s98, 0x300\n"
"ds_read_b128 a[112:115], v205 offset:2048\n"
"v_mfma_f32_16x16x32_bf16 v[96:99], a[44:47], a[28:31], v[96:99]\n"
"s_add_u32 m0, s40, 0\n"
"ds_read_b128 a[116:119], v206 offset:2048\n"
"v_mfma_f32_16x16x32_bf16 v[128:131], a[40:43], a[32:35], v[128:131]\n"
"buffer_load_dwordx4 v199, s[44:47], s74 offen offset:0 lds\n"
"v_mfma_f32_16x16x32_bf16 v[128:131], a[44:47], a[36:39], v[128:131]\n"
"ds_read_b128 a[120:123], v205 offset:4096\n"
"v_mfma_f32_16x16x32_bf16 v[4:7], a[48:51], a[0:3], v[4:7]\n"
"ds_read_b128 a[124:127], v206 offset:4096\n"
"v_mfma_f32_16x16x32_bf16 v[4:7], a[52:55], a[4:7], v[4:7]\n"
"ds_read_b128 a[128:131], v205 offset:6144\n"
"v_mfma_f32_16x16x32_bf16 v[36:39], a[48:51], a[8:11], v[36:39]\n"
"s_add_u32 s99, s87, 0x300000\n"
"ds_read_b128 a[132:135], v206 offset:6144\n"
"v_mfma_f32_16x16x32_bf16 v[36:39], a[52:55], a[12:15], v[36:39]\n"
"s_add_u32 m0, s29, 0\n"
"ds_read_b128 a[136:139], v205 offset:8192\n"
"v_mfma_f32_16x16x32_bf16 v[68:71], a[48:51], a[16:19], v[68:71]\n"
"buffer_load_dwordx4 v200, s[48:51], s99 offen offset:0 lds\n"
"v_mfma_f32_16x16x32_bf16 v[68:71], a[52:55], a[20:23], v[68:71]\n"
"ds_read_b128 a[140:143], v206 offset:8192\n"
"v_mfma_f32_16x16x32_bf16 v[100:103], a[48:51], a[24:27], v[100:103]\n"
"ds_read_b64_tr_b16 a[144:145], v201 offset:0\n"
"v_mfma_f32_16x16x32_bf16 v[100:103], a[52:55], a[28:31], v[100:103]\n"
"s_add_u32 s99, s88, 0x300000\n"
"ds_read_b64_tr_b16 a[146:147], v201 offset:512\n"
"v_mfma_f32_16x16x32_bf16 v[132:135], a[48:51], a[32:35], v[132:135]\n"
"s_add_u32 m0, s30, 0\n"
"ds_read_b64_tr_b16 a[148:149], v201 offset:16384\n"
"v_mfma_f32_16x16x32_bf16 v[132:135], a[52:55], a[36:39], v[132:135]\n"
"buffer_load_dwordx4 v200, s[48:51], s99 offen offset:0 lds\n"
"v_mfma_f32_16x16x32_bf16 v[8:11], a[56:59], a[0:3], v[8:11]\n"
"ds_read_b64_tr_b16 a[150:151], v201 offset:16896\n"
"v_mfma_f32_16x16x32_bf16 v[8:11], a[60:63], a[4:7], v[8:11]\n"
"ds_read_b64_tr_b16 a[152:153], v202 offset:0\n"
"v_mfma_f32_16x16x32_bf16 v[40:43], a[56:59], a[8:11], v[40:43]\n"
"ds_read_b64_tr_b16 a[154:155], v202 offset:512\n"
"v_mfma_f32_16x16x32_bf16 v[40:43], a[60:63], a[12:15], v[40:43]\n"
"s_add_u32 s99, s89, 0x300000\n"
"ds_read_b64_tr_b16 a[156:157], v202 offset:16384\n"
"v_mfma_f32_16x16x32_bf16 v[72:75], a[56:59], a[16:19], v[72:75]\n"
"s_add_u32 m0, s31, 0\n"
"ds_read_b64_tr_b16 a[158:159], v202 offset:16896\n"
"v_mfma_f32_16x16x32_bf16 v[72:75], a[60:63], a[20:23], v[72:75]\n"
"buffer_load_dwordx4 v200, s[48:51], s99 offen offset:0 lds\n"
"v_mfma_f32_16x16x32_bf16 v[104:107], a[56:59], a[24:27], v[104:107]\n"
"ds_read_b64_tr_b16 a[160:161], v203 offset:0\n"
"v_mfma_f32_16x16x32_bf16 v[104:107], a[60:63], a[28:31], v[104:107]\n"
"ds_read_b64_tr_b16 a[162:163], v203 offset:512\n"
"v_mfma_f32_16x16x32_bf16 v[136:139], a[56:59], a[32:35], v[136:139]\n"
"ds_read_b64_tr_b16 a[164:165], v203 offset:16384\n"
"v_mfma_f32_16x16x32_bf16 v[136:139], a[60:63], a[36:39], v[136:139]\n"
"s_add_u32 s99, s90, 0x300000\n"
"ds_read_b64_tr_b16 a[166:167], v203 offset:16896\n"
"v_mfma_f32_16x16x32_bf16 v[12:15], a[64:67], a[0:3], v[12:15]\n"
"s_add_u32 m0, s32, 0\n"
"ds_read_b64_tr_b16 a[168:169], v204 offset:0\n"
"v_mfma_f32_16x16x32_bf16 v[12:15], a[68:71], a[4:7], v[12:15]\n"
"buffer_load_dwordx4 v200, s[48:51], s99 offen offset:0 lds\n"
"v_mfma_f32_16x16x32_bf16 v[44:47], a[64:67], a[8:11], v[44:47]\n"
"ds_read_b64_tr_b16 a[170:171], v204 offset:512\n"
"v_mfma_f32_16x16x32_bf16 v[44:47], a[68:71], a[12:15], v[44:47]\n"
"ds_read_b64_tr_b16 a[172:173], v204 offset:16384\n"
"v_mfma_f32_16x16x32_bf16 v[76:79], a[64:67], a[16:19], v[76:79]\n"
"ds_read_b64_tr_b16 a[174:175], v204 offset:16896\n"
"v_mfma_f32_16x16x32_bf16 v[76:79], a[68:71], a[20:23], v[76:79]\n"
"s_add_u32 s99, s91, 0x300000\n"
"ds_read_b64_tr_b16 a[176:177], v201 offset:1024\n"
"v_mfma_f32_16x16x32_bf16 v[108:111], a[64:67], a[24:27], v[108:111]\n"
"s_add_u32 m0, s33, 0\n"
"ds_read_b64_tr_b16 a[178:179], v201 offset:1536\n"
"v_mfma_f32_16x16x32_bf16 v[108:111], a[68:71], a[28:31], v[108:111]\n"
"buffer_load_dwordx4 v200, s[48:51], s99 offen offset:0 lds\n"
"v_mfma_f32_16x16x32_bf16 v[140:143], a[64:67], a[32:35], v[140:143]\n"
"ds_read_b64_tr_b16 a[180:181], v201 offset:17408\n"
"v_mfma_f32_16x16x32_bf16 v[140:143], a[68:71], a[36:39], v[140:143]\n"
"ds_read_b64_tr_b16 a[182:183], v201 offset:17920\n"
"v_mfma_f32_16x16x32_bf16 v[16:19], a[72:75], a[0:3], v[16:19]\n"
"ds_read_b64_tr_b16 a[184:185], v202 offset:1024\n"
"v_mfma_f32_16x16x32_bf16 v[16:19], a[76:79], a[4:7], v[16:19]\n"
"s_add_u32 s99, s92, 0x300000\n"
"ds_read_b64_tr_b16 a[186:187], v202 offset:1536\n"
"v_mfma_f32_16x16x32_bf16 v[48:51], a[72:75], a[8:11], v[48:51]\n"
"s_add_u32 m0, s34, 0\n"
"ds_read_b64_tr_b16 a[188:189], v202 offset:17408\n"
"v_mfma_f32_16x16x32_bf16 v[48:51], a[76:79], a[12:15], v[48:51]\n"
"buffer_load_dwordx4 v200, s[48:51], s99 offen offset:0 lds\n"
"v_mfma_f32_16x16x32_bf16 v[80:83], a[72:75], a[16:19], v[80:83]\n"
"ds_read_b64_tr_b16 a[190:191], v202 offset:17920\n"
"v_mfma_f32_16x16x32_bf16 v[80:83], a[76:79], a[20:23], v[80:83]\n"
"ds_read_b64_tr_b16 a[192:193], v203 offset:1024\n"
"v_mfma_f32_16x16x32_bf16 v[112:115], a[72:75], a[24:27], v[112:115]\n"
"ds_read_b64_tr_b16 a[194:195], v203 offset:1536\n"
"v_mfma_f32_16x16x32_bf16 v[112:115], a[76:79], a[28:31], v[112:115]\n"
"s_add_u32 s99, s93, 0x300000\n"
"ds_read_b64_tr_b16 a[196:197], v203 offset:17408\n"
"v_mfma_f32_16x16x32_bf16 v[144:147], a[72:75], a[32:35], v[144:147]\n"
"s_add_u32 m0, s35, 0\n"
"ds_read_b64_tr_b16 a[198:199], v203 offset:17920\n"
"v_mfma_f32_16x16x32_bf16 v[144:147], a[76:79], a[36:39], v[144:147]\n"
"ds_read_b64_tr_b16 a[200:201], v204 offset:1024\n"
"v_mfma_f32_16x16x32_bf16 v[20:23], a[80:83], a[0:3], v[20:23]\n"
"ds_read_b64_tr_b16 a[202:203], v204 offset:1536\n"
"v_mfma_f32_16x16x32_bf16 v[20:23], a[84:87], a[4:7], v[20:23]\n"
"ds_read_b64_tr_b16 a[204:205], v204 offset:17408\n"
"v_mfma_f32_16x16x32_bf16 v[52:55], a[80:83], a[8:11], v[52:55]\n"
"ds_read_b64_tr_b16 a[206:207], v204 offset:17920\n"
"v_mfma_f32_16x16x32_bf16 v[52:55], a[84:87], a[12:15], v[52:55]\n"
"buffer_load_dwordx4 v200, s[48:51], s99 offen offset:0 lds\n"
"v_add_u32 v205, 0xfffe6000, v205\n"
"v_mfma_f32_16x16x32_bf16 v[84:87], a[80:83], a[16:19], v[84:87]\n"
"v_add_u32 v206, 0xfffe6000, v206\n"
"v_add_u32 v201, 0xfffe6000, v201\n"
"v_mfma_f32_16x16x32_bf16 v[84:87], a[84:87], a[20:23], v[84:87]\n"
"s_waitcnt lgkmcnt(0)\n"
"v_add_u32 v202, 0xfffe6000, v202\n"
"v_add_u32 v203, 0xfffe6000, v203\n"
"v_mfma_f32_16x16x32_bf16 v[116:119], a[80:83], a[24:27], v[116:119]\n"
"s_barrier\n"
"v_add_u32 v204, 0xfffe6000, v204\n"
"v_mfma_f32_16x16x32_bf16 v[116:119], a[84:87], a[28:31], v[116:119]\n"
"s_add_u32 s99, s86, 0x400000\n"
"v_mfma_f32_16x16x32_bf16 v[148:151], a[80:83], a[32:35], v[148:151]\n"
"s_add_u32 m0, s28, 0x1a000\n"
"v_mfma_f32_16x16x32_bf16 v[148:151], a[84:87], a[36:39], v[148:151]\n"
"buffer_load_dwordx4 v200, s[48:51], s99 offen offset:0 lds\n"
"v_mfma_f32_16x16x32_bf16 v[24:27], a[88:91], a[0:3], v[24:27]\n"
"v_mfma_f32_16x16x32_bf16 v[24:27], a[92:95], a[4:7], v[24:27]\n"
"v_mfma_f32_16x16x32_bf16 v[56:59], a[88:91], a[8:11], v[56:59]\n"
"s_add_u32 s74, s94, 0x400\n"
"v_mfma_f32_16x16x32_bf16 v[56:59], a[92:95], a[12:15], v[56:59]\n"
"s_add_u32 m0, s36, 0x1a000\n"
"v_mfma_f32_16x16x32_bf16 v[88:91], a[88:91], a[16:19], v[88:91]\n"
"buffer_load_dwordx4 v199, s[44:47], s74 offen offset:0 lds\n"
"v_mfma_f32_16x16x32_bf16 v[88:91], a[92:95], a[20:23], v[88:91]\n"
"v_mfma_f32_16x16x32_bf16 v[120:123], a[88:91], a[24:27], v[120:123]\n"
"v_mfma_f32_16x16x32_bf16 v[120:123], a[92:95], a[28:31], v[120:123]\n"
"v_mfma_f32_16x16x32_bf16 v[152:155], a[88:91], a[32:35], v[152:155]\n"
"s_add_u32 s74, s95, 0x400\n"
"v_mfma_f32_16x16x32_bf16 v[152:155], a[92:95], a[36:39], v[152:155]\n"
"s_add_u32 m0, s37, 0x1a000\n"
"v_mfma_f32_16x16x32_bf16 v[28:31], a[96:99], a[0:3], v[28:31]\n"
"buffer_load_dwordx4 v199, s[44:47], s74 offen offset:0 lds\n"
"v_mfma_f32_16x16x32_bf16 v[28:31], a[100:103], a[4:7], v[28:31]\n"
"v_mfma_f32_16x16x32_bf16 v[60:63], a[96:99], a[8:11], v[60:63]\n"
"v_mfma_f32_16x16x32_bf16 v[60:63], a[100:103], a[12:15], v[60:63]\n"
"v_mfma_f32_16x16x32_bf16 v[92:95], a[96:99], a[16:19], v[92:95]\n"
"s_add_u32 s74, s96, 0x380\n"
"v_mfma_f32_16x16x32_bf16 v[92:95], a[100:103], a[20:23], v[92:95]\n"
"s_add_u32 m0, s38, 0xd000\n"
"v_mfma_f32_16x16x32_bf16 v[124:127], a[96:99], a[24:27], v[124:127]\n"
"buffer_load_dwordx4 v199, s[44:47], s74 offen offset:0 lds\n"
"v_mfma_f32_16x16x32_bf16 v[124:127], a[100:103], a[28:31], v[124:127]\n"
"v_mfma_f32_16x16x32_bf16 v[156:159], a[96:99], a[32:35], v[156:159]\n"
"v_mfma_f32_16x16x32_bf16 v[156:159], a[100:103], a[36:39], v[156:159]\n"
"v_mfma_f32_16x16x32_bf16 v[0:3], a[144:147], a[104:107], v[0:3]\n"
"s_add_u32 s74, s97, 0x380\n"
"v_mfma_f32_16x16x32_bf16 v[0:3], a[148:151], a[108:111], v[0:3]\n"
"s_add_u32 m0, s39, 0xd000\n"
"v_mfma_f32_16x16x32_bf16 v[32:35], a[144:147], a[112:115], v[32:35]\n"
"s_waitcnt vmcnt(4)\n"
"buffer_load_dwordx4 v199, s[44:47], s74 offen offset:0 lds\n"
"v_mfma_f32_16x16x32_bf16 v[32:35], a[148:151], a[116:119], v[32:35]\n"
"s_barrier\n"
"v_mfma_f32_16x16x32_bf16 v[64:67], a[144:147], a[120:123], v[64:67]\n"
"ds_read_b128 a[0:3], v205 offset:0\n"
"v_mfma_f32_16x16x32_bf16 v[64:67], a[148:151], a[124:127], v[64:67]\n"
"ds_read_b128 a[4:7], v206 offset:0\n"
"v_mfma_f32_16x16x32_bf16 v[96:99], a[144:147], a[128:131], v[96:99]\n"
"s_add_u32 s74, s98, 0x380\n"
"ds_read_b128 a[8:11], v205 offset:2048\n"
"v_mfma_f32_16x16x32_bf16 v[96:99], a[148:151], a[132:135], v[96:99]\n"
"s_add_u32 m0, s40, 0xd000\n"
"ds_read_b128 a[12:15], v206 offset:2048\n"
"v_mfma_f32_16x16x32_bf16 v[128:131], a[144:147], a[136:139], v[128:131]\n"
"buffer_load_dwordx4 v199, s[44:47], s74 offen offset:0 lds\n"
"v_mfma_f32_16x16x32_bf16 v[128:131], a[148:151], a[140:143], v[128:131]\n"
"ds_read_b128 a[16:19], v205 offset:4096\n"
"v_mfma_f32_16x16x32_bf16 v[4:7], a[152:155], a[104:107], v[4:7]\n"
"ds_read_b128 a[20:23], v206 offset:4096\n"
"v_mfma_f32_16x16x32_bf16 v[4:7], a[156:159], a[108:111], v[4:7]\n"
"ds_read_b128 a[24:27], v205 offset:6144\n"
"v_mfma_f32_16x16x32_bf16 v[36:39], a[152:155], a[112:115], v[36:39]\n"
"s_add_u32 s99, s87, 0x380000\n"
"ds_read_b128 a[28:31], v206 offset:6144\n"
"v_mfma_f32_16x16x32_bf16 v[36:39], a[156:159], a[116:119], v[36:39]\n"
"s_add_u32 m0, s29, 0xd000\n"
"ds_read_b128 a[32:35], v205 offset:8192\n"
"v_mfma_f32_16x16x32_bf16 v[68:71], a[152:155], a[120:123], v[68:71]\n"
"buffer_load_dwordx4 v200, s[48:51], s99 offen offset:0 lds\n"
"v_mfma_f32_16x16x32_bf16 v[68:71], a[156:159], a[124:127], v[68:71]\n"
"ds_read_b128 a[36:39], v206 offset:8192\n"
"v_mfma_f32_16x16x32_bf16 v[100:103], a[152:155], a[128:131], v[100:103]\n"
"ds_read_b64_tr_b16 a[40:41], v201 offset:0\n"
"v_mfma_f32_16x16x32_bf16 v[100:103], a[156:159], a[132:135], v[100:103]\n"
"s_add_u32 s99, s88, 0x380000\n"
"ds_read_b64_tr_b16 a[42:43], v201 offset:512\n"
"v_mfma_f32_16x16x32_bf16 v[132:135], a[152:155], a[136:139], v[132:135]\n"
"s_add_u32 m0, s30, 0xd000\n"
"ds_read_b64_tr_b16 a[44:45], v201 offset:16384\n"
"v_mfma_f32_16x16x32_bf16 v[132:135], a[156:159], a[140:143], v[132:135]\n"
"buffer_load_dwordx4 v200, s[48:51], s99 offen offset:0 lds\n"
"v_mfma_f32_16x16x32_bf16 v[8:11], a[160:163], a[104:107], v[8:11]\n"
"ds_read_b64_tr_b16 a[46:47], v201 offset:16896\n"
"v_mfma_f32_16x16x32_bf16 v[8:11], a[164:167], a[108:111], v[8:11]\n"
"ds_read_b64_tr_b16 a[48:49], v202 offset:0\n"
"v_mfma_f32_16x16x32_bf16 v[40:43], a[160:163], a[112:115], v[40:43]\n"
"ds_read_b64_tr_b16 a[50:51], v202 offset:512\n"
"v_mfma_f32_16x16x32_bf16 v[40:43], a[164:167], a[116:119], v[40:43]\n"
"s_add_u32 s99, s89, 0x380000\n"
"ds_read_b64_tr_b16 a[52:53], v202 offset:16384\n"
"v_mfma_f32_16x16x32_bf16 v[72:75], a[160:163], a[120:123], v[72:75]\n"
"s_add_u32 m0, s31, 0xd000\n"
"ds_read_b64_tr_b16 a[54:55], v202 offset:16896\n"
"v_mfma_f32_16x16x32_bf16 v[72:75], a[164:167], a[124:127], v[72:75]\n"
"buffer_load_dwordx4 v200, s[48:51], s99 offen offset:0 lds\n"
"v_mfma_f32_16x16x32_bf16 v[104:107], a[160:163], a[128:131], v[104:107]\n"
"ds_read_b64_tr_b16 a[56:57], v203 offset:0\n"
"v_mfma_f32_16x16x32_bf16 v[104:107], a[164:167], a[132:135], v[104:107]\n"
"ds_read_b64_tr_b16 a[58:59], v203 offset:512\n"
"v_mfma_f32_16x16x32_bf16 v[136:139], a[160:163], a[136:139], v[136:139]\n"
"ds_read_b64_tr_b16 a[60:61], v203 offset:16384\n"
"v_mfma_f32_16x16x32_bf16 v[136:139], a[164:167], a[140:143], v[136:139]\n"
"s_add_u32 s99, s90, 0x380000\n"
"ds_read_b64_tr_b16 a[62:63], v203 offset:16896\n"
"v_mfma_f32_16x16x32_bf16 v[12:15], a[168:171], a[104:107], v[12:15]\n"
"s_add_u32 m0, s32, 0xd000\n"
"ds_read_b64_tr_b16 a[64:65], v204 offset:0\n"
"v_mfma_f32_16x16x32_bf16 v[12:15], a[172:175], a[108:111], v[12:15]\n"
"buffer_load_dwordx4 v200, s[48:51], s99 offen offset:0 lds\n"
"v_mfma_f32_16x16x32_bf16 v[44:47], a[168:171], a[112:115], v[44:47]\n"
"ds_read_b64_tr_b16 a[66:67], v204 offset:512\n"
"v_mfma_f32_16x16x32_bf16 v[44:47], a[172:175], a[116:119], v[44:47]\n"
"ds_read_b64_tr_b16 a[68:69], v204 offset:16384\n"
"v_mfma_f32_16x16x32_bf16 v[76:79], a[168:171], a[120:123], v[76:79]\n"
"ds_read_b64_tr_b16 a[70:71], v204 offset:16896\n"
"v_mfma_f32_16x16x32_bf16 v[76:79], a[172:175], a[124:127], v[76:79]\n"
"s_add_u32 s99, s91, 0x380000\n"
"ds_read_b64_tr_b16 a[72:73], v201 offset:1024\n"
"v_mfma_f32_16x16x32_bf16 v[108:111], a[168:171], a[128:131], v[108:111]\n"
"s_add_u32 m0, s33, 0xd000\n"
"ds_read_b64_tr_b16 a[74:75], v201 offset:1536\n"
"v_mfma_f32_16x16x32_bf16 v[108:111], a[172:175], a[132:135], v[108:111]\n"
"buffer_load_dwordx4 v200, s[48:51], s99 offen offset:0 lds\n"
"v_mfma_f32_16x16x32_bf16 v[140:143], a[168:171], a[136:139], v[140:143]\n"
"ds_read_b64_tr_b16 a[76:77], v201 offset:17408\n"
"v_mfma_f32_16x16x32_bf16 v[140:143], a[172:175], a[140:143], v[140:143]\n"
"ds_read_b64_tr_b16 a[78:79], v201 offset:17920\n"
"v_mfma_f32_16x16x32_bf16 v[16:19], a[176:179], a[104:107], v[16:19]\n"
"ds_read_b64_tr_b16 a[80:81], v202 offset:1024\n"
"v_mfma_f32_16x16x32_bf16 v[16:19], a[180:183], a[108:111], v[16:19]\n"
"s_add_u32 s99, s92, 0x380000\n"
"ds_read_b64_tr_b16 a[82:83], v202 offset:1536\n"
"v_mfma_f32_16x16x32_bf16 v[48:51], a[176:179], a[112:115], v[48:51]\n"
"s_add_u32 m0, s34, 0xd000\n"
"ds_read_b64_tr_b16 a[84:85], v202 offset:17408\n"
"v_mfma_f32_16x16x32_bf16 v[48:51], a[180:183], a[116:119], v[48:51]\n"
"buffer_load_dwordx4 v200, s[48:51], s99 offen offset:0 lds\n"
"v_mfma_f32_16x16x32_bf16 v[80:83], a[176:179], a[120:123], v[80:83]\n"
"ds_read_b64_tr_b16 a[86:87], v202 offset:17920\n"
"v_mfma_f32_16x16x32_bf16 v[80:83], a[180:183], a[124:127], v[80:83]\n"
"ds_read_b64_tr_b16 a[88:89], v203 offset:1024\n"
"v_mfma_f32_16x16x32_bf16 v[112:115], a[176:179], a[128:131], v[112:115]\n"
"ds_read_b64_tr_b16 a[90:91], v203 offset:1536\n"
"v_mfma_f32_16x16x32_bf16 v[112:115], a[180:183], a[132:135], v[112:115]\n"
"s_add_u32 s99, s93, 0x380000\n"
"ds_read_b64_tr_b16 a[92:93], v203 offset:17408\n"
"v_mfma_f32_16x16x32_bf16 v[144:147], a[176:179], a[136:139], v[144:147]\n"
"s_add_u32 m0, s35, 0xd000\n"
"ds_read_b64_tr_b16 a[94:95], v203 offset:17920\n"
"v_mfma_f32_16x16x32_bf16 v[144:147], a[180:183], a[140:143], v[144:147]\n"
"ds_read_b64_tr_b16 a[96:97], v204 offset:1024\n"
"v_mfma_f32_16x16x32_bf16 v[20:23], a[184:187], a[104:107], v[20:23]\n"
"ds_read_b64_tr_b16 a[98:99], v204 offset:1536\n"
"v_mfma_f32_16x16x32_bf16 v[20:23], a[188:191], a[108:111], v[20:23]\n"
"ds_read_b64_tr_b16 a[100:101], v204 offset:17408\n"
"v_mfma_f32_16x16x32_bf16 v[52:55], a[184:187], a[112:115], v[52:55]\n"
"ds_read_b64_tr_b16 a[102:103], v204 offset:17920\n"
"v_mfma_f32_16x16x32_bf16 v[52:55], a[188:191], a[116:119], v[52:55]\n"
"buffer_load_dwordx4 v200, s[48:51], s99 offen offset:0 lds\n"
"v_add_u32 v205, 0xd000, v205\n"
"v_mfma_f32_16x16x32_bf16 v[84:87], a[184:187], a[120:123], v[84:87]\n"
"v_add_u32 v206, 0xd000, v206\n"
"v_add_u32 v201, 0xd000, v201\n"
"v_mfma_f32_16x16x32_bf16 v[84:87], a[188:191], a[124:127], v[84:87]\n"
"s_waitcnt lgkmcnt(0)\n"
"v_add_u32 v202, 0xd000, v202\n"
"v_add_u32 v203, 0xd000, v203\n"
"v_mfma_f32_16x16x32_bf16 v[116:119], a[184:187], a[128:131], v[116:119]\n"
"s_barrier\n"
"v_add_u32 v204, 0xd000, v204\n"
"v_mfma_f32_16x16x32_bf16 v[116:119], a[188:191], a[132:135], v[116:119]\n"
"s_add_u32 s74, s96, 0x400\n"
"v_mfma_f32_16x16x32_bf16 v[148:151], a[184:187], a[136:139], v[148:151]\n"
"s_add_u32 m0, s38, 0x1a000\n"
"v_mfma_f32_16x16x32_bf16 v[148:151], a[188:191], a[140:143], v[148:151]\n"
"buffer_load_dwordx4 v199, s[44:47], s74 offen offset:0 lds\n"
"v_mfma_f32_16x16x32_bf16 v[24:27], a[192:195], a[104:107], v[24:27]\n"
"v_mfma_f32_16x16x32_bf16 v[24:27], a[196:199], a[108:111], v[24:27]\n"
"v_mfma_f32_16x16x32_bf16 v[56:59], a[192:195], a[112:115], v[56:59]\n"
"s_add_u32 s74, s97, 0x400\n"
"v_mfma_f32_16x16x32_bf16 v[56:59], a[196:199], a[116:119], v[56:59]\n"
"s_add_u32 m0, s39, 0x1a000\n"
"v_mfma_f32_16x16x32_bf16 v[88:91], a[192:195], a[120:123], v[88:91]\n"
"buffer_load_dwordx4 v199, s[44:47], s74 offen offset:0 lds\n"
"v_mfma_f32_16x16x32_bf16 v[88:91], a[196:199], a[124:127], v[88:91]\n"
"v_mfma_f32_16x16x32_bf16 v[120:123], a[192:195], a[128:131], v[120:123]\n"
"v_mfma_f32_16x16x32_bf16 v[120:123], a[196:199], a[132:135], v[120:123]\n"
"s_add_u32 s74, s98, 0x400\n"
"v_mfma_f32_16x16x32_bf16 v[152:155], a[192:195], a[136:139], v[152:155]\n"
"s_add_u32 m0, s40, 0x1a000\n"
"v_mfma_f32_16x16x32_bf16 v[152:155], a[196:199], a[140:143], v[152:155]\n"
"buffer_load_dwordx4 v199, s[44:47], s74 offen offset:0 lds\n"
"v_mfma_f32_16x16x32_bf16 v[28:31], a[200:203], a[104:107], v[28:31]\n"
"v_mfma_f32_16x16x32_bf16 v[28:31], a[204:207], a[108:111], v[28:31]\n"
"v_mfma_f32_16x16x32_bf16 v[60:63], a[200:203], a[112:115], v[60:63]\n"
"v_mfma_f32_16x16x32_bf16 v[60:63], a[204:207], a[116:119], v[60:63]\n"
"s_add_u32 s99, s87, 0x400000\n"
"v_mfma_f32_16x16x32_bf16 v[92:95], a[200:203], a[120:123], v[92:95]\n"
"s_add_u32 m0, s29, 0x1a000\n"
"v_mfma_f32_16x16x32_bf16 v[92:95], a[204:207], a[124:127], v[92:95]\n"
"buffer_load_dwordx4 v200, s[48:51], s99 offen offset:0 lds\n"
"v_mfma_f32_16x16x32_bf16 v[124:127], a[200:203], a[128:131], v[124:127]\n"
"v_mfma_f32_16x16x32_bf16 v[124:127], a[204:207], a[132:135], v[124:127]\n"
"v_mfma_f32_16x16x32_bf16 v[156:159], a[200:203], a[136:139], v[156:159]\n"
"s_add_u32 s99, s88, 0x400000\n"
"v_mfma_f32_16x16x32_bf16 v[156:159], a[204:207], a[140:143], v[156:159]\n"
"s_add_u32 m0, s30, 0x1a000\n"
"v_mfma_f32_16x16x32_bf16 v[0:3], a[40:43], a[0:3], v[0:3]\n"
"buffer_load_dwordx4 v200, s[48:51], s99 offen offset:0 lds\n"
"v_mfma_f32_16x16x32_bf16 v[0:3], a[44:47], a[4:7], v[0:3]\n"
"v_mfma_f32_16x16x32_bf16 v[32:35], a[40:43], a[8:11], v[32:35]\n"
"s_waitcnt vmcnt(5)\n"
"v_mfma_f32_16x16x32_bf16 v[32:35], a[44:47], a[12:15], v[32:35]\n"
"s_barrier\n"
"v_mfma_f32_16x16x32_bf16 v[64:67], a[40:43], a[16:19], v[64:67]\n"
"s_add_u32 s99, s89, 0x400000\n"
"ds_read_b128 a[104:107], v205 offset:0\n"
"v_mfma_f32_16x16x32_bf16 v[64:67], a[44:47], a[20:23], v[64:67]\n"
"s_add_u32 m0, s31, 0x1a000\n"
"ds_read_b128 a[108:111], v206 offset:0\n"
"v_mfma_f32_16x16x32_bf16 v[96:99], a[40:43], a[24:27], v[96:99]\n"
"buffer_load_dwordx4 v200, s[48:51], s99 offen offset:0 lds\n"
"v_mfma_f32_16x16x32_bf16 v[96:99], a[44:47], a[28:31], v[96:99]\n"
"ds_read_b128 a[112:115], v205 offset:2048\n"
"v_mfma_f32_16x16x32_bf16 v[128:131], a[40:43], a[32:35], v[128:131]\n"
"ds_read_b128 a[116:119], v206 offset:2048\n"
"v_mfma_f32_16x16x32_bf16 v[128:131], a[44:47], a[36:39], v[128:131]\n"
"s_add_u32 s99, s90, 0x400000\n"
"ds_read_b128 a[120:123], v205 offset:4096\n"
"v_mfma_f32_16x16x32_bf16 v[4:7], a[48:51], a[0:3], v[4:7]\n"
"s_add_u32 m0, s32, 0x1a000\n"
"ds_read_b128 a[124:127], v206 offset:4096\n"
"v_mfma_f32_16x16x32_bf16 v[4:7], a[52:55], a[4:7], v[4:7]\n"
"buffer_load_dwordx4 v200, s[48:51], s99 offen offset:0 lds\n"
"v_mfma_f32_16x16x32_bf16 v[36:39], a[48:51], a[8:11], v[36:39]\n"
"ds_read_b128 a[128:131], v205 offset:6144\n"
"v_mfma_f32_16x16x32_bf16 v[36:39], a[52:55], a[12:15], v[36:39]\n"
"ds_read_b128 a[132:135], v206 offset:6144\n"
"v_mfma_f32_16x16x32_bf16 v[68:71], a[48:51], a[16:19], v[68:71]\n"
"ds_read_b128 a[136:139], v205 offset:8192\n"
"v_mfma_f32_16x16x32_bf16 v[68:71], a[52:55], a[20:23], v[68:71]\n"
"s_add_u32 s99, s91, 0x400000\n"
"ds_read_b128 a[140:143], v206 offset:8192\n"
"v_mfma_f32_16x16x32_bf16 v[100:103], a[48:51], a[24:27], v[100:103]\n"
"s_add_u32 m0, s33, 0x1a000\n"
"ds_read_b64_tr_b16 a[144:145], v201 offset:0\n"
"v_mfma_f32_16x16x32_bf16 v[100:103], a[52:55], a[28:31], v[100:103]\n"
"buffer_load_dwordx4 v200, s[48:51], s99 offen offset:0 lds\n"
"v_mfma_f32_16x16x32_bf16 v[132:135], a[48:51], a[32:35], v[132:135]\n"
"ds_read_b64_tr_b16 a[146:147], v201 offset:512\n"
"v_mfma_f32_16x16x32_bf16 v[132:135], a[52:55], a[36:39], v[132:135]\n"
"ds_read_b64_tr_b16 a[148:149], v201 offset:16384\n"
"v_mfma_f32_16x16x32_bf16 v[8:11], a[56:59], a[0:3], v[8:11]\n"
"s_add_u32 s99, s92, 0x400000\n"
"ds_read_b64_tr_b16 a[150:151], v201 offset:16896\n"
"v_mfma_f32_16x16x32_bf16 v[8:11], a[60:63], a[4:7], v[8:11]\n"
"s_add_u32 m0, s34, 0x1a000\n"
"ds_read_b64_tr_b16 a[152:153], v202 offset:0\n"
"v_mfma_f32_16x16x32_bf16 v[40:43], a[56:59], a[8:11], v[40:43]\n"
"buffer_load_dwordx4 v200, s[48:51], s99 offen offset:0 lds\n"
"v_mfma_f32_16x16x32_bf16 v[40:43], a[60:63], a[12:15], v[40:43]\n"
"ds_read_b64_tr_b16 a[154:155], v202 offset:512\n"
"v_mfma_f32_16x16x32_bf16 v[72:75], a[56:59], a[16:19], v[72:75]\n"
"ds_read_b64_tr_b16 a[156:157], v202 offset:16384\n"
"v_mfma_f32_16x16x32_bf16 v[72:75], a[60:63], a[20:23], v[72:75]\n"
"s_add_u32 s99, s93, 0x400000\n"
"ds_read_b64_tr_b16 a[158:159], v202 offset:16896\n"
"v_mfma_f32_16x16x32_bf16 v[104:107], a[56:59], a[24:27], v[104:107]\n"
"s_add_u32 m0, s35, 0x1a000\n"
"ds_read_b64_tr_b16 a[160:161], v203 offset:0\n"
"v_mfma_f32_16x16x32_bf16 v[104:107], a[60:63], a[28:31], v[104:107]\n"
"buffer_load_dwordx4 v200, s[48:51], s99 offen offset:0 lds\n"
"v_mfma_f32_16x16x32_bf16 v[136:139], a[56:59], a[32:35], v[136:139]\n"
"ds_read_b64_tr_b16 a[162:163], v203 offset:512\n"
"v_mfma_f32_16x16x32_bf16 v[136:139], a[60:63], a[36:39], v[136:139]\n"
"ds_read_b64_tr_b16 a[164:165], v203 offset:16384\n"
"v_mfma_f32_16x16x32_bf16 v[12:15], a[64:67], a[0:3], v[12:15]\n"
"ds_read_b64_tr_b16 a[166:167], v203 offset:16896\n"
"v_mfma_f32_16x16x32_bf16 v[12:15], a[68:71], a[4:7], v[12:15]\n"
"ds_read_b64_tr_b16 a[168:169], v204 offset:0\n"
"v_mfma_f32_16x16x32_bf16 v[44:47], a[64:67], a[8:11], v[44:47]\n"
"s_add_u32 m0, s78, 0\n"
"ds_read_b64_tr_b16 a[170:171], v204 offset:512\n"
"v_mfma_f32_16x16x32_bf16 v[44:47], a[68:71], a[12:15], v[44:47]\n"
"buffer_load_dwordx4 v198, s[52:55], s75 offen offset:0 nt lds\n"
"v_mfma_f32_16x16x32_bf16 v[76:79], a[64:67], a[16:19], v[76:79]\n"
"ds_read_b64_tr_b16 a[172:173], v204 offset:16384\n"
"v_mfma_f32_16x16x32_bf16 v[76:79], a[68:71], a[20:23], v[76:79]\n"
"ds_read_b64_tr_b16 a[174:175], v204 offset:16896\n"
"v_mfma_f32_16x16x32_bf16 v[108:111], a[64:67], a[24:27], v[108:111]\n"
"ds_read_b64_tr_b16 a[176:177], v201 offset:1024\n"
"v_mfma_f32_16x16x32_bf16 v[108:111], a[68:71], a[28:31], v[108:111]\n"
"ds_read_b64_tr_b16 a[178:179], v201 offset:1536\n"
"v_mfma_f32_16x16x32_bf16 v[140:143], a[64:67], a[32:35], v[140:143]\n"
"buffer_load_dwordx4 v198, s[52:55], s75 offen offset:2048 nt lds\n"
"v_mfma_f32_16x16x32_bf16 v[140:143], a[68:71], a[36:39], v[140:143]\n"
"ds_read_b64_tr_b16 a[180:181], v201 offset:17408\n"
"v_mfma_f32_16x16x32_bf16 v[16:19], a[72:75], a[0:3], v[16:19]\n"
"ds_read_b64_tr_b16 a[182:183], v201 offset:17920\n"
"v_mfma_f32_16x16x32_bf16 v[16:19], a[76:79], a[4:7], v[16:19]\n"
"ds_read_b64_tr_b16 a[184:185], v202 offset:1024\n"
"v_mfma_f32_16x16x32_bf16 v[48:51], a[72:75], a[8:11], v[48:51]\n"
"ds_read_b64_tr_b16 a[186:187], v202 offset:1536\n"
"v_mfma_f32_16x16x32_bf16 v[48:51], a[76:79], a[12:15], v[48:51]\n"
"s_add_u32 m0, s78, 0x380\n"
"ds_read_b64_tr_b16 a[188:189], v202 offset:17408\n"
"v_mfma_f32_16x16x32_bf16 v[80:83], a[72:75], a[16:19], v[80:83]\n"
"buffer_load_dwordx4 v198, s[52:55], s75 offen offset:128 nt lds\n"
"v_mfma_f32_16x16x32_bf16 v[80:83], a[76:79], a[20:23], v[80:83]\n"
"ds_read_b64_tr_b16 a[190:191], v202 offset:17920\n"
"v_mfma_f32_16x16x32_bf16 v[112:115], a[72:75], a[24:27], v[112:115]\n"
"ds_read_b64_tr_b16 a[192:193], v203 offset:1024\n"
"v_mfma_f32_16x16x32_bf16 v[112:115], a[76:79], a[28:31], v[112:115]\n"
"ds_read_b64_tr_b16 a[194:195], v203 offset:1536\n"
"v_mfma_f32_16x16x32_bf16 v[144:147], a[72:75], a[32:35], v[144:147]\n"
"ds_read_b64_tr_b16 a[196:197], v203 offset:17408\n"
"v_mfma_f32_16x16x32_bf16 v[144:147], a[76:79], a[36:39], v[144:147]\n"
"ds_read_b64_tr_b16 a[198:199], v203 offset:17920\n"
"v_mfma_f32_16x16x32_bf16 v[20:23], a[80:83], a[0:3], v[20:23]\n"
"ds_read_b64_tr_b16 a[200:201], v204 offset:1024\n"
"v_mfma_f32_16x16x32_bf16 v[20:23], a[84:87], a[4:7], v[20:23]\n"
"ds_read_b64_tr_b16 a[202:203], v204 offset:1536\n"
"v_mfma_f32_16x16x32_bf16 v[52:55], a[80:83], a[8:11], v[52:55]\n"
"ds_read_b64_tr_b16 a[204:205], v204 offset:17408\n"
"ds_read_b64_tr_b16 a[206:207], v204 offset:17920\n"
"v_mfma_f32_16x16x32_bf16 v[52:55], a[84:87], a[12:15], v[52:55]\n"
"buffer_load_dwordx4 v198, s[52:55], s75 offen offset:2176 nt lds\n"
"v_add_u32 v205, 0xd000, v205\n"
"v_mfma_f32_16x16x32_bf16 v[84:87], a[80:83], a[16:19], v[84:87]\n"
"v_add_u32 v206, 0xd000, v206\n"
"v_add_u32 v201, 0xd000, v201\n"
"v_mfma_f32_16x16x32_bf16 v[84:87], a[84:87], a[20:23], v[84:87]\n"
"s_waitcnt lgkmcnt(0)\n"
"v_add_u32 v202, 0xd000, v202\n"
"v_add_u32 v203, 0xd000, v203\n"
"v_mfma_f32_16x16x32_bf16 v[116:119], a[80:83], a[24:27], v[116:119]\n"
"s_barrier\n"
"v_add_u32 v204, 0xd000, v204\n"
"v_mfma_f32_16x16x32_bf16 v[116:119], a[84:87], a[28:31], v[116:119]\n"
"v_mfma_f32_16x16x32_bf16 v[148:151], a[80:83], a[32:35], v[148:151]\n"
"s_add_u32 m0, s78, 0x4000\n"
"v_mfma_f32_16x16x32_bf16 v[148:151], a[84:87], a[36:39], v[148:151]\n"
"buffer_load_dwordx4 v198, s[56:59], s75 offen offset:0 nt lds\n"
"v_mfma_f32_16x16x32_bf16 v[24:27], a[88:91], a[0:3], v[24:27]\n"
"v_mfma_f32_16x16x32_bf16 v[24:27], a[92:95], a[4:7], v[24:27]\n"
"v_mfma_f32_16x16x32_bf16 v[56:59], a[88:91], a[8:11], v[56:59]\n"
"v_mfma_f32_16x16x32_bf16 v[56:59], a[92:95], a[12:15], v[56:59]\n"
"v_mfma_f32_16x16x32_bf16 v[88:91], a[88:91], a[16:19], v[88:91]\n"
"v_mfma_f32_16x16x32_bf16 v[88:91], a[92:95], a[20:23], v[88:91]\n"
"buffer_load_dwordx4 v198, s[56:59], s75 offen offset:2048 nt lds\n"
"v_mfma_f32_16x16x32_bf16 v[120:123], a[88:91], a[24:27], v[120:123]\n"
"v_mfma_f32_16x16x32_bf16 v[120:123], a[92:95], a[28:31], v[120:123]\n"
"v_mfma_f32_16x16x32_bf16 v[152:155], a[88:91], a[32:35], v[152:155]\n"
"v_mfma_f32_16x16x32_bf16 v[152:155], a[92:95], a[36:39], v[152:155]\n"
"v_mfma_f32_16x16x32_bf16 v[28:31], a[96:99], a[0:3], v[28:31]\n"
"s_add_u32 m0, s78, 0x4380\n"
"v_mfma_f32_16x16x32_bf16 v[28:31], a[100:103], a[4:7], v[28:31]\n"
"buffer_load_dwordx4 v198, s[56:59], s75 offen offset:128 nt lds\n"
"v_mfma_f32_16x16x32_bf16 v[60:63], a[96:99], a[8:11], v[60:63]\n"
"v_mfma_f32_16x16x32_bf16 v[60:63], a[100:103], a[12:15], v[60:63]\n"
"v_mfma_f32_16x16x32_bf16 v[92:95], a[96:99], a[16:19], v[92:95]\n"
"v_mfma_f32_16x16x32_bf16 v[92:95], a[100:103], a[20:23], v[92:95]\n"
"v_mfma_f32_16x16x32_bf16 v[124:127], a[96:99], a[24:27], v[124:127]\n"
"v_mfma_f32_16x16x32_bf16 v[124:127], a[100:103], a[28:31], v[124:127]\n"
"v_mfma_f32_16x16x32_bf16 v[156:159], a[96:99], a[32:35], v[156:159]\n"
"buffer_load_dwordx4 v198, s[56:59], s75 offen offset:2176 nt lds\n"
"v_mfma_f32_16x16x32_bf16 v[156:159], a[100:103], a[36:39], v[156:159]\n"
"v_mfma_f32_16x16x32_bf16 v[0:3], a[144:147], a[104:107], v[0:3]\n"
"s_add_u32 s75, s75, 0x4000\n"
"s_add_u32 m0, s78, 0x8000\n"
"v_mfma_f32_16x16x32_bf16 v[0:3], a[148:151], a[108:111], v[0:3]\n"
"buffer_load_dwordx4 v198, s[52:55], s75 offen offset:0 nt lds\n"
"v_mfma_f32_16x16x32_bf16 v[4:7], a[152:155], a[104:107], v[4:7]\n"
"s_waitcnt vmcnt(9)\n"
"s_barrier\n"
"ds_read_b128 a[0:3], v205 offset:0\n"
"v_mfma_f32_16x16x32_bf16 v[4:7], a[156:159], a[108:111], v[4:7]\n"
"ds_read_b64_tr_b16 a[40:41], v201 offset:0\n"
"v_mfma_f32_16x16x32_bf16 v[8:11], a[160:163], a[104:107], v[8:11]\n"
"ds_read_b64_tr_b16 a[42:43], v201 offset:512\n"
"v_mfma_f32_16x16x32_bf16 v[8:11], a[164:167], a[108:111], v[8:11]\n"
"ds_read_b128 a[4:7], v206 offset:0\n"
"v_mfma_f32_16x16x32_bf16 v[12:15], a[168:171], a[104:107], v[12:15]\n"
"ds_read_b64_tr_b16 a[44:45], v201 offset:16384\n"
"v_mfma_f32_16x16x32_bf16 v[12:15], a[172:175], a[108:111], v[12:15]\n"
"ds_read_b64_tr_b16 a[46:47], v201 offset:16896\n"
"v_mfma_f32_16x16x32_bf16 v[16:19], a[176:179], a[104:107], v[16:19]\n"
"ds_read_b64_tr_b16 a[48:49], v202 offset:0\n"
"v_mfma_f32_16x16x32_bf16 v[16:19], a[180:183], a[108:111], v[16:19]\n"
"ds_read_b64_tr_b16 a[50:51], v202 offset:512\n"
"v_mfma_f32_16x16x32_bf16 v[20:23], a[184:187], a[104:107], v[20:23]\n"
"ds_read_b64_tr_b16 a[52:53], v202 offset:16384\n"
"v_mfma_f32_16x16x32_bf16 v[20:23], a[188:191], a[108:111], v[20:23]\n"
"ds_read_b64_tr_b16 a[54:55], v202 offset:16896\n"
"v_mfma_f32_16x16x32_bf16 v[24:27], a[192:195], a[104:107], v[24:27]\n"
"ds_read_b64_tr_b16 a[56:57], v203 offset:0\n"
"v_mfma_f32_16x16x32_bf16 v[24:27], a[196:199], a[108:111], v[24:27]\n"
"ds_read_b64_tr_b16 a[58:59], v203 offset:512\n"
"v_mfma_f32_16x16x32_bf16 v[28:31], a[200:203], a[104:107], v[28:31]\n"
"ds_read_b64_tr_b16 a[60:61], v203 offset:16384\n"
"v_mfma_f32_16x16x32_bf16 v[28:31], a[204:207], a[108:111], v[28:31]\n"
"ds_read_b64_tr_b16 a[62:63], v203 offset:16896\n"
"v_mfma_f32_16x16x32_bf16 v[32:35], a[144:147], a[112:115], v[32:35]\n"
"ds_read_b64_tr_b16 a[64:65], v204 offset:0\n"
"v_mfma_f32_16x16x32_bf16 v[32:35], a[148:151], a[116:119], v[32:35]\n"
"ds_read_b64_tr_b16 a[66:67], v204 offset:512\n"
"v_mfma_f32_16x16x32_bf16 v[36:39], a[152:155], a[112:115], v[36:39]\n"
"ds_read_b64_tr_b16 a[68:69], v204 offset:16384\n"
"v_mfma_f32_16x16x32_bf16 v[36:39], a[156:159], a[116:119], v[36:39]\n"
"ds_read_b64_tr_b16 a[70:71], v204 offset:16896\n"
"v_mfma_f32_16x16x32_bf16 v[40:43], a[160:163], a[112:115], v[40:43]\n"
"ds_read_b64_tr_b16 a[72:73], v201 offset:1024\n"
"v_mfma_f32_16x16x32_bf16 v[40:43], a[164:167], a[116:119], v[40:43]\n"
"ds_read_b64_tr_b16 a[74:75], v201 offset:1536\n"
"v_mfma_f32_16x16x32_bf16 v[44:47], a[168:171], a[112:115], v[44:47]\n"
"ds_read_b64_tr_b16 a[76:77], v201 offset:17408\n"
"v_mfma_f32_16x16x32_bf16 v[44:47], a[172:175], a[116:119], v[44:47]\n"
"ds_read_b64_tr_b16 a[78:79], v201 offset:17920\n"
"v_mfma_f32_16x16x32_bf16 v[48:51], a[176:179], a[112:115], v[48:51]\n"
"ds_read_b64_tr_b16 a[80:81], v202 offset:1024\n"
"v_mfma_f32_16x16x32_bf16 v[48:51], a[180:183], a[116:119], v[48:51]\n"
"ds_read_b64_tr_b16 a[82:83], v202 offset:1536\n"
"v_mfma_f32_16x16x32_bf16 v[52:55], a[184:187], a[112:115], v[52:55]\n"
"ds_read_b64_tr_b16 a[84:85], v202 offset:17408\n"
"v_mfma_f32_16x16x32_bf16 v[52:55], a[188:191], a[116:119], v[52:55]\n"
"ds_read_b64_tr_b16 a[86:87], v202 offset:17920\n"
"v_mfma_f32_16x16x32_bf16 v[56:59], a[192:195], a[112:115], v[56:59]\n"
"ds_read_b64_tr_b16 a[88:89], v203 offset:1024\n"
"v_mfma_f32_16x16x32_bf16 v[56:59], a[196:199], a[116:119], v[56:59]\n"
"ds_read_b64_tr_b16 a[90:91], v203 offset:1536\n"
"v_mfma_f32_16x16x32_bf16 v[60:63], a[200:203], a[112:115], v[60:63]\n"
"ds_read_b64_tr_b16 a[92:93], v203 offset:17408\n"
"v_mfma_f32_16x16x32_bf16 v[60:63], a[204:207], a[116:119], v[60:63]\n"
"s_waitcnt lgkmcnt(6)\n"
"ds_read_b64_tr_b16 a[94:95], v203 offset:17920\n"
"v_mfma_f32_16x16x32_bf16 v[0:3], a[40:43], a[0:3], v[0:3]\n"
"ds_read_b64_tr_b16 a[96:97], v204 offset:1024\n"
"v_mfma_f32_16x16x32_bf16 v[0:3], a[44:47], a[4:7], v[0:3]\n"
"ds_read_b64_tr_b16 a[98:99], v204 offset:1536\n"
"v_mfma_f32_16x16x32_bf16 v[4:7], a[48:51], a[0:3], v[4:7]\n"
"ds_read_b64_tr_b16 a[100:101], v204 offset:17408\n"
"v_mfma_f32_16x16x32_bf16 v[4:7], a[52:55], a[4:7], v[4:7]\n"
"ds_read_b64_tr_b16 a[102:103], v204 offset:17920\n"
"s_nop 2\n"
"v_mul_f32 v0, s84, v0\n"
"v_mfma_f32_16x16x32_bf16 v[8:11], a[56:59], a[0:3], v[8:11]\n"
"ds_read_b128 a[8:11], v205 offset:2048\n"
"v_mul_f32 v1, s84, v1\n"
"v_mfma_f32_16x16x32_bf16 v[8:11], a[60:63], a[4:7], v[8:11]\n"
"ds_read_b128 a[12:15], v206 offset:2048\n"
"v_mul_f32 v2, s84, v2\n"
"v_mfma_f32_16x16x32_bf16 v[12:15], a[64:67], a[0:3], v[12:15]\n"
"ds_read_b128 a[16:19], v205 offset:4096\n"
"v_mul_f32 v3, s84, v3\n"
"v_mfma_f32_16x16x32_bf16 v[12:15], a[68:71], a[4:7], v[12:15]\n"
"ds_read_b128 a[20:23], v206 offset:4096\n"
"v_cvt_pk_bf16_f32 v0, v0, v1\n"
"v_mfma_f32_16x16x32_bf16 v[16:19], a[72:75], a[0:3], v[16:19]\n"
"ds_read_b128 a[24:27], v205 offset:6144\n"
"v_mul_f32 v4, s84, v4\n"
"v_mfma_f32_16x16x32_bf16 v[16:19], a[76:79], a[4:7], v[16:19]\n"
"s_waitcnt lgkmcnt(6)\n"
"ds_read_b128 a[28:31], v206 offset:6144\n"
"v_mul_f32 v5, s84, v5\n"
"v_mfma_f32_16x16x32_bf16 v[20:23], a[80:83], a[0:3], v[20:23]\n"
"ds_read_b128 a[32:35], v205 offset:8192\n"
"v_mul_f32 v6, s84, v6\n"
"v_mfma_f32_16x16x32_bf16 v[20:23], a[84:87], a[4:7], v[20:23]\n"
"ds_read_b128 a[36:39], v206 offset:8192\n"
"v_mul_f32 v7, s84, v7\n"
"v_mfma_f32_16x16x32_bf16 v[24:27], a[88:91], a[0:3], v[24:27]\n"
"buffer_load_dwordx4 v198, s[52:55], s75 offen offset:2048 nt lds\n"
"s_add_u32 m0, s78, 0x8380\n"
"v_mfma_f32_16x16x32_bf16 v[24:27], a[92:95], a[4:7], v[24:27]\n"
"buffer_load_dwordx4 v198, s[52:55], s75 offen offset:128 nt lds\n"
"v_cvt_pk_bf16_f32 v1, v2, v3\n"
"v_mfma_f32_16x16x32_bf16 v[28:31], a[96:99], a[0:3], v[28:31]\n"
"s_waitcnt lgkmcnt(4)\n"
"buffer_load_dwordx4 v198, s[52:55], s75 offen offset:2176 nt lds\n"
"s_add_u32 m0, s78, 0xc000\n"
"v_mfma_f32_16x16x32_bf16 v[28:31], a[100:103], a[4:7], v[28:31]\n"
"v_cvt_pk_bf16_f32 v2, v4, v5\n"
"v_cvt_pk_bf16_f32 v3, v6, v7\n"
"v_mfma_f32_16x16x32_bf16 v[64:67], a[144:147], a[120:123], v[64:67]\n"
"buffer_load_dwordx4 v198, s[56:59], s75 offen offset:0 nt lds\n"
"v_mul_f32 v8, s84, v8\n"
"v_mfma_f32_16x16x32_bf16 v[64:67], a[148:151], a[124:127], v[64:67]\n"
"v_mul_f32 v9, s84, v9\n"
"v_mul_f32 v10, s84, v10\n"
"v_mfma_f32_16x16x32_bf16 v[68:71], a[152:155], a[120:123], v[68:71]\n"
"v_mul_f32 v11, s84, v11\n"
"v_mul_f32 v12, s84, v12\n"
"v_mfma_f32_16x16x32_bf16 v[68:71], a[156:159], a[124:127], v[68:71]\n"
"v_mul_f32 v13, s84, v13\n"
"v_mul_f32 v14, s84, v14\n"
"v_mfma_f32_16x16x32_bf16 v[72:75], a[160:163], a[120:123], v[72:75]\n"
"v_mul_f32 v15, s84, v15\n"
"v_cvt_pk_bf16_f32 v4, v8, v9\n"
"v_mfma_f32_16x16x32_bf16 v[72:75], a[164:167], a[124:127], v[72:75]\n"
"v_permlane16_swap_b32 v0, v2\n"
"v_mfma_f32_16x16x32_bf16 v[76:79], a[168:171], a[120:123], v[76:79]\n"
"v_permlane16_swap_b32 v1, v3\n"
"v_mfma_f32_16x16x32_bf16 v[76:79], a[172:175], a[124:127], v[76:79]\n"
"v_cvt_pk_bf16_f32 v5, v10, v11\n"
"v_cvt_pk_bf16_f32 v6, v12, v13\n"
"v_mfma_f32_16x16x32_bf16 v[80:83], a[176:179], a[120:123], v[80:83]\n"
"v_cvt_pk_bf16_f32 v7, v14, v15\n"
"v_mul_f32 v16, s84, v16\n"
"v_mfma_f32_16x16x32_bf16 v[80:83], a[180:183], a[124:127], v[80:83]\n"
"v_mul_f32 v17, s84, v17\n"
"v_mul_f32 v18, s84, v18\n"
"v_mfma_f32_16x16x32_bf16 v[84:87], a[184:187], a[120:123], v[84:87]\n"
"v_mul_f32 v19, s84, v19\n"
"v_mul_f32 v20, s84, v20\n"
"v_mfma_f32_16x16x32_bf16 v[84:87], a[188:191], a[124:127], v[84:87]\n"
"v_mul_f32 v21, s84, v21\n"
"v_mul_f32 v22, s84, v22\n"
"v_mfma_f32_16x16x32_bf16 v[88:91], a[192:195], a[120:123], v[88:91]\n"
"buffer_load_dwordx4 v198, s[56:59], s75 offen offset:2048 nt lds\n"
"s_add_u32 m0, s78, 0xc380\n"
"v_mfma_f32_16x16x32_bf16 v[88:91], a[196:199], a[124:127], v[88:91]\n"
"v_mul_f32 v23, s84, v23\n"
"v_cvt_pk_bf16_f32 v8, v16, v17\n"
"v_mfma_f32_16x16x32_bf16 v[92:95], a[200:203], a[120:123], v[92:95]\n"
"v_permlane16_swap_b32 v4, v6\n"
"v_mfma_f32_16x16x32_bf16 v[92:95], a[204:207], a[124:127], v[92:95]\n"
"v_permlane16_swap_b32 v5, v7\n"
"v_mfma_f32_16x16x32_bf16 v[32:35], a[40:43], a[8:11], v[32:35]\n"
"v_cvt_pk_bf16_f32 v9, v18, v19\n"
"v_cvt_pk_bf16_f32 v10, v20, v21\n"
"v_mfma_f32_16x16x32_bf16 v[32:35], a[44:47], a[12:15], v[32:35]\n"
"v_cvt_pk_bf16_f32 v11, v22, v23\n"
"v_mul_f32 v24, s84, v24\n"
"v_mfma_f32_16x16x32_bf16 v[36:39], a[48:51], a[8:11], v[36:39]\n"
"v_mul_f32 v25, s84, v25\n"
"v_mul_f32 v26, s84, v26\n"
"v_mfma_f32_16x16x32_bf16 v[36:39], a[52:55], a[12:15], v[36:39]\n"
"v_mul_f32 v27, s84, v27\n"
"v_mul_f32 v28, s84, v28\n"
"v_mfma_f32_16x16x32_bf16 v[40:43], a[56:59], a[8:11], v[40:43]\n"
"v_mul_f32 v29, s84, v29\n"
"v_mul_f32 v30, s84, v30\n"
"v_mfma_f32_16x16x32_bf16 v[40:43], a[60:63], a[12:15], v[40:43]\n"
"v_mul_f32 v31, s84, v31\n"
"v_cvt_pk_bf16_f32 v12, v24, v25\n"
"v_mfma_f32_16x16x32_bf16 v[44:47], a[64:67], a[8:11], v[44:47]\n"
"v_permlane16_swap_b32 v8, v10\n"
"v_mfma_f32_16x16x32_bf16 v[44:47], a[68:71], a[12:15], v[44:47]\n"
"v_permlane16_swap_b32 v9, v11\n"
"v_mfma_f32_16x16x32_bf16 v[48:51], a[72:75], a[8:11], v[48:51]\n"
"buffer_load_dwordx4 v198, s[56:59], s75 offen offset:128 nt lds\n"
"v_cvt_pk_bf16_f32 v13, v26, v27\n"
"v_mfma_f32_16x16x32_bf16 v[48:51], a[76:79], a[12:15], v[48:51]\n"
"v_cvt_pk_bf16_f32 v14, v28, v29\n"
"v_cvt_pk_bf16_f32 v15, v30, v31\n"
"v_mfma_f32_16x16x32_bf16 v[52:55], a[80:83], a[8:11], v[52:55]\n"
"v_permlane16_swap_b32 v12, v14\n"
"v_mfma_f32_16x16x32_bf16 v[52:55], a[84:87], a[12:15], v[52:55]\n"
"v_permlane16_swap_b32 v13, v15\n"
"v_mfma_f32_16x16x32_bf16 v[56:59], a[88:91], a[8:11], v[56:59]\n"
"v_mul_f32 v32, s84, v32\n"
"v_mul_f32 v33, s84, v33\n"
"v_mfma_f32_16x16x32_bf16 v[56:59], a[92:95], a[12:15], v[56:59]\n"
"v_mul_f32 v34, s84, v34\n"
"v_mul_f32 v35, s84, v35\n"
"v_mfma_f32_16x16x32_bf16 v[60:63], a[96:99], a[8:11], v[60:63]\n"
"v_mul_f32 v36, s84, v36\n"
"v_mul_f32 v37, s84, v37\n"
"v_mfma_f32_16x16x32_bf16 v[60:63], a[100:103], a[12:15], v[60:63]\n"
"v_mul_f32 v38, s84, v38\n"
"v_mul_f32 v39, s84, v39\n"
"v_mfma_f32_16x16x32_bf16 v[96:99], a[144:147], a[128:131], v[96:99]\n"
"v_cvt_pk_bf16_f32 v16, v32, v33\n"
"v_cvt_pk_bf16_f32 v17, v34, v35\n"
"v_mfma_f32_16x16x32_bf16 v[96:99], a[148:151], a[132:135], v[96:99]\n"
"v_cvt_pk_bf16_f32 v18, v36, v37\n"
"v_cvt_pk_bf16_f32 v19, v38, v39\n"
"v_mfma_f32_16x16x32_bf16 v[100:103], a[152:155], a[128:131], v[100:103]\n"
"v_mul_f32 v40, s84, v40\n"
"v_mul_f32 v41, s84, v41\n"
"v_mfma_f32_16x16x32_bf16 v[100:103], a[156:159], a[132:135], v[100:103]\n"
"v_mul_f32 v42, s84, v42\n"
"v_mul_f32 v43, s84, v43\n"
"v_mfma_f32_16x16x32_bf16 v[104:107], a[160:163], a[128:131], v[104:107]\n"
"buffer_load_dwordx4 v198, s[56:59], s75 offen offset:2176 nt lds\n"
"s_waitcnt lgkmcnt(0)\n"
"v_mfma_f32_16x16x32_bf16 v[104:107], a[164:167], a[132:135], v[104:107]\n"
"s_add_u32 s75, s75, 0x4000\n"
"v_mul_f32 v44, s84, v44\n"
"v_mul_f32 v45, s84, v45\n"
"v_mfma_f32_16x16x32_bf16 v[108:111], a[168:171], a[128:131], v[108:111]\n"
"v_mul_f32 v46, s84, v46\n"
"v_mul_f32 v47, s84, v47\n"
"v_mfma_f32_16x16x32_bf16 v[108:111], a[172:175], a[132:135], v[108:111]\n"
"v_permlane16_swap_b32 v16, v18\n"
"v_mfma_f32_16x16x32_bf16 v[112:115], a[176:179], a[128:131], v[112:115]\n"
"v_permlane16_swap_b32 v17, v19\n"
"v_mfma_f32_16x16x32_bf16 v[112:115], a[180:183], a[132:135], v[112:115]\n"
"v_cvt_pk_bf16_f32 v20, v40, v41\n"
"v_cvt_pk_bf16_f32 v21, v42, v43\n"
"v_mfma_f32_16x16x32_bf16 v[116:119], a[184:187], a[128:131], v[116:119]\n"
"v_cvt_pk_bf16_f32 v22, v44, v45\n"
"v_cvt_pk_bf16_f32 v23, v46, v47\n"
"v_mfma_f32_16x16x32_bf16 v[116:119], a[188:191], a[132:135], v[116:119]\n"
"v_mul_f32 v48, s84, v48\n"
"v_mul_f32 v49, s84, v49\n"
"v_mfma_f32_16x16x32_bf16 v[120:123], a[192:195], a[128:131], v[120:123]\n"
"v_mul_f32 v50, s84, v50\n"
"v_mul_f32 v51, s84, v51\n"
"v_mfma_f32_16x16x32_bf16 v[120:123], a[196:199], a[132:135], v[120:123]\n"
"v_mul_f32 v52, s84, v52\n"
"v_mul_f32 v53, s84, v53\n"
"v_mfma_f32_16x16x32_bf16 v[124:127], a[200:203], a[128:131], v[124:127]\n"
"v_mul_f32 v54, s84, v54\n"
"v_mul_f32 v55, s84, v55\n"
"v_mfma_f32_16x16x32_bf16 v[124:127], a[204:207], a[132:135], v[124:127]\n"
"v_permlane16_swap_b32 v20, v22\n"
"v_mfma_f32_16x16x32_bf16 v[64:67], a[40:43], a[16:19], v[64:67]\n"
"v_permlane16_swap_b32 v21, v23\n"
"v_mfma_f32_16x16x32_bf16 v[64:67], a[44:47], a[20:23], v[64:67]\n"
"v_cvt_pk_bf16_f32 v24, v48, v49\n"
"v_cvt_pk_bf16_f32 v25, v50, v51\n"
"v_mfma_f32_16x16x32_bf16 v[68:71], a[48:51], a[16:19], v[68:71]\n"
"v_cvt_pk_bf16_f32 v26, v52, v53\n"
"v_cvt_pk_bf16_f32 v27, v54, v55\n"
"v_mfma_f32_16x16x32_bf16 v[68:71], a[52:55], a[20:23], v[68:71]\n"
"v_mul_f32 v56, s84, v56\n"
"v_mul_f32 v57, s84, v57\n"
"v_mfma_f32_16x16x32_bf16 v[72:75], a[56:59], a[16:19], v[72:75]\n"
"v_mul_f32 v58, s84, v58\n"
"v_mul_f32 v59, s84, v59\n"
"v_mfma_f32_16x16x32_bf16 v[72:75], a[60:63], a[20:23], v[72:75]\n"
"v_mul_f32 v60, s84, v60\n"
"v_mul_f32 v61, s84, v61\n"
"v_mfma_f32_16x16x32_bf16 v[76:79], a[64:67], a[16:19], v[76:79]\n"
"v_mul_f32 v62, s84, v62\n"
"v_mul_f32 v63, s84, v63\n"
"v_mfma_f32_16x16x32_bf16 v[76:79], a[68:71], a[20:23], v[76:79]\n"
"v_permlane16_swap_b32 v24, v26\n"
"v_mfma_f32_16x16x32_bf16 v[80:83], a[72:75], a[16:19], v[80:83]\n"
"v_permlane16_swap_b32 v25, v27\n"
"v_mfma_f32_16x16x32_bf16 v[80:83], a[76:79], a[20:23], v[80:83]\n"
"v_cvt_pk_bf16_f32 v28, v56, v57\n"
"v_cvt_pk_bf16_f32 v29, v58, v59\n"
"v_mfma_f32_16x16x32_bf16 v[84:87], a[80:83], a[16:19], v[84:87]\n"
"v_cvt_pk_bf16_f32 v30, v60, v61\n"
"v_cvt_pk_bf16_f32 v31, v62, v63\n"
"v_mfma_f32_16x16x32_bf16 v[84:87], a[84:87], a[20:23], v[84:87]\n"
"v_permlane16_swap_b32 v28, v30\n"
"v_mfma_f32_16x16x32_bf16 v[88:91], a[88:91], a[16:19], v[88:91]\n"
"v_permlane16_swap_b32 v29, v31\n"
"v_mfma_f32_16x16x32_bf16 v[88:91], a[92:95], a[20:23], v[88:91]\n"
"v_mul_f32 v64, s84, v64\n"
"v_mul_f32 v65, s84, v65\n"
"v_mfma_f32_16x16x32_bf16 v[92:95], a[96:99], a[16:19], v[92:95]\n"
"v_mul_f32 v66, s84, v66\n"
"v_mul_f32 v67, s84, v67\n"
"v_mfma_f32_16x16x32_bf16 v[92:95], a[100:103], a[20:23], v[92:95]\n"
"v_mul_f32 v68, s84, v68\n"
"v_mul_f32 v69, s84, v69\n"
"v_mfma_f32_16x16x32_bf16 v[128:131], a[144:147], a[136:139], v[128:131]\n"
"v_mul_f32 v70, s84, v70\n"
"v_mul_f32 v71, s84, v71\n"
"v_mfma_f32_16x16x32_bf16 v[128:131], a[148:151], a[140:143], v[128:131]\n"
"v_cvt_pk_bf16_f32 v32, v64, v65\n"
"v_cvt_pk_bf16_f32 v33, v66, v67\n"
"v_mfma_f32_16x16x32_bf16 v[132:135], a[152:155], a[136:139], v[132:135]\n"
"v_cvt_pk_bf16_f32 v34, v68, v69\n"
"v_cvt_pk_bf16_f32 v35, v70, v71\n"
"v_mfma_f32_16x16x32_bf16 v[132:135], a[156:159], a[140:143], v[132:135]\n"
"v_mul_f32 v72, s84, v72\n"
"v_mul_f32 v73, s84, v73\n"
"v_mfma_f32_16x16x32_bf16 v[136:139], a[160:163], a[136:139], v[136:139]\n"
"v_mul_f32 v74, s84, v74\n"
"v_mul_f32 v75, s84, v75\n"
"v_mfma_f32_16x16x32_bf16 v[136:139], a[164:167], a[140:143], v[136:139]\n"
"v_mul_f32 v76, s84, v76\n"
"v_mul_f32 v77, s84, v77\n"
"v_mfma_f32_16x16x32_bf16 v[140:143], a[168:171], a[136:139], v[140:143]\n"
"v_mul_f32 v78, s84, v78\n"
"v_mul_f32 v79, s84, v79\n"
"v_mfma_f32_16x16x32_bf16 v[140:143], a[172:175], a[140:143], v[140:143]\n"
"v_permlane16_swap_b32 v32, v34\n"
"v_mfma_f32_16x16x32_bf16 v[144:147], a[176:179], a[136:139], v[144:147]\n"
"v_permlane16_swap_b32 v33, v35\n"
"v_mfma_f32_16x16x32_bf16 v[144:147], a[180:183], a[140:143], v[144:147]\n"
"v_cvt_pk_bf16_f32 v36, v72, v73\n"
"v_cvt_pk_bf16_f32 v37, v74, v75\n"
"v_mfma_f32_16x16x32_bf16 v[148:151], a[184:187], a[136:139], v[148:151]\n"
"v_cvt_pk_bf16_f32 v38, v76, v77\n"
"v_cvt_pk_bf16_f32 v39, v78, v79\n"
"v_mfma_f32_16x16x32_bf16 v[148:151], a[188:191], a[140:143], v[148:151]\n"
"v_mul_f32 v80, s84, v80\n"
"v_mul_f32 v81, s84, v81\n"
"v_mfma_f32_16x16x32_bf16 v[152:155], a[192:195], a[136:139], v[152:155]\n"
"v_mul_f32 v82, s84, v82\n"
"v_mul_f32 v83, s84, v83\n"
"v_mfma_f32_16x16x32_bf16 v[152:155], a[196:199], a[140:143], v[152:155]\n"
"v_mul_f32 v84, s84, v84\n"
"v_mul_f32 v85, s84, v85\n"
"v_mfma_f32_16x16x32_bf16 v[156:159], a[200:203], a[136:139], v[156:159]\n"
"v_mul_f32 v86, s84, v86\n"
"v_mul_f32 v87, s84, v87\n"
"v_mfma_f32_16x16x32_bf16 v[156:159], a[204:207], a[140:143], v[156:159]\n"
"s_waitcnt vmcnt(12)\n"
"s_barrier\n"
"ds_read_b128 a[160:163], v186 offset:0\n"
"v_mfma_f32_16x16x32_bf16 v[96:99], a[40:43], a[24:27], v[96:99]\n"
"ds_read_b128 a[164:167], v187 offset:0\n"
"v_cvt_pk_bf16_f32 v40, v80, v81\n"
"v_mfma_f32_16x16x32_bf16 v[96:99], a[44:47], a[28:31], v[96:99]\n"
"ds_read_b128 a[168:171], v186 offset:1024\n"
"v_cvt_pk_bf16_f32 v41, v82, v83\n"
"v_mfma_f32_16x16x32_bf16 v[100:103], a[48:51], a[24:27], v[100:103]\n"
"ds_read_b128 a[172:175], v187 offset:1024\n"
"v_cvt_pk_bf16_f32 v42, v84, v85\n"
"v_mfma_f32_16x16x32_bf16 v[100:103], a[52:55], a[28:31], v[100:103]\n"
"ds_read_b128 a[176:179], v186 offset:4096\n"
"v_cvt_pk_bf16_f32 v43, v86, v87\n"
"v_mfma_f32_16x16x32_bf16 v[104:107], a[56:59], a[24:27], v[104:107]\n"
"ds_read_b128 a[180:183], v187 offset:4096\n"
"v_mul_f32 v88, s84, v88\n"
"v_mfma_f32_16x16x32_bf16 v[104:107], a[60:63], a[28:31], v[104:107]\n"
"ds_read_b128 a[184:187], v186 offset:5120\n"
"v_mul_f32 v89, s84, v89\n"
"v_mfma_f32_16x16x32_bf16 v[108:111], a[64:67], a[24:27], v[108:111]\n"
"ds_read_b128 a[188:191], v187 offset:5120\n"
"v_mul_f32 v90, s84, v90\n"
"v_mfma_f32_16x16x32_bf16 v[108:111], a[68:71], a[28:31], v[108:111]\n"
"ds_read_b128 a[192:195], v186 offset:8192\n"
"v_mul_f32 v91, s84, v91\n"
"v_mfma_f32_16x16x32_bf16 v[112:115], a[72:75], a[24:27], v[112:115]\n"
"ds_read_b128 a[196:199], v187 offset:8192\n"
"v_mul_f32 v92, s84, v92\n"
"v_mfma_f32_16x16x32_bf16 v[112:115], a[76:79], a[28:31], v[112:115]\n"
"ds_read_b128 a[200:203], v186 offset:9216\n"
"v_mul_f32 v93, s84, v93\n"
"v_mfma_f32_16x16x32_bf16 v[116:119], a[80:83], a[24:27], v[116:119]\n"
"ds_read_b128 a[204:207], v187 offset:9216\n"
"v_mul_f32 v94, s84, v94\n"
"v_mfma_f32_16x16x32_bf16 v[116:119], a[84:87], a[28:31], v[116:119]\n"
"ds_read_b128 a[208:211], v186 offset:12288\n"
"v_mul_f32 v95, s84, v95\n"
"v_mfma_f32_16x16x32_bf16 v[120:123], a[88:91], a[24:27], v[120:123]\n"
"ds_read_b128 a[212:215], v187 offset:12288\n"
"v_cvt_pk_bf16_f32 v44, v88, v89\n"
"v_mfma_f32_16x16x32_bf16 v[120:123], a[92:95], a[28:31], v[120:123]\n"
"ds_read_b128 a[216:219], v186 offset:13312\n"
"v_cvt_pk_bf16_f32 v45, v90, v91\n"
"v_mfma_f32_16x16x32_bf16 v[124:127], a[96:99], a[24:27], v[124:127]\n"
"ds_read_b128 a[220:223], v187 offset:13312\n"
"v_cvt_pk_bf16_f32 v46, v92, v93\n"
"v_mfma_f32_16x16x32_bf16 v[124:127], a[100:103], a[28:31], v[124:127]\n"
"v_permlane16_swap_b32 v36, v38\n"
"v_mfma_f32_16x16x32_bf16 v[128:131], a[40:43], a[32:35], v[128:131]\n"
"v_permlane16_swap_b32 v37, v39\n"
"v_mfma_f32_16x16x32_bf16 v[128:131], a[44:47], a[36:39], v[128:131]\n"
"v_permlane16_swap_b32 v40, v42\n"
"v_mfma_f32_16x16x32_bf16 v[132:135], a[48:51], a[32:35], v[132:135]\n"
"v_permlane16_swap_b32 v41, v43\n"
"v_mfma_f32_16x16x32_bf16 v[132:135], a[52:55], a[36:39], v[132:135]\n"
"v_cvt_pk_bf16_f32 v47, v94, v95\n"
"v_mul_f32 v96, s84, v96\n"
"v_mfma_f32_16x16x32_bf16 v[136:139], a[56:59], a[32:35], v[136:139]\n"
"v_permlane16_swap_b32 v44, v46\n"
"v_mfma_f32_16x16x32_bf16 v[136:139], a[60:63], a[36:39], v[136:139]\n"
"v_permlane16_swap_b32 v45, v47\n"
"v_mfma_f32_16x16x32_bf16 v[140:143], a[64:67], a[32:35], v[140:143]\n"
"v_mul_f32 v97, s84, v97\n"
"v_mul_f32 v98, s84, v98\n"
"v_mfma_f32_16x16x32_bf16 v[140:143], a[68:71], a[36:39], v[140:143]\n"
"v_mul_f32 v99, s84, v99\n"
"v_mul_f32 v100, s84, v100\n"
"v_mfma_f32_16x16x32_bf16 v[144:147], a[72:75], a[32:35], v[144:147]\n"
"v_mul_f32 v101, s84, v101\n"
"v_mul_f32 v102, s84, v102\n"
"v_mfma_f32_16x16x32_bf16 v[144:147], a[76:79], a[36:39], v[144:147]\n"
"v_mul_f32 v103, s84, v103\n"
"v_cvt_pk_bf16_f32 v48, v96, v97\n"
"v_mfma_f32_16x16x32_bf16 v[148:151], a[80:83], a[32:35], v[148:151]\n"
"v_cvt_pk_bf16_f32 v49, v98, v99\n"
"v_cvt_pk_bf16_f32 v50, v100, v101\n"
"v_mfma_f32_16x16x32_bf16 v[148:151], a[84:87], a[36:39], v[148:151]\n"
"v_cvt_pk_bf16_f32 v51, v102, v103\n"
"v_mul_f32 v104, s84, v104\n"
"v_mfma_f32_16x16x32_bf16 v[152:155], a[88:91], a[32:35], v[152:155]\n"
"v_mul_f32 v105, s84, v105\n"
"v_mul_f32 v106, s84, v106\n"
"v_mfma_f32_16x16x32_bf16 v[152:155], a[92:95], a[36:39], v[152:155]\n"
"v_mul_f32 v107, s84, v107\n"
"v_mul_f32 v108, s84, v108\n"
"v_mfma_f32_16x16x32_bf16 v[156:159], a[96:99], a[32:35], v[156:159]\n"
"v_mul_f32 v109, s84, v109\n"
"v_mul_f32 v110, s84, v110\n"
"v_mfma_f32_16x16x32_bf16 v[156:159], a[100:103], a[36:39], v[156:159]\n"
"v_mul_f32 v111, s84, v111\n"
"v_cvt_pk_bf16_f32 v52, v104, v105\n"
"v_mfma_f32_32x32x16_bf16 a[0:15], v[216:219], v[216:219], 0\n"
"v_permlane16_swap_b32 v48, v50\n"
"v_permlane16_swap_b32 v49, v51\n"
"v_cvt_pk_bf16_f32 v53, v106, v107\n"
"v_cvt_pk_bf16_f32 v54, v108, v109\n"
"v_mfma_f32_32x32x16_bf16 a[16:31], v[216:219], v[216:219], 0\n"
"s_waitcnt lgkmcnt(0)\n"
"v_cvt_pk_bf16_f32 v55, v110, v111\n"
"v_mul_f32 v112, s84, v112\n"
"v_mul_f32 v113, s84, v113\n"
"v_mul_f32 v114, s84, v114\n"
"v_mul_f32 v115, s84, v115\n"
"v_mul_f32 v116, s84, v116\n"
"v_mfma_f32_16x16x32_bf16 v[220:223], a[160:163], v[0:3], 0\n"
"v_mul_f32 v117, s84, v117\n"
"v_mul_f32 v118, s84, v118\n"
"v_mfma_f32_16x16x32_bf16 v[220:223], a[164:167], v[4:7], v[220:223]\n"
"v_mul_f32 v119, s84, v119\n"
"v_cvt_pk_bf16_f32 v56, v112, v113\n"
"v_mfma_f32_16x16x32_bf16 v[220:223], a[168:171], v[8:11], v[220:223]\n"
"v_permlane16_swap_b32 v52, v54\n"
"v_mfma_f32_16x16x32_bf16 v[220:223], a[172:175], v[12:15], v[220:223]\n"
"v_permlane16_swap_b32 v53, v55\n"
"v_mfma_f32_32x32x16_bf16 a[32:47], v[216:219], v[216:219], 0\n"
"v_cvt_pk_bf16_f32 v57, v114, v115\n"
"v_cvt_pk_bf16_f32 v58, v116, v117\n"
"v_cvt_pk_bf16_f32 v59, v118, v119\n"
"v_mul_f32 v120, s84, v120\n"
"v_mul_f32 v121, s84, v121\n"
"v_mul_f32 v122, s84, v122\n"
"v_mfma_f32_32x32x16_bf16 a[48:63], v[216:219], v[216:219], 0\n"
"v_mul_f32 v123, s84, v123\n"
"v_mul_f32 v124, s84, v124\n"
"v_mul_f32 v125, s84, v125\n"
"v_mul_f32 v126, s84, v126\n"
"v_mul_f32 v127, s84, v127\n"
"v_cvt_pk_bf16_f32 v60, v120, v121\n"
"v_mfma_f32_16x16x32_bf16 v[224:227], a[160:163], v[16:19], 0\n"
"v_permlane16_swap_b32 v56, v58\n"
"v_mfma_f32_16x16x32_bf16 v[224:227], a[164:167], v[20:23], v[224:227]\n"
"v_permlane16_swap_b32 v57, v59\n"
"v_mfma_f32_16x16x32_bf16 v[224:227], a[168:171], v[24:27], v[224:227]\n"
"v_cvt_pk_bf16_f32 v61, v122, v123\n"
"v_cvt_pk_bf16_f32 v62, v124, v125\n"
"v_mfma_f32_16x16x32_bf16 v[224:227], a[172:175], v[28:31], v[224:227]\n"
"v_cvt_pk_bf16_f32 v63, v126, v127\n"
"v_mul_f32 v128, s84, v128\n"
"v_mfma_f32_32x32x16_bf16 a[64:79], v[216:219], v[216:219], 0\n"
"v_permlane16_swap_b32 v60, v62\n"
"v_permlane16_swap_b32 v61, v63\n"
"v_mul_f32 v129, s84, v129\n"
"v_mul_f32 v130, s84, v130\n"
"v_mfma_f32_32x32x16_bf16 a[80:95], v[216:219], v[216:219], 0\n"
"v_mul_f32 v131, s84, v131\n"
"v_mul_f32 v132, s84, v132\n"
"v_mul_f32 v133, s84, v133\n"
"v_mul_f32 v134, s84, v134\n"
"v_mul_f32 v135, s84, v135\n"
"v_cvt_pk_bf16_f32 v64, v128, v129\n"
"v_mfma_f32_16x16x32_bf16 v[228:231], a[160:163], v[32:35], 0\n"
"v_cvt_pk_bf16_f32 v65, v130, v131\n"
"v_cvt_pk_bf16_f32 v66, v132, v133\n"
"v_mfma_f32_16x16x32_bf16 v[228:231], a[164:167], v[36:39], v[228:231]\n"
"v_cvt_pk_bf16_f32 v67, v134, v135\n"
"v_mul_f32 v136, s84, v136\n"
"v_mfma_f32_16x16x32_bf16 v[228:231], a[168:171], v[40:43], v[228:231]\n"
"v_mul_f32 v137, s84, v137\n"
"v_mul_f32 v138, s84, v138\n"
"v_mfma_f32_16x16x32_bf16 v[228:231], a[172:175], v[44:47], v[228:231]\n"
"v_mul_f32 v139, s84, v139\n"
"v_mul_f32 v140, s84, v140\n"
"v_mfma_f32_32x32x16_bf16 a[96:111], v[216:219], v[216:219], 0\n"
"v_mul_f32 v141, s84, v141\n"
"v_mul_f32 v142, s84, v142\n"
"v_mul_f32 v143, s84, v143\n"
"v_permlane16_swap_b32 v64, v66\n"
"v_cvt_pk_bf16_f32 v68, v136, v137\n"
"v_mfma_f32_32x32x16_bf16 a[112:127], v[216:219], v[216:219], 0\n"
"v_permlane16_swap_b32 v65, v67\n"
"v_cvt_pk_bf16_f32 v69, v138, v139\n"
"v_cvt_pk_bf16_f32 v70, v140, v141\n"
"v_cvt_pk_bf16_f32 v71, v142, v143\n"
"v_mul_f32 v144, s84, v144\n"
"v_mfma_f32_16x16x32_bf16 v[232:235], a[160:163], v[48:51], 0\n"
"v_mul_f32 v145, s84, v145\n"
"v_mul_f32 v146, s84, v146\n"
"v_mfma_f32_16x16x32_bf16 v[232:235], a[164:167], v[52:55], v[232:235]\n"
"v_mul_f32 v147, s84, v147\n"
"v_mul_f32 v148, s84, v148\n"
"v_mfma_f32_16x16x32_bf16 v[232:235], a[168:171], v[56:59], v[232:235]\n"
"v_mul_f32 v149, s84, v149\n"
"v_mul_f32 v150, s84, v150\n"
"v_mfma_f32_16x16x32_bf16 v[232:235], a[172:175], v[60:63], v[232:235]\n"
"v_mul_f32 v151, s84, v151\n"
"v_cvt_pk_bf16_f32 v72, v144, v145\n"
"v_mfma_f32_32x32x16_bf16 a[128:143], v[216:219], v[216:219], 0\n"
"v_permlane16_swap_b32 v68, v70\n"
"v_permlane16_swap_b32 v69, v71\n"
"v_cvt_pk_bf16_f32 v73, v146, v147\n"
"v_cvt_pk_bf16_f32 v74, v148, v149\n"
"v_mfma_f32_32x32x16_bf16 a[144:159], v[216:219], v[216:219], 0\n"
"v_cvt_pk_bf16_f32 v75, v150, v151\n"
"v_mul_f32 v152, s84, v152\n"
"v_mul_f32 v153, s84, v153\n"
"v_mul_f32 v154, s84, v154\n"
"v_mul_f32 v155, s84, v155\n"
"v_mul_f32 v156, s84, v156\n"
"v_mfma_f32_16x16x32_bf16 v[236:239], a[160:163], v[64:67], 0\n"
"v_mul_f32 v157, s84, v157\n"
"v_mul_f32 v158, s84, v158\n"
"v_mfma_f32_16x16x32_bf16 v[236:239], a[164:167], v[68:71], v[236:239]\n"
"v_mul_f32 v159, s84, v159\n"
"v_permlane16_swap_b32 v72, v74\n"
"v_permlane16_swap_b32 v73, v75\n"
"v_cvt_pk_bf16_f32 v76, v152, v153\n"
"s_nop 0\n"
"v_mfma_f32_16x16x32_bf16 v[236:239], a[168:171], v[72:75], v[236:239]\n"
"v_cvt_pk_bf16_f32 v77, v154, v155\n"
"v_cvt_pk_bf16_f32 v78, v156, v157\n"
"v_cvt_pk_bf16_f32 v79, v158, v159\n"
"v_max3_f32 v220, v220, v221, v222\n"
"v_permlane16_swap_b32 v76, v78\n"
"v_permlane16_swap_b32 v77, v79\n"
"v_max3_f32 v224, v224, v225, v226\n"
"s_nop 0\n"
"v_mfma_f32_16x16x32_bf16 v[236:239], a[172:175], v[76:79], v[236:239]\n"
"v_max3_f32 v228, v228, v229, v230\n"
"v_max3_f32 v232, v232, v233, v234\n"
"v_max_f32 v220, v220, v223\n"
"v_max_f32 v224, v224, v227\n"
"v_max_f32 v228, v228, v231\n"
"v_max_f32 v232, v232, v235\n"
"v_mov_b32 v221, v220\n"
"s_nop 0\n"
"v_max3_f32 v236, v236, v237, v238\n"
"v_max_f32 v236, v236, v239\n"
"v_mov_b32 v225, v224\n"
"v_mov_b32 v229, v228\n"
"v_mov_b32 v233, v232\n"
"v_mov_b32 v237, v236\n"
"v_permlane16_swap_b32 v220, v221\n"
"v_permlane16_swap_b32 v224, v225\n"
"v_permlane16_swap_b32 v228, v229\n"
"v_permlane16_swap_b32 v232, v233\n"
"v_permlane16_swap_b32 v236, v237\n"
"v_max_f32 v220, v220, v221\n"
"v_max_f32 v224, v224, v225\n"
"v_max_f32 v228, v228, v229\n"
"v_max_f32 v232, v232, v233\n"
"v_max_f32 v236, v236, v237\n"
"v_mov_b32 v221, v220\n"
"v_mov_b32 v225, v224\n"
"v_mov_b32 v229, v228\n"
"v_mov_b32 v233, v232\n"
"v_mov_b32 v237, v236\n"
"v_permlane32_swap_b32 v220, v221\n"
"v_permlane32_swap_b32 v224, v225\n"
"v_permlane32_swap_b32 v228, v229\n"
"v_permlane32_swap_b32 v232, v233\n"
"v_permlane32_swap_b32 v236, v237\n"
"v_min_f32_e64 v160, -v220, -v221\n"
"v_min_f32_e64 v161, -v220, -v221\n"
"v_min_f32_e64 v162, -v220, -v221\n"
"v_min_f32_e64 v163, -v220, -v221\n"
"v_min_f32_e64 v164, -v224, -v225\n"
"v_min_f32_e64 v165, -v224, -v225\n"
"v_min_f32_e64 v166, -v224, -v225\n"
"v_min_f32_e64 v167, -v224, -v225\n"
"v_min_f32_e64 v168, -v228, -v229\n"
"v_min_f32_e64 v169, -v228, -v229\n"
"v_min_f32_e64 v170, -v228, -v229\n"
"v_min_f32_e64 v171, -v228, -v229\n"
"v_min_f32_e64 v172, -v232, -v233\n"
"v_min_f32_e64 v173, -v232, -v233\n"
"v_min_f32_e64 v174, -v232, -v233\n"
"v_min_f32_e64 v175, -v232, -v233\n"
"v_min_f32_e64 v176, -v236, -v237\n"
"v_min_f32_e64 v177, -v236, -v237\n"
"v_min_f32_e64 v178, -v236, -v237\n"
"v_min_f32_e64 v179, -v236, -v237\n"
"s_waitcnt vmcnt(8)\n"
"s_waitcnt lgkmcnt(0)\n"
"s_barrier\n"
"s_cmp_lg_u32 s85, 0\n"
"s_cbranch_scc1 L_fbm_%=\n"
"L_attn_%=:\n"
"v_mfma_f32_16x16x32_bf16 v[112:115], a[160:163], v[0:3], v[160:163]\n"
"ds_read_b64_tr_b16 a[224:225], v190 offset:0\n"
"ds_read_b64_tr_b16 a[226:227], v190 offset:4096\n"
"ds_read_b64_tr_b16 a[228:229], v190 offset:8192\n"
"ds_read_b64_tr_b16 a[230:231], v190 offset:12288\n"
"ds_read_b64_tr_b16 a[236:237], v191 offset:8192\n"
"ds_read_b64_tr_b16 a[238:239], v191 offset:12288\n"
"ds_read_b64_tr_b16 a[232:233], v191 offset:0\n"
"ds_read_b64_tr_b16 a[234:235], v191 offset:4096\n"
"ds_read_b64_tr_b16 a[240:241], v192 offset:0\n"
"ds_read_b64_tr_b16 a[242:243], v192 offset:4096\n"
"v_mfma_f32_16x16x32_bf16 v[112:115], a[164:167], v[4:7], v[112:115]\n"
"v_mfma_f32_16x16x32_bf16 v[112:115], a[168:171], v[8:11], v[112:115]\n"
"v_mfma_f32_16x16x32_bf16 v[112:115], a[172:175], v[12:15], v[112:115]\n"
"v_mfma_f32_16x16x32_bf16 v[116:119], a[188:191], v[12:15], v[160:163]\n"
"v_mfma_f32_16x16x32_bf16 v[116:119], a[184:187], v[8:11], v[116:119]\n"
"v_mfma_f32_16x16x32_bf16 v[116:119], a[180:183], v[4:7], v[116:119]\n"
"v_mfma_f32_16x16x32_bf16 v[116:119], a[176:179], v[0:3], v[116:119]\n"
"v_mfma_f32_16x16x32_bf16 v[120:123], a[192:195], v[0:3], v[160:163]\n"
"v_mfma_f32_16x16x32_bf16 v[120:123], a[196:199], v[4:7], v[120:123]\n"
"v_mfma_f32_16x16x32_bf16 v[120:123], a[200:203], v[8:11], v[120:123]\n"
"v_mfma_f32_16x16x32_bf16 v[120:123], a[204:207], v[12:15], v[120:123]\n"
"v_mfma_f32_16x16x32_bf16 v[124:127], a[220:223], v[12:15], v[160:163]\n"
"v_exp_f32 v112, v112\n"
"v_mfma_f32_16x16x32_bf16 v[124:127], a[216:219], v[8:11], v[124:127]\n"
"v_add_f32 v180, v180, v112\n"
"v_mfma_f32_16x16x32_bf16 v[124:127], a[212:215], v[4:7], v[124:127]\n"
"ds_read_b64_tr_b16 a[244:245], v192 offset:8192\n"
"v_exp_f32 v113, v113\n"
"v_mfma_f32_16x16x32_bf16 v[124:127], a[208:211], v[0:3], v[124:127]\n"
"v_add_f32 v180, v180, v113\n"
"v_exp_f32 v114, v114\n"
"s_nop 0\n"
"v_add_f32 v180, v180, v114\n"
"v_exp_f32 v115, v115\n"
"s_nop 0\n"
"v_add_f32 v180, v180, v115\n"
"ds_read_b64_tr_b16 a[246:247], v192 offset:12288\n"
"v_exp_f32 v116, v116\n"
"s_nop 0\n"
"v_add_f32 v180, v180, v116\n"
"v_exp_f32 v117, v117\n"
"s_nop 0\n"
"v_add_f32 v180, v180, v117\n"
"v_exp_f32 v118, v118\n"
"s_nop 0\n"
"v_add_f32 v180, v180, v118\n"
"ds_read_b64_tr_b16 a[252:253], v193 offset:8192\n"
"v_exp_f32 v119, v119\n"
"ds_read_b64_tr_b16 a[254:255], v193 offset:12288\n"
"v_add_f32 v180, v180, v119\n"
"v_exp_f32 v120, v120\n"
"ds_read_b64_tr_b16 a[248:249], v193 offset:0\n"
"v_add_f32 v180, v180, v120\n"
"v_exp_f32 v121, v121\n"
"ds_read_b64_tr_b16 a[250:251], v193 offset:4096\n"
"v_add_f32 v180, v180, v121\n"
"v_mfma_f32_16x16x32_bf16 v[128:131], a[160:163], v[16:19], v[164:167]\n"
"v_exp_f32 v122, v122\n"
"v_mfma_f32_16x16x32_bf16 v[128:131], a[164:167], v[20:23], v[128:131]\n"
"s_add_u32 m0, s78, 0x10000\n"
"v_add_f32 v180, v180, v122\n"
"v_cvt_pk_bf16_f32 v144, v112, v113\n"
"v_mfma_f32_16x16x32_bf16 v[128:131], a[168:171], v[24:27], v[128:131]\n"
"buffer_load_dwordx4 v198, s[52:55], s75 offen offset:0 nt lds\n"
"v_exp_f32 v123, v123\n"
"v_mfma_f32_16x16x32_bf16 v[128:131], a[172:175], v[28:31], v[128:131]\n"
"v_add_f32 v180, v180, v123\n"
"v_cvt_pk_bf16_f32 v145, v114, v115\n"
"v_mfma_f32_16x16x32_bf16 v[132:135], a[188:191], v[28:31], v[164:167]\n"
"v_exp_f32 v124, v124\n"
"v_mfma_f32_16x16x32_bf16 v[132:135], a[184:187], v[24:27], v[132:135]\n"
"buffer_load_dwordx4 v198, s[52:55], s75 offen offset:2048 nt lds\n"
"v_add_f32 v180, v180, v124\n"
"v_mfma_f32_16x16x32_bf16 v[132:135], a[180:183], v[20:23], v[132:135]\n"
"v_exp_f32 v125, v125\n"
"v_mfma_f32_16x16x32_bf16 v[132:135], a[176:179], v[16:19], v[132:135]\n"
"s_add_u32 m0, s78, 0x10380\n"
"v_add_f32 v180, v180, v125\n"
"v_cvt_pk_bf16_f32 v146, v116, v117\n"
"v_mfma_f32_16x16x32_bf16 v[136:139], a[192:195], v[16:19], v[164:167]\n"
"buffer_load_dwordx4 v198, s[52:55], s75 offen offset:128 nt lds\n"
"v_exp_f32 v126, v126\n"
"v_mfma_f32_16x16x32_bf16 v[136:139], a[196:199], v[20:23], v[136:139]\n"
"v_add_f32 v180, v180, v126\n"
"v_cvt_pk_bf16_f32 v147, v118, v119\n"
"v_mfma_f32_16x16x32_bf16 v[136:139], a[200:203], v[24:27], v[136:139]\n"
"v_exp_f32 v127, v127\n"
"v_mfma_f32_16x16x32_bf16 v[136:139], a[204:207], v[28:31], v[136:139]\n"
"buffer_load_dwordx4 v198, s[52:55], s75 offen offset:2176 nt lds\n"
"v_add_f32 v180, v180, v127\n"
"v_mfma_f32_16x16x32_bf16 v[140:143], a[220:223], v[28:31], v[164:167]\n"
"v_exp_f32 v128, v128\n"
"v_mfma_f32_16x16x32_bf16 v[140:143], a[216:219], v[24:27], v[140:143]\n"
"s_add_u32 m0, s78, 0x14000\n"
"v_add_f32 v181, v181, v128\n"
"v_cvt_pk_bf16_f32 v148, v120, v121\n"
"v_mfma_f32_16x16x32_bf16 v[140:143], a[212:215], v[20:23], v[140:143]\n"
"buffer_load_dwordx4 v198, s[56:59], s75 offen offset:0 nt lds\n"
"v_exp_f32 v129, v129\n"
"v_mfma_f32_16x16x32_bf16 v[140:143], a[208:211], v[16:19], v[140:143]\n"
"s_waitcnt lgkmcnt(0)\n"
"v_add_f32 v181, v181, v129\n"
"v_cvt_pk_bf16_f32 v149, v122, v123\n"
"v_exp_f32 v130, v130\n"
"buffer_load_dwordx4 v198, s[56:59], s75 offen offset:2048 nt lds\n"
"v_add_f32 v181, v181, v130\n"
"v_exp_f32 v131, v131\n"
"s_add_u32 m0, s78, 0x14380\n"
"v_add_f32 v181, v181, v131\n"
"v_cvt_pk_bf16_f32 v150, v124, v125\n"
"buffer_load_dwordx4 v198, s[56:59], s75 offen offset:128 nt lds\n"
"v_exp_f32 v132, v132\n"
"s_nop 0\n"
"v_add_f32 v181, v181, v132\n"
"v_cvt_pk_bf16_f32 v151, v126, v127\n"
"v_exp_f32 v133, v133\n"
"buffer_load_dwordx4 v198, s[56:59], s75 offen offset:2176 nt lds\n"
"v_add_f32 v181, v181, v133\n"
"s_add_u32 s75, s75, 0x4000\n"
"v_exp_f32 v134, v134\n"
"ds_read_b64_tr_b16 v[80:81], v190 offset:1024\n"
"v_add_f32 v181, v181, v134\n"
"v_exp_f32 v135, v135\n"
"ds_read_b64_tr_b16 v[82:83], v190 offset:5120\n"
"v_add_f32 v181, v181, v135\n"
"v_exp_f32 v136, v136\n"
"ds_read_b64_tr_b16 v[84:85], v190 offset:9216\n"
"v_add_f32 v181, v181, v136\n"
"v_exp_f32 v137, v137\n"
"ds_read_b64_tr_b16 v[86:87], v190 offset:13312\n"
"v_add_f32 v181, v181, v137\n"
"s_mov_b32 s77, 3\n"
"L_att_%=:\n"
"v_mfma_f32_16x16x32_bf16 v[112:115], a[160:163], v[32:35], v[168:171]\n"
"v_exp_f32 v138, v138\n"
"v_mfma_f32_16x16x32_bf16 v[112:115], a[164:167], v[36:39], v[112:115]\n"
"ds_read_b64_tr_b16 v[92:93], v191 offset:9216\n"
"v_add_f32 v181, v181, v138\n"
"v_mfma_f32_16x16x32_bf16 v[112:115], a[168:171], v[40:43], v[112:115]\n"
"v_exp_f32 v139, v139\n"
"v_mfma_f32_16x16x32_bf16 v[112:115], a[172:175], v[44:47], v[112:115]\n"
"ds_read_b64_tr_b16 v[94:95], v191 offset:13312\n"
"v_add_f32 v181, v181, v139\n"
"v_mfma_f32_16x16x32_bf16 v[116:119], a[188:191], v[44:47], v[168:171]\n"
"v_exp_f32 v140, v140\n"
"v_mfma_f32_16x16x32_bf16 v[116:119], a[184:187], v[40:43], v[116:119]\n"
"ds_read_b64_tr_b16 v[88:89], v191 offset:1024\n"
"v_add_f32 v181, v181, v140\n"
"v_mfma_f32_16x16x32_bf16 v[116:119], a[180:183], v[36:39], v[116:119]\n"
"v_exp_f32 v141, v141\n"
"v_mfma_f32_16x16x32_bf16 v[116:119], a[176:179], v[32:35], v[116:119]\n"
"ds_read_b64_tr_b16 v[90:91], v191 offset:5120\n"
"v_add_f32 v181, v181, v141\n"
"v_mfma_f32_16x16x32_bf16 v[120:123], a[192:195], v[32:35], v[168:171]\n"
"v_exp_f32 v142, v142\n"
"v_mfma_f32_16x16x32_bf16 v[120:123], a[196:199], v[36:39], v[120:123]\n"
"ds_read_b64_tr_b16 v[96:97], v192 offset:1024\n"
"v_add_f32 v181, v181, v142\n"
"v_mfma_f32_16x16x32_bf16 v[120:123], a[200:203], v[40:43], v[120:123]\n"
"v_exp_f32 v143, v143\n"
"v_mfma_f32_16x16x32_bf16 v[120:123], a[204:207], v[44:47], v[120:123]\n"
"ds_read_b64_tr_b16 v[98:99], v192 offset:5120\n"
"v_add_f32 v181, v181, v143\n"
"v_mfma_f32_16x16x32_bf16 v[124:127], a[220:223], v[44:47], v[168:171]\n"
"v_exp_f32 v112, v112\n"
"v_mfma_f32_16x16x32_bf16 v[124:127], a[216:219], v[40:43], v[124:127]\n"
"ds_read_b64_tr_b16 v[100:101], v192 offset:9216\n"
"v_add_f32 v182, v182, v112\n"
"v_mfma_f32_16x16x32_bf16 v[124:127], a[212:215], v[36:39], v[124:127]\n"
"v_exp_f32 v113, v113\n"
"v_mfma_f32_16x16x32_bf16 v[124:127], a[208:211], v[32:35], v[124:127]\n"
"ds_read_b64_tr_b16 v[102:103], v192 offset:13312\n"
"v_add_f32 v182, v182, v113\n"
"v_mfma_f32_16x16x32_bf16 a[0:3], v[144:147], a[224:227], a[0:3]\n"
"v_exp_f32 v114, v114\n"
"v_mfma_f32_16x16x32_bf16 a[0:3], v[148:151], a[228:231], a[0:3]\n"
"ds_read_b64_tr_b16 v[108:109], v193 offset:9216\n"
"v_add_f32 v182, v182, v114\n"
"v_mfma_f32_16x16x32_bf16 a[4:7], v[148:151], a[236:239], a[4:7]\n"
"v_exp_f32 v115, v115\n"
"v_mfma_f32_16x16x32_bf16 a[4:7], v[144:147], a[232:235], a[4:7]\n"
"ds_read_b64_tr_b16 v[110:111], v193 offset:13312\n"
"v_add_f32 v182, v182, v115\n"
"v_mfma_f32_16x16x32_bf16 a[8:11], v[144:147], a[240:243], a[8:11]\n"
"v_exp_f32 v116, v116\n"
"v_mfma_f32_16x16x32_bf16 a[8:11], v[148:151], a[244:247], a[8:11]\n"
"ds_read_b64_tr_b16 v[104:105], v193 offset:1024\n"
"v_add_f32 v182, v182, v116\n"
"v_mfma_f32_16x16x32_bf16 a[12:15], v[148:151], a[252:255], a[12:15]\n"
"v_exp_f32 v117, v117\n"
"v_mfma_f32_16x16x32_bf16 a[12:15], v[144:147], a[248:251], a[12:15]\n"
"s_waitcnt lgkmcnt(3)\n"
"ds_read_b64_tr_b16 v[106:107], v193 offset:5120\n"
"v_add_f32 v182, v182, v117\n"
"v_mfma_f32_16x16x32_bf16 a[16:19], v[144:147], v[80:83], a[16:19]\n"
"v_exp_f32 v118, v118\n"
"v_mfma_f32_16x16x32_bf16 a[16:19], v[148:151], v[84:87], a[16:19]\n"
"v_add_f32 v182, v182, v118\n"
"v_cvt_pk_bf16_f32 v152, v128, v129\n"
"v_mfma_f32_16x16x32_bf16 a[20:23], v[148:151], v[92:95], a[20:23]\n"
"v_exp_f32 v119, v119\n"
"v_mfma_f32_16x16x32_bf16 a[20:23], v[144:147], v[88:91], a[20:23]\n"
"v_add_f32 v182, v182, v119\n"
"v_cvt_pk_bf16_f32 v153, v130, v131\n"
"v_mfma_f32_16x16x32_bf16 a[24:27], v[144:147], v[96:99], a[24:27]\n"
"v_exp_f32 v120, v120\n"
"v_mfma_f32_16x16x32_bf16 a[24:27], v[148:151], v[100:103], a[24:27]\n"
"s_waitcnt lgkmcnt(1)\n"
"v_add_f32 v182, v182, v120\n"
"v_cvt_pk_bf16_f32 v154, v132, v133\n"
"v_mfma_f32_16x16x32_bf16 a[28:31], v[148:151], v[108:111], a[28:31]\n"
"s_waitcnt lgkmcnt(0)\n"
"v_exp_f32 v121, v121\n"
"v_mfma_f32_16x16x32_bf16 a[28:31], v[144:147], v[104:107], a[28:31]\n"
"v_add_f32 v182, v182, v121\n"
"v_cvt_pk_bf16_f32 v155, v134, v135\n"
"v_mfma_f32_16x16x32_bf16 v[128:131], a[160:163], v[48:51], v[172:175]\n"
"s_waitcnt vmcnt(8)\n"
"v_exp_f32 v122, v122\n"
"v_mfma_f32_16x16x32_bf16 v[128:131], a[164:167], v[52:55], v[128:131]\n"
"s_barrier\n"
"v_add_f32 v182, v182, v122\n"
"v_cvt_pk_bf16_f32 v156, v136, v137\n"
"v_mfma_f32_16x16x32_bf16 v[128:131], a[168:171], v[56:59], v[128:131]\n"
"v_exp_f32 v123, v123\n"
"v_mfma_f32_16x16x32_bf16 v[128:131], a[172:175], v[60:63], v[128:131]\n"
"v_add_f32 v182, v182, v123\n"
"v_cvt_pk_bf16_f32 v157, v138, v139\n"
"v_mfma_f32_16x16x32_bf16 v[132:135], a[188:191], v[60:63], v[172:175]\n"
"ds_read_b64_tr_b16 v[220:221], v190 offset:32768\n"
"v_exp_f32 v124, v124\n"
"v_mfma_f32_16x16x32_bf16 v[132:135], a[184:187], v[56:59], v[132:135]\n"
"v_add_f32 v182, v182, v124\n"
"v_cvt_pk_bf16_f32 v158, v140, v141\n"
"v_mfma_f32_16x16x32_bf16 v[132:135], a[180:183], v[52:55], v[132:135]\n"
"v_exp_f32 v125, v125\n"
"v_mfma_f32_16x16x32_bf16 v[132:135], a[176:179], v[48:51], v[132:135]\n"
"v_add_f32 v182, v182, v125\n"
"v_cvt_pk_bf16_f32 v159, v142, v143\n"
"v_mfma_f32_16x16x32_bf16 v[136:139], a[192:195], v[48:51], v[172:175]\n"
"ds_read_b64_tr_b16 v[222:223], v190 offset:36864\n"
"v_exp_f32 v126, v126\n"
"v_mfma_f32_16x16x32_bf16 v[136:139], a[196:199], v[52:55], v[136:139]\n"
"v_add_f32 v182, v182, v126\n"
"v_cvt_pk_bf16_f32 v144, v112, v113\n"
"v_mfma_f32_16x16x32_bf16 v[136:139], a[200:203], v[56:59], v[136:139]\n"
"v_exp_f32 v127, v127\n"
"v_mfma_f32_16x16x32_bf16 v[136:139], a[204:207], v[60:63], v[136:139]\n"
"v_add_f32 v182, v182, v127\n"
"v_cvt_pk_bf16_f32 v145, v114, v115\n"
"v_mfma_f32_16x16x32_bf16 v[140:143], a[220:223], v[60:63], v[172:175]\n"
"ds_read_b64_tr_b16 v[224:225], v190 offset:40960\n"
"v_exp_f32 v128, v128\n"
"v_mfma_f32_16x16x32_bf16 v[140:143], a[216:219], v[56:59], v[140:143]\n"
"v_add_f32 v183, v183, v128\n"
"v_cvt_pk_bf16_f32 v146, v116, v117\n"
"v_mfma_f32_16x16x32_bf16 v[140:143], a[212:215], v[52:55], v[140:143]\n"
"v_exp_f32 v129, v129\n"
"v_mfma_f32_16x16x32_bf16 v[140:143], a[208:211], v[48:51], v[140:143]\n"
"v_add_f32 v183, v183, v129\n"
"v_cvt_pk_bf16_f32 v147, v118, v119\n"
"v_mfma_f32_16x16x32_bf16 a[32:35], v[152:155], a[224:227], a[32:35]\n"
"ds_read_b64_tr_b16 v[226:227], v190 offset:45056\n"
"v_exp_f32 v130, v130\n"
"v_mfma_f32_16x16x32_bf16 a[32:35], v[156:159], a[228:231], a[32:35]\n"
"v_add_f32 v183, v183, v130\n"
"v_cvt_pk_bf16_f32 v148, v120, v121\n"
"v_mfma_f32_16x16x32_bf16 a[36:39], v[156:159], a[236:239], a[36:39]\n"
"v_exp_f32 v131, v131\n"
"v_mfma_f32_16x16x32_bf16 a[36:39], v[152:155], a[232:235], a[36:39]\n"
"v_add_f32 v183, v183, v131\n"
"v_cvt_pk_bf16_f32 v149, v122, v123\n"
"v_mfma_f32_16x16x32_bf16 a[40:43], v[152:155], a[240:243], a[40:43]\n"
"ds_read_b64_tr_b16 v[232:233], v191 offset:40960\n"
"v_exp_f32 v132, v132\n"
"v_mfma_f32_16x16x32_bf16 a[40:43], v[156:159], a[244:247], a[40:43]\n"
"v_add_f32 v183, v183, v132\n"
"v_cvt_pk_bf16_f32 v150, v124, v125\n"
"v_mfma_f32_16x16x32_bf16 a[44:47], v[156:159], a[252:255], a[44:47]\n"
"v_exp_f32 v133, v133\n"
"v_mfma_f32_16x16x32_bf16 a[44:47], v[152:155], a[248:251], a[44:47]\n"
"v_add_f32 v183, v183, v133\n"
"v_cvt_pk_bf16_f32 v151, v126, v127\n"
"v_mfma_f32_16x16x32_bf16 a[48:51], v[152:155], v[80:83], a[48:51]\n"
"ds_read_b64_tr_b16 v[234:235], v191 offset:45056\n"
"v_exp_f32 v134, v134\n"
"v_mfma_f32_16x16x32_bf16 a[48:51], v[156:159], v[84:87], a[48:51]\n"
"ds_read_b64_tr_b16 v[228:229], v191 offset:32768\n"
"v_add_f32 v183, v183, v134\n"
"v_mfma_f32_16x16x32_bf16 a[52:55], v[156:159], v[92:95], a[52:55]\n"
"v_exp_f32 v135, v135\n"
"v_mfma_f32_16x16x32_bf16 a[52:55], v[152:155], v[88:91], a[52:55]\n"
"ds_read_b64_tr_b16 v[230:231], v191 offset:36864\n"
"v_add_f32 v183, v183, v135\n"
"v_mfma_f32_16x16x32_bf16 a[56:59], v[152:155], v[96:99], a[56:59]\n"
"v_exp_f32 v136, v136\n"
"v_mfma_f32_16x16x32_bf16 a[56:59], v[156:159], v[100:103], a[56:59]\n"
"ds_read_b64_tr_b16 v[236:237], v192 offset:32768\n"
"v_add_f32 v183, v183, v136\n"
"v_mfma_f32_16x16x32_bf16 a[60:63], v[156:159], v[108:111], a[60:63]\n"
"v_exp_f32 v137, v137\n"
"v_mfma_f32_16x16x32_bf16 a[60:63], v[152:155], v[104:107], a[60:63]\n"
"ds_read_b64_tr_b16 v[238:239], v192 offset:36864\n"
"v_add_f32 v183, v183, v137\n"
"v_mfma_f32_16x16x32_bf16 v[112:115], a[160:163], v[64:67], v[176:179]\n"
"v_exp_f32 v138, v138\n"
"v_mfma_f32_16x16x32_bf16 v[112:115], a[164:167], v[68:71], v[112:115]\n"
"ds_read_b128 a[160:163], v186 offset:32768\n"
"v_add_f32 v183, v183, v138\n"
"v_mfma_f32_16x16x32_bf16 v[112:115], a[168:171], v[72:75], v[112:115]\n"
"v_exp_f32 v139, v139\n"
"v_mfma_f32_16x16x32_bf16 v[112:115], a[172:175], v[76:79], v[112:115]\n"
"ds_read_b128 a[164:167], v187 offset:32768\n"
"v_add_f32 v183, v183, v139\n"
"v_mfma_f32_16x16x32_bf16 v[116:119], a[188:191], v[76:79], v[176:179]\n"
"v_exp_f32 v140, v140\n"
"v_mfma_f32_16x16x32_bf16 v[116:119], a[184:187], v[72:75], v[116:119]\n"
"ds_read_b128 a[168:171], v186 offset:33792\n"
"v_add_f32 v183, v183, v140\n"
"v_mfma_f32_16x16x32_bf16 v[116:119], a[180:183], v[68:71], v[116:119]\n"
"v_exp_f32 v141, v141\n"
"v_mfma_f32_16x16x32_bf16 v[116:119], a[176:179], v[64:67], v[116:119]\n"
"ds_read_b128 a[172:175], v187 offset:33792\n"
"v_add_f32 v183, v183, v141\n"
"v_mfma_f32_16x16x32_bf16 v[120:123], a[192:195], v[64:67], v[176:179]\n"
"v_exp_f32 v142, v142\n"
"v_mfma_f32_16x16x32_bf16 v[120:123], a[196:199], v[68:71], v[120:123]\n"
"ds_read_b128 a[188:191], v187 offset:37888\n"
"v_add_f32 v183, v183, v142\n"
"v_mfma_f32_16x16x32_bf16 v[120:123], a[200:203], v[72:75], v[120:123]\n"
"v_exp_f32 v143, v143\n"
"v_mfma_f32_16x16x32_bf16 v[120:123], a[204:207], v[76:79], v[120:123]\n"
"ds_read_b128 a[184:187], v186 offset:37888\n"
"v_add_f32 v183, v183, v143\n"
"v_mfma_f32_16x16x32_bf16 v[124:127], a[220:223], v[76:79], v[176:179]\n"
"ds_read_b128 a[180:183], v187 offset:36864\n"
"v_exp_f32 v112, v112\n"
"v_mfma_f32_16x16x32_bf16 v[124:127], a[216:219], v[72:75], v[124:127]\n"
"v_add_f32 v184, v184, v112\n"
"v_cvt_pk_bf16_f32 v152, v128, v129\n"
"v_mfma_f32_16x16x32_bf16 v[124:127], a[212:215], v[68:71], v[124:127]\n"
"v_exp_f32 v113, v113\n"
"v_mfma_f32_16x16x32_bf16 v[124:127], a[208:211], v[64:67], v[124:127]\n"
"v_add_f32 v184, v184, v113\n"
"v_cvt_pk_bf16_f32 v153, v130, v131\n"
"v_mfma_f32_16x16x32_bf16 a[64:67], v[144:147], a[224:227], a[64:67]\n"
"v_exp_f32 v114, v114\n"
"v_mfma_f32_16x16x32_bf16 a[64:67], v[148:151], a[228:231], a[64:67]\n"
"ds_read_b128 a[176:179], v186 offset:36864\n"
"v_add_f32 v184, v184, v114\n"
"v_mfma_f32_16x16x32_bf16 a[68:71], v[148:151], a[236:239], a[68:71]\n"
"v_exp_f32 v115, v115\n"
"v_mfma_f32_16x16x32_bf16 a[68:71], v[144:147], a[232:235], a[68:71]\n"
"ds_read_b128 a[192:195], v186 offset:40960\n"
"v_add_f32 v184, v184, v115\n"
"v_mfma_f32_16x16x32_bf16 a[72:75], v[144:147], a[240:243], a[72:75]\n"
"v_exp_f32 v116, v116\n"
"v_mfma_f32_16x16x32_bf16 a[72:75], v[148:151], a[244:247], a[72:75]\n"
"ds_read_b128 a[196:199], v187 offset:40960\n"
"v_add_f32 v184, v184, v116\n"
"v_mfma_f32_16x16x32_bf16 a[76:79], v[148:151], a[252:255], a[76:79]\n"
"v_exp_f32 v117, v117\n"
"v_mfma_f32_16x16x32_bf16 a[76:79], v[144:147], a[248:251], a[76:79]\n"
"ds_read_b128 a[200:203], v186 offset:41984\n"
"v_add_f32 v184, v184, v117\n"
"v_mfma_f32_16x16x32_bf16 a[80:83], v[144:147], v[80:83], a[80:83]\n"
"v_exp_f32 v118, v118\n"
"v_cvt_pk_bf16_f32 v154, v132, v133\n"
"v_mfma_f32_16x16x32_bf16 a[80:83], v[148:151], v[84:87], a[80:83]\n"
"v_add_f32 v184, v184, v118\n"
"v_cvt_pk_bf16_f32 v155, v134, v135\n"
"v_mfma_f32_16x16x32_bf16 a[84:87], v[148:151], v[92:95], a[84:87]\n"
"v_exp_f32 v119, v119\n"
"v_mfma_f32_16x16x32_bf16 a[84:87], v[144:147], v[88:91], a[84:87]\n"
"ds_read_b128 a[204:207], v187 offset:41984\n"
"v_add_f32 v184, v184, v119\n"
"v_mfma_f32_16x16x32_bf16 a[88:91], v[144:147], v[96:99], a[88:91]\n"
"v_exp_f32 v120, v120\n"
"v_mfma_f32_16x16x32_bf16 a[88:91], v[148:151], v[100:103], a[88:91]\n"
"ds_read_b128 a[220:223], v187 offset:46080\n"
"v_add_f32 v184, v184, v120\n"
"v_mfma_f32_16x16x32_bf16 a[92:95], v[148:151], v[108:111], a[92:95]\n"
"v_exp_f32 v121, v121\n"
"v_mfma_f32_16x16x32_bf16 a[92:95], v[144:147], v[104:107], a[92:95]\n"
"s_waitcnt lgkmcnt(2)\n"
"ds_read_b128 a[216:219], v186 offset:46080\n"
"v_add_f32 v184, v184, v121\n"
"v_mfma_f32_16x16x32_bf16 v[128:131], a[160:163], v[0:3], v[160:163]\n"
"v_exp_f32 v122, v122\n"
"v_mfma_f32_16x16x32_bf16 v[128:131], a[164:167], v[4:7], v[128:131]\n"
"ds_read_b128 a[212:215], v187 offset:45056\n"
"v_add_f32 v184, v184, v122\n"
"v_mfma_f32_16x16x32_bf16 v[128:131], a[168:171], v[8:11], v[128:131]\n"
"v_exp_f32 v123, v123\n"
"v_mfma_f32_16x16x32_bf16 v[128:131], a[172:175], v[12:15], v[128:131]\n"
"v_add_f32 v184, v184, v123\n"
"v_cvt_pk_bf16_f32 v156, v136, v137\n"
"v_mfma_f32_16x16x32_bf16 v[132:135], a[188:191], v[12:15], v[160:163]\n"
"v_exp_f32 v124, v124\n"
"v_mfma_f32_16x16x32_bf16 v[132:135], a[184:187], v[8:11], v[132:135]\n"
"v_add_f32 v184, v184, v124\n"
"v_cvt_pk_bf16_f32 v157, v138, v139\n"
"v_mfma_f32_16x16x32_bf16 v[132:135], a[180:183], v[4:7], v[132:135]\n"
"ds_read_b128 a[208:211], v186 offset:45056\n"
"v_exp_f32 v125, v125\n"
"v_mfma_f32_16x16x32_bf16 v[132:135], a[176:179], v[0:3], v[132:135]\n"
"v_add_f32 v184, v184, v125\n"
"v_cvt_pk_bf16_f32 v158, v140, v141\n"
"v_mfma_f32_16x16x32_bf16 v[136:139], a[192:195], v[0:3], v[160:163]\n"
"v_exp_f32 v126, v126\n"
"v_mfma_f32_16x16x32_bf16 v[136:139], a[196:199], v[4:7], v[136:139]\n"
"v_add_f32 v184, v184, v126\n"
"v_cvt_pk_bf16_f32 v159, v142, v143\n"
"v_mfma_f32_16x16x32_bf16 v[136:139], a[200:203], v[8:11], v[136:139]\n"
"s_waitcnt lgkmcnt(1)\n"
"v_exp_f32 v127, v127\n"
"v_mfma_f32_16x16x32_bf16 v[136:139], a[204:207], v[12:15], v[136:139]\n"
"v_add_f32 v184, v184, v127\n"
"v_cvt_pk_bf16_f32 v144, v112, v113\n"
"v_mfma_f32_16x16x32_bf16 v[140:143], a[220:223], v[12:15], v[160:163]\n"
"v_exp_f32 v128, v128\n"
"v_mfma_f32_16x16x32_bf16 v[140:143], a[216:219], v[8:11], v[140:143]\n"
"v_cvt_pk_bf16_f32 v145, v114, v115\n"
"v_add_f32 v180, v180, v128\n"
"v_mfma_f32_16x16x32_bf16 v[140:143], a[212:215], v[4:7], v[140:143]\n"
"s_waitcnt lgkmcnt(0)\n"
"ds_read_b64_tr_b16 v[240:241], v192 offset:40960\n"
"v_exp_f32 v129, v129\n"
"v_mfma_f32_16x16x32_bf16 v[140:143], a[208:211], v[0:3], v[140:143]\n"
"v_cvt_pk_bf16_f32 v146, v116, v117\n"
"v_add_f32 v180, v180, v129\n"
"v_mfma_f32_16x16x32_bf16 a[96:99], v[152:155], a[224:227], a[96:99]\n"
"v_exp_f32 v130, v130\n"
"v_mfma_f32_16x16x32_bf16 a[96:99], v[156:159], a[228:231], a[96:99]\n"
"v_cvt_pk_bf16_f32 v147, v118, v119\n"
"v_add_f32 v180, v180, v130\n"
"v_mfma_f32_16x16x32_bf16 a[100:103], v[156:159], a[236:239], a[100:103]\n"
"v_exp_f32 v131, v131\n"
"v_mfma_f32_16x16x32_bf16 a[100:103], v[152:155], a[232:235], a[100:103]\n"
"v_cvt_pk_bf16_f32 v148, v120, v121\n"
"v_add_f32 v180, v180, v131\n"
"v_mfma_f32_16x16x32_bf16 a[104:107], v[152:155], a[240:243], a[104:107]\n"
"ds_read_b64_tr_b16 v[242:243], v192 offset:45056\n"
"v_exp_f32 v132, v132\n"
"v_mfma_f32_16x16x32_bf16 a[104:107], v[156:159], a[244:247], a[104:107]\n"
"v_cvt_pk_bf16_f32 v149, v122, v123\n"
"v_add_f32 v180, v180, v132\n"
"v_mfma_f32_16x16x32_bf16 a[108:111], v[156:159], a[252:255], a[108:111]\n"
"v_exp_f32 v133, v133\n"
"v_mfma_f32_16x16x32_bf16 a[108:111], v[152:155], a[248:251], a[108:111]\n"
"v_cvt_pk_bf16_f32 v150, v124, v125\n"
"v_add_f32 v180, v180, v133\n"
"v_mfma_f32_16x16x32_bf16 a[112:115], v[152:155], v[80:83], a[112:115]\n"
"v_exp_f32 v134, v134\n"
"v_mfma_f32_16x16x32_bf16 a[112:115], v[156:159], v[84:87], a[112:115]\n"
"v_cvt_pk_bf16_f32 v151, v126, v127\n"
"v_add_f32 v180, v180, v134\n"
"v_mfma_f32_16x16x32_bf16 a[116:119], v[156:159], v[92:95], a[116:119]\n"
"ds_read_b64_tr_b16 v[248:249], v193 offset:40960\n"
"v_exp_f32 v135, v135\n"
"v_mfma_f32_16x16x32_bf16 a[116:119], v[152:155], v[88:91], a[116:119]\n"
"ds_read_b64_tr_b16 v[250:251], v193 offset:45056\n"
"v_add_f32 v180, v180, v135\n"
"v_mfma_f32_16x16x32_bf16 a[120:123], v[152:155], v[96:99], a[120:123]\n"
"v_exp_f32 v136, v136\n"
"v_mfma_f32_16x16x32_bf16 a[120:123], v[156:159], v[100:103], a[120:123]\n"
"ds_read_b64_tr_b16 v[244:245], v193 offset:32768\n"
"v_add_f32 v180, v180, v136\n"
"v_mfma_f32_16x16x32_bf16 a[124:127], v[156:159], v[108:111], a[124:127]\n"
"v_exp_f32 v137, v137\n"
"v_mfma_f32_16x16x32_bf16 a[124:127], v[152:155], v[104:107], a[124:127]\n"
"ds_read_b64_tr_b16 v[246:247], v193 offset:36864\n"
"v_add_f32 v180, v180, v137\n"
"v_mfma_f32_16x16x32_bf16 v[112:115], a[160:163], v[16:19], v[164:167]\n"
"v_exp_f32 v138, v138\n"
"v_mfma_f32_16x16x32_bf16 v[112:115], a[164:167], v[20:23], v[112:115]\n"
"s_add_u32 m0, s78, 0x18000\n"
"v_add_f32 v180, v180, v138\n"
"v_cvt_pk_bf16_f32 v152, v128, v129\n"
"v_mfma_f32_16x16x32_bf16 v[112:115], a[168:171], v[24:27], v[112:115]\n"
"buffer_load_dwordx4 v198, s[52:55], s75 offen offset:0 nt lds\n"
"v_exp_f32 v139, v139\n"
"v_mfma_f32_16x16x32_bf16 v[112:115], a[172:175], v[28:31], v[112:115]\n"
"v_add_f32 v180, v180, v139\n"
"v_cvt_pk_bf16_f32 v153, v130, v131\n"
"v_mfma_f32_16x16x32_bf16 v[116:119], a[188:191], v[28:31], v[164:167]\n"
"v_exp_f32 v140, v140\n"
"v_mfma_f32_16x16x32_bf16 v[116:119], a[184:187], v[24:27], v[116:119]\n"
"buffer_load_dwordx4 v198, s[52:55], s75 offen offset:2048 nt lds\n"
"v_add_f32 v180, v180, v140\n"
"v_mfma_f32_16x16x32_bf16 v[116:119], a[180:183], v[20:23], v[116:119]\n"
"v_exp_f32 v141, v141\n"
"v_mfma_f32_16x16x32_bf16 v[116:119], a[176:179], v[16:19], v[116:119]\n"
"s_add_u32 m0, s78, 0x18380\n"
"v_add_f32 v180, v180, v141\n"
"v_cvt_pk_bf16_f32 v154, v132, v133\n"
"v_mfma_f32_16x16x32_bf16 v[120:123], a[192:195], v[16:19], v[164:167]\n"
"buffer_load_dwordx4 v198, s[52:55], s75 offen offset:128 nt lds\n"
"v_exp_f32 v142, v142\n"
"v_mfma_f32_16x16x32_bf16 v[120:123], a[196:199], v[20:23], v[120:123]\n"
"v_add_f32 v180, v180, v142\n"
"v_cvt_pk_bf16_f32 v155, v134, v135\n"
"v_mfma_f32_16x16x32_bf16 v[120:123], a[200:203], v[24:27], v[120:123]\n"
"v_exp_f32 v143, v143\n"
"v_mfma_f32_16x16x32_bf16 v[120:123], a[204:207], v[28:31], v[120:123]\n"
"buffer_load_dwordx4 v198, s[52:55], s75 offen offset:2176 nt lds\n"
"v_add_f32 v180, v180, v143\n"
"v_mfma_f32_16x16x32_bf16 v[124:127], a[220:223], v[28:31], v[164:167]\n"
"v_exp_f32 v112, v112\n"
"v_mfma_f32_16x16x32_bf16 v[124:127], a[216:219], v[24:27], v[124:127]\n"
"s_add_u32 m0, s78, 0x1c000\n"
"v_add_f32 v181, v181, v112\n"
"v_cvt_pk_bf16_f32 v156, v136, v137\n"
"v_mfma_f32_16x16x32_bf16 v[124:127], a[212:215], v[20:23], v[124:127]\n"
"buffer_load_dwordx4 v198, s[56:59], s75 offen offset:0 nt lds\n"
"v_exp_f32 v113, v113\n"
"v_mfma_f32_16x16x32_bf16 v[124:127], a[208:211], v[16:19], v[124:127]\n"
"s_waitcnt lgkmcnt(0)\n"
"v_add_f32 v181, v181, v113\n"
"v_cvt_pk_bf16_f32 v157, v138, v139\n"
"v_mfma_f32_16x16x32_bf16 a[128:131], v[144:147], a[224:227], a[128:131]\n"
"v_exp_f32 v114, v114\n"
"v_mfma_f32_16x16x32_bf16 a[128:131], v[148:151], a[228:231], a[128:131]\n"
"buffer_load_dwordx4 v198, s[56:59], s75 offen offset:2048 nt lds\n"
"v_add_f32 v181, v181, v114\n"
"v_mfma_f32_16x16x32_bf16 a[132:135], v[148:151], a[236:239], a[132:135]\n"
"v_exp_f32 v115, v115\n"
"v_mfma_f32_16x16x32_bf16 a[132:135], v[144:147], a[232:235], a[132:135]\n"
"s_add_u32 m0, s78, 0x1c380\n"
"v_add_f32 v181, v181, v115\n"
"v_cvt_pk_bf16_f32 v158, v140, v141\n"
"v_mfma_f32_16x16x32_bf16 a[136:139], v[144:147], a[240:243], a[136:139]\n"
"buffer_load_dwordx4 v198, s[56:59], s75 offen offset:128 nt lds\n"
"v_exp_f32 v116, v116\n"
"v_mfma_f32_16x16x32_bf16 a[136:139], v[148:151], a[244:247], a[136:139]\n"
"v_add_f32 v181, v181, v116\n"
"v_cvt_pk_bf16_f32 v159, v142, v143\n"
"v_mfma_f32_16x16x32_bf16 a[140:143], v[148:151], a[252:255], a[140:143]\n"
"v_exp_f32 v117, v117\n"
"v_mfma_f32_16x16x32_bf16 a[140:143], v[144:147], a[248:251], a[140:143]\n"
"buffer_load_dwordx4 v198, s[56:59], s75 offen offset:2176 nt lds\n"
"v_add_f32 v181, v181, v117\n"
"v_mfma_f32_16x16x32_bf16 a[144:147], v[144:147], v[80:83], a[144:147]\n"
"s_add_u32 s75, s75, 0x4000\n"
"v_exp_f32 v118, v118\n"
"v_mfma_f32_16x16x32_bf16 a[144:147], v[148:151], v[84:87], a[144:147]\n"
"ds_read_b64_tr_b16 v[80:81], v190 offset:33792\n"
"v_add_f32 v181, v181, v118\n"
"v_mfma_f32_16x16x32_bf16 a[148:151], v[148:151], v[92:95], a[148:151]\n"
"v_exp_f32 v119, v119\n"
"v_mfma_f32_16x16x32_bf16 a[148:151], v[144:147], v[88:91], a[148:151]\n"
"ds_read_b64_tr_b16 v[82:83], v190 offset:37888\n"
"v_add_f32 v181, v181, v119\n"
"v_mfma_f32_16x16x32_bf16 a[152:155], v[144:147], v[96:99], a[152:155]\n"
"v_exp_f32 v120, v120\n"
"v_mfma_f32_16x16x32_bf16 a[152:155], v[148:151], v[100:103], a[152:155]\n"
"ds_read_b64_tr_b16 v[84:85], v190 offset:41984\n"
"v_add_f32 v181, v181, v120\n"
"v_mfma_f32_16x16x32_bf16 a[156:159], v[148:151], v[108:111], a[156:159]\n"
"v_exp_f32 v121, v121\n"
"v_mfma_f32_16x16x32_bf16 a[156:159], v[144:147], v[104:107], a[156:159]\n"
"ds_read_b64_tr_b16 v[86:87], v190 offset:46080\n"
"v_add_f32 v181, v181, v121\n"
"v_mfma_f32_16x16x32_bf16 v[128:131], a[160:163], v[32:35], v[168:171]\n"
"v_exp_f32 v122, v122\n"
"v_mfma_f32_16x16x32_bf16 v[128:131], a[164:167], v[36:39], v[128:131]\n"
"ds_read_b64_tr_b16 v[92:93], v191 offset:41984\n"
"v_add_f32 v181, v181, v122\n"
"v_mfma_f32_16x16x32_bf16 v[128:131], a[168:171], v[40:43], v[128:131]\n"
"v_exp_f32 v123, v123\n"
"v_mfma_f32_16x16x32_bf16 v[128:131], a[172:175], v[44:47], v[128:131]\n"
"ds_read_b64_tr_b16 v[94:95], v191 offset:46080\n"
"v_add_f32 v181, v181, v123\n"
"v_mfma_f32_16x16x32_bf16 v[132:135], a[188:191], v[44:47], v[168:171]\n"
"v_exp_f32 v124, v124\n"
"v_mfma_f32_16x16x32_bf16 v[132:135], a[184:187], v[40:43], v[132:135]\n"
"ds_read_b64_tr_b16 v[88:89], v191 offset:33792\n"
"v_add_f32 v181, v181, v124\n"
"v_mfma_f32_16x16x32_bf16 v[132:135], a[180:183], v[36:39], v[132:135]\n"
"v_exp_f32 v125, v125\n"
"v_mfma_f32_16x16x32_bf16 v[132:135], a[176:179], v[32:35], v[132:135]\n"
"ds_read_b64_tr_b16 v[90:91], v191 offset:37888\n"
"v_add_f32 v181, v181, v125\n"
"v_mfma_f32_16x16x32_bf16 v[136:139], a[192:195], v[32:35], v[168:171]\n"
"v_exp_f32 v126, v126\n"
"v_mfma_f32_16x16x32_bf16 v[136:139], a[196:199], v[36:39], v[136:139]\n"
"ds_read_b64_tr_b16 v[96:97], v192 offset:33792\n"
"v_add_f32 v181, v181, v126\n"
"v_mfma_f32_16x16x32_bf16 v[136:139], a[200:203], v[40:43], v[136:139]\n"
"v_exp_f32 v127, v127\n"
"v_mfma_f32_16x16x32_bf16 v[136:139], a[204:207], v[44:47], v[136:139]\n"
"ds_read_b64_tr_b16 v[98:99], v192 offset:37888\n"
"v_add_f32 v181, v181, v127\n"
"v_mfma_f32_16x16x32_bf16 v[140:143], a[220:223], v[44:47], v[168:171]\n"
"v_exp_f32 v128, v128\n"
"v_mfma_f32_16x16x32_bf16 v[140:143], a[216:219], v[40:43], v[140:143]\n"
"ds_read_b64_tr_b16 v[100:101], v192 offset:41984\n"
"v_add_f32 v182, v182, v128\n"
"v_mfma_f32_16x16x32_bf16 v[140:143], a[212:215], v[36:39], v[140:143]\n"
"v_exp_f32 v129, v129\n"
"v_mfma_f32_16x16x32_bf16 v[140:143], a[208:211], v[32:35], v[140:143]\n"
"ds_read_b64_tr_b16 v[102:103], v192 offset:46080\n"
"v_add_f32 v182, v182, v129\n"
"v_mfma_f32_16x16x32_bf16 a[0:3], v[152:155], v[220:223], a[0:3]\n"
"v_exp_f32 v130, v130\n"
"v_mfma_f32_16x16x32_bf16 a[0:3], v[156:159], v[224:227], a[0:3]\n"
"ds_read_b64_tr_b16 v[108:109], v193 offset:41984\n"
"v_add_f32 v182, v182, v130\n"
"v_mfma_f32_16x16x32_bf16 a[4:7], v[156:159], v[232:235], a[4:7]\n"
"v_exp_f32 v131, v131\n"
"v_mfma_f32_16x16x32_bf16 a[4:7], v[152:155], v[228:231], a[4:7]\n"
"ds_read_b64_tr_b16 v[110:111], v193 offset:46080\n"
"v_add_f32 v182, v182, v131\n"
"v_mfma_f32_16x16x32_bf16 a[8:11], v[152:155], v[236:239], a[8:11]\n"
"v_exp_f32 v132, v132\n"
"v_mfma_f32_16x16x32_bf16 a[8:11], v[156:159], v[240:243], a[8:11]\n"
"ds_read_b64_tr_b16 v[104:105], v193 offset:33792\n"
"v_add_f32 v182, v182, v132\n"
"v_mfma_f32_16x16x32_bf16 a[12:15], v[156:159], v[248:251], a[12:15]\n"
"v_exp_f32 v133, v133\n"
"v_mfma_f32_16x16x32_bf16 a[12:15], v[152:155], v[244:247], a[12:15]\n"
"s_waitcnt lgkmcnt(3)\n"
"ds_read_b64_tr_b16 v[106:107], v193 offset:37888\n"
"v_add_f32 v182, v182, v133\n"
"v_mfma_f32_16x16x32_bf16 a[16:19], v[152:155], v[80:83], a[16:19]\n"
"v_exp_f32 v134, v134\n"
"v_mfma_f32_16x16x32_bf16 a[16:19], v[156:159], v[84:87], a[16:19]\n"
"v_add_f32 v182, v182, v134\n"
"v_cvt_pk_bf16_f32 v144, v112, v113\n"
"v_mfma_f32_16x16x32_bf16 a[20:23], v[156:159], v[92:95], a[20:23]\n"
"v_exp_f32 v135, v135\n"
"v_mfma_f32_16x16x32_bf16 a[20:23], v[152:155], v[88:91], a[20:23]\n"
"v_add_f32 v182, v182, v135\n"
"v_cvt_pk_bf16_f32 v145, v114, v115\n"
"v_mfma_f32_16x16x32_bf16 a[24:27], v[152:155], v[96:99], a[24:27]\n"
"v_exp_f32 v136, v136\n"
"v_mfma_f32_16x16x32_bf16 a[24:27], v[156:159], v[100:103], a[24:27]\n"
"s_waitcnt lgkmcnt(1)\n"
"v_add_f32 v182, v182, v136\n"
"v_cvt_pk_bf16_f32 v146, v116, v117\n"
"v_mfma_f32_16x16x32_bf16 a[28:31], v[156:159], v[108:111], a[28:31]\n"
"s_waitcnt lgkmcnt(0)\n"
"v_exp_f32 v137, v137\n"
"v_mfma_f32_16x16x32_bf16 a[28:31], v[152:155], v[104:107], a[28:31]\n"
"v_add_f32 v182, v182, v137\n"
"v_cvt_pk_bf16_f32 v147, v118, v119\n"
"v_mfma_f32_16x16x32_bf16 v[112:115], a[160:163], v[48:51], v[172:175]\n"
"s_waitcnt vmcnt(8)\n"
"v_exp_f32 v138, v138\n"
"v_mfma_f32_16x16x32_bf16 v[112:115], a[164:167], v[52:55], v[112:115]\n"
"s_barrier\n"
"v_add_f32 v182, v182, v138\n"
"v_cvt_pk_bf16_f32 v148, v120, v121\n"
"v_mfma_f32_16x16x32_bf16 v[112:115], a[168:171], v[56:59], v[112:115]\n"
"v_exp_f32 v139, v139\n"
"v_mfma_f32_16x16x32_bf16 v[112:115], a[172:175], v[60:63], v[112:115]\n"
"v_add_f32 v182, v182, v139\n"
"v_cvt_pk_bf16_f32 v149, v122, v123\n"
"v_mfma_f32_16x16x32_bf16 v[116:119], a[188:191], v[60:63], v[172:175]\n"
"ds_read_b64_tr_b16 a[224:225], v194 offset:0\n"
"v_exp_f32 v140, v140\n"
"v_mfma_f32_16x16x32_bf16 v[116:119], a[184:187], v[56:59], v[116:119]\n"
"v_add_f32 v182, v182, v140\n"
"v_cvt_pk_bf16_f32 v150, v124, v125\n"
"v_mfma_f32_16x16x32_bf16 v[116:119], a[180:183], v[52:55], v[116:119]\n"
"v_exp_f32 v141, v141\n"
"v_mfma_f32_16x16x32_bf16 v[116:119], a[176:179], v[48:51], v[116:119]\n"
"v_add_f32 v182, v182, v141\n"
"v_cvt_pk_bf16_f32 v151, v126, v127\n"
"v_mfma_f32_16x16x32_bf16 v[120:123], a[192:195], v[48:51], v[172:175]\n"
"ds_read_b64_tr_b16 a[226:227], v194 offset:4096\n"
"v_exp_f32 v142, v142\n"
"v_mfma_f32_16x16x32_bf16 v[120:123], a[196:199], v[52:55], v[120:123]\n"
"v_add_f32 v182, v182, v142\n"
"v_cvt_pk_bf16_f32 v152, v128, v129\n"
"v_mfma_f32_16x16x32_bf16 v[120:123], a[200:203], v[56:59], v[120:123]\n"
"v_exp_f32 v143, v143\n"
"v_mfma_f32_16x16x32_bf16 v[120:123], a[204:207], v[60:63], v[120:123]\n"
"v_add_f32 v182, v182, v143\n"
"v_cvt_pk_bf16_f32 v153, v130, v131\n"
"v_mfma_f32_16x16x32_bf16 v[124:127], a[220:223], v[60:63], v[172:175]\n"
"ds_read_b64_tr_b16 a[228:229], v194 offset:8192\n"
"v_exp_f32 v112, v112\n"
"v_mfma_f32_16x16x32_bf16 v[124:127], a[216:219], v[56:59], v[124:127]\n"
"v_add_f32 v183, v183, v112\n"
"v_cvt_pk_bf16_f32 v154, v132, v133\n"
"v_mfma_f32_16x16x32_bf16 v[124:127], a[212:215], v[52:55], v[124:127]\n"
"v_exp_f32 v113, v113\n"
"v_mfma_f32_16x16x32_bf16 v[124:127], a[208:211], v[48:51], v[124:127]\n"
"v_add_f32 v183, v183, v113\n"
"v_cvt_pk_bf16_f32 v155, v134, v135\n"
"v_mfma_f32_16x16x32_bf16 a[32:35], v[144:147], v[220:223], a[32:35]\n"
"ds_read_b64_tr_b16 a[230:231], v194 offset:12288\n"
"v_exp_f32 v114, v114\n"
"v_mfma_f32_16x16x32_bf16 a[32:35], v[148:151], v[224:227], a[32:35]\n"
"v_add_f32 v183, v183, v114\n"
"v_cvt_pk_bf16_f32 v156, v136, v137\n"
"v_mfma_f32_16x16x32_bf16 a[36:39], v[148:151], v[232:235], a[36:39]\n"
"v_exp_f32 v115, v115\n"
"v_mfma_f32_16x16x32_bf16 a[36:39], v[144:147], v[228:231], a[36:39]\n"
"v_add_f32 v183, v183, v115\n"
"v_cvt_pk_bf16_f32 v157, v138, v139\n"
"v_mfma_f32_16x16x32_bf16 a[40:43], v[144:147], v[236:239], a[40:43]\n"
"ds_read_b64_tr_b16 a[236:237], v195 offset:8192\n"
"v_exp_f32 v116, v116\n"
"v_mfma_f32_16x16x32_bf16 a[40:43], v[148:151], v[240:243], a[40:43]\n"
"v_add_f32 v183, v183, v116\n"
"v_cvt_pk_bf16_f32 v158, v140, v141\n"
"v_mfma_f32_16x16x32_bf16 a[44:47], v[148:151], v[248:251], a[44:47]\n"
"v_exp_f32 v117, v117\n"
"v_mfma_f32_16x16x32_bf16 a[44:47], v[144:147], v[244:247], a[44:47]\n"
"v_add_f32 v183, v183, v117\n"
"v_cvt_pk_bf16_f32 v159, v142, v143\n"
"v_mfma_f32_16x16x32_bf16 a[48:51], v[144:147], v[80:83], a[48:51]\n"
"ds_read_b64_tr_b16 a[238:239], v195 offset:12288\n"
"v_exp_f32 v118, v118\n"
"v_mfma_f32_16x16x32_bf16 a[48:51], v[148:151], v[84:87], a[48:51]\n"
"ds_read_b64_tr_b16 a[232:233], v195 offset:0\n"
"v_add_f32 v183, v183, v118\n"
"v_mfma_f32_16x16x32_bf16 a[52:55], v[148:151], v[92:95], a[52:55]\n"
"v_exp_f32 v119, v119\n"
"v_mfma_f32_16x16x32_bf16 a[52:55], v[144:147], v[88:91], a[52:55]\n"
"ds_read_b64_tr_b16 a[234:235], v195 offset:4096\n"
"v_add_f32 v183, v183, v119\n"
"v_mfma_f32_16x16x32_bf16 a[56:59], v[144:147], v[96:99], a[56:59]\n"
"v_exp_f32 v120, v120\n"
"v_mfma_f32_16x16x32_bf16 a[56:59], v[148:151], v[100:103], a[56:59]\n"
"ds_read_b64_tr_b16 a[240:241], v196 offset:0\n"
"v_add_f32 v183, v183, v120\n"
"v_mfma_f32_16x16x32_bf16 a[60:63], v[148:151], v[108:111], a[60:63]\n"
"v_exp_f32 v121, v121\n"
"v_mfma_f32_16x16x32_bf16 a[60:63], v[144:147], v[104:107], a[60:63]\n"
"ds_read_b64_tr_b16 a[242:243], v196 offset:4096\n"
"v_add_f32 v183, v183, v121\n"
"v_mfma_f32_16x16x32_bf16 v[128:131], a[160:163], v[64:67], v[176:179]\n"
"v_exp_f32 v122, v122\n"
"v_mfma_f32_16x16x32_bf16 v[128:131], a[164:167], v[68:71], v[128:131]\n"
"ds_read_b128 a[160:163], v188 offset:0\n"
"v_add_f32 v183, v183, v122\n"
"v_mfma_f32_16x16x32_bf16 v[128:131], a[168:171], v[72:75], v[128:131]\n"
"v_exp_f32 v123, v123\n"
"v_mfma_f32_16x16x32_bf16 v[128:131], a[172:175], v[76:79], v[128:131]\n"
"ds_read_b128 a[164:167], v189 offset:0\n"
"v_add_f32 v183, v183, v123\n"
"v_mfma_f32_16x16x32_bf16 v[132:135], a[188:191], v[76:79], v[176:179]\n"
"v_exp_f32 v124, v124\n"
"v_mfma_f32_16x16x32_bf16 v[132:135], a[184:187], v[72:75], v[132:135]\n"
"ds_read_b128 a[168:171], v188 offset:1024\n"
"v_add_f32 v183, v183, v124\n"
"v_mfma_f32_16x16x32_bf16 v[132:135], a[180:183], v[68:71], v[132:135]\n"
"v_exp_f32 v125, v125\n"
"v_mfma_f32_16x16x32_bf16 v[132:135], a[176:179], v[64:67], v[132:135]\n"
"ds_read_b128 a[172:175], v189 offset:1024\n"
"v_add_f32 v183, v183, v125\n"
"v_mfma_f32_16x16x32_bf16 v[136:139], a[192:195], v[64:67], v[176:179]\n"
"v_exp_f32 v126, v126\n"
"v_mfma_f32_16x16x32_bf16 v[136:139], a[196:199], v[68:71], v[136:139]\n"
"ds_read_b128 a[188:191], v189 offset:5120\n"
"v_add_f32 v183, v183, v126\n"
"v_mfma_f32_16x16x32_bf16 v[136:139], a[200:203], v[72:75], v[136:139]\n"
"v_exp_f32 v127, v127\n"
"v_mfma_f32_16x16x32_bf16 v[136:139], a[204:207], v[76:79], v[136:139]\n"
"ds_read_b128 a[184:187], v188 offset:5120\n"
"v_add_f32 v183, v183, v127\n"
"v_mfma_f32_16x16x32_bf16 v[140:143], a[220:223], v[76:79], v[176:179]\n"
"ds_read_b128 a[180:183], v189 offset:4096\n"
"v_exp_f32 v128, v128\n"
"v_mfma_f32_16x16x32_bf16 v[140:143], a[216:219], v[72:75], v[140:143]\n"
"v_add_f32 v184, v184, v128\n"
"v_cvt_pk_bf16_f32 v144, v112, v113\n"
"v_mfma_f32_16x16x32_bf16 v[140:143], a[212:215], v[68:71], v[140:143]\n"
"v_exp_f32 v129, v129\n"
"v_mfma_f32_16x16x32_bf16 v[140:143], a[208:211], v[64:67], v[140:143]\n"
"v_add_f32 v184, v184, v129\n"
"v_cvt_pk_bf16_f32 v145, v114, v115\n"
"v_mfma_f32_16x16x32_bf16 a[64:67], v[152:155], v[220:223], a[64:67]\n"
"v_exp_f32 v130, v130\n"
"v_mfma_f32_16x16x32_bf16 a[64:67], v[156:159], v[224:227], a[64:67]\n"
"ds_read_b128 a[176:179], v188 offset:4096\n"
"v_add_f32 v184, v184, v130\n"
"v_mfma_f32_16x16x32_bf16 a[68:71], v[156:159], v[232:235], a[68:71]\n"
"v_exp_f32 v131, v131\n"
"v_mfma_f32_16x16x32_bf16 a[68:71], v[152:155], v[228:231], a[68:71]\n"
"ds_read_b128 a[192:195], v188 offset:8192\n"
"v_add_f32 v184, v184, v131\n"
"v_mfma_f32_16x16x32_bf16 a[72:75], v[152:155], v[236:239], a[72:75]\n"
"v_exp_f32 v132, v132\n"
"v_mfma_f32_16x16x32_bf16 a[72:75], v[156:159], v[240:243], a[72:75]\n"
"ds_read_b128 a[196:199], v189 offset:8192\n"
"v_add_f32 v184, v184, v132\n"
"v_mfma_f32_16x16x32_bf16 a[76:79], v[156:159], v[248:251], a[76:79]\n"
"v_exp_f32 v133, v133\n"
"v_mfma_f32_16x16x32_bf16 a[76:79], v[152:155], v[244:247], a[76:79]\n"
"ds_read_b128 a[200:203], v188 offset:9216\n"
"v_add_f32 v184, v184, v133\n"
"v_mfma_f32_16x16x32_bf16 a[80:83], v[152:155], v[80:83], a[80:83]\n"
"v_exp_f32 v134, v134\n"
"v_cvt_pk_bf16_f32 v146, v116, v117\n"
"v_mfma_f32_16x16x32_bf16 a[80:83], v[156:159], v[84:87], a[80:83]\n"
"v_add_f32 v184, v184, v134\n"
"v_cvt_pk_bf16_f32 v147, v118, v119\n"
"v_mfma_f32_16x16x32_bf16 a[84:87], v[156:159], v[92:95], a[84:87]\n"
"v_exp_f32 v135, v135\n"
"v_mfma_f32_16x16x32_bf16 a[84:87], v[152:155], v[88:91], a[84:87]\n"
"ds_read_b128 a[204:207], v189 offset:9216\n"
"v_add_f32 v184, v184, v135\n"
"v_mfma_f32_16x16x32_bf16 a[88:91], v[152:155], v[96:99], a[88:91]\n"
"v_exp_f32 v136, v136\n"
"v_mfma_f32_16x16x32_bf16 a[88:91], v[156:159], v[100:103], a[88:91]\n"
"ds_read_b128 a[220:223], v189 offset:13312\n"
"v_add_f32 v184, v184, v136\n"
"v_mfma_f32_16x16x32_bf16 a[92:95], v[156:159], v[108:111], a[92:95]\n"
"v_exp_f32 v137, v137\n"
"v_mfma_f32_16x16x32_bf16 a[92:95], v[152:155], v[104:107], a[92:95]\n"
"s_waitcnt lgkmcnt(2)\n"
"ds_read_b128 a[216:219], v188 offset:13312\n"
"v_add_f32 v184, v184, v137\n"
"v_mfma_f32_16x16x32_bf16 v[112:115], a[160:163], v[0:3], v[160:163]\n"
"v_exp_f32 v138, v138\n"
"v_mfma_f32_16x16x32_bf16 v[112:115], a[164:167], v[4:7], v[112:115]\n"
"ds_read_b128 a[212:215], v189 offset:12288\n"
"v_add_f32 v184, v184, v138\n"
"v_mfma_f32_16x16x32_bf16 v[112:115], a[168:171], v[8:11], v[112:115]\n"
"v_exp_f32 v139, v139\n"
"v_mfma_f32_16x16x32_bf16 v[112:115], a[172:175], v[12:15], v[112:115]\n"
"v_add_f32 v184, v184, v139\n"
"v_cvt_pk_bf16_f32 v148, v120, v121\n"
"v_mfma_f32_16x16x32_bf16 v[116:119], a[188:191], v[12:15], v[160:163]\n"
"v_exp_f32 v140, v140\n"
"v_mfma_f32_16x16x32_bf16 v[116:119], a[184:187], v[8:11], v[116:119]\n"
"v_add_f32 v184, v184, v140\n"
"v_cvt_pk_bf16_f32 v149, v122, v123\n"
"v_mfma_f32_16x16x32_bf16 v[116:119], a[180:183], v[4:7], v[116:119]\n"
"ds_read_b128 a[208:211], v188 offset:12288\n"
"v_exp_f32 v141, v141\n"
"v_mfma_f32_16x16x32_bf16 v[116:119], a[176:179], v[0:3], v[116:119]\n"
"v_add_f32 v184, v184, v141\n"
"v_cvt_pk_bf16_f32 v150, v124, v125\n"
"v_mfma_f32_16x16x32_bf16 v[120:123], a[192:195], v[0:3], v[160:163]\n"
"v_exp_f32 v142, v142\n"
"v_mfma_f32_16x16x32_bf16 v[120:123], a[196:199], v[4:7], v[120:123]\n"
"v_add_f32 v184, v184, v142\n"
"v_cvt_pk_bf16_f32 v151, v126, v127\n"
"v_mfma_f32_16x16x32_bf16 v[120:123], a[200:203], v[8:11], v[120:123]\n"
"s_waitcnt lgkmcnt(1)\n"
"v_exp_f32 v143, v143\n"
"v_mfma_f32_16x16x32_bf16 v[120:123], a[204:207], v[12:15], v[120:123]\n"
"v_add_f32 v184, v184, v143\n"
"v_cvt_pk_bf16_f32 v152, v128, v129\n"
"v_mfma_f32_16x16x32_bf16 v[124:127], a[220:223], v[12:15], v[160:163]\n"
"v_exp_f32 v112, v112\n"
"v_mfma_f32_16x16x32_bf16 v[124:127], a[216:219], v[8:11], v[124:127]\n"
"v_cvt_pk_bf16_f32 v153, v130, v131\n"
"v_add_f32 v180, v180, v112\n"
"v_mfma_f32_16x16x32_bf16 v[124:127], a[212:215], v[4:7], v[124:127]\n"
"s_waitcnt lgkmcnt(0)\n"
"ds_read_b64_tr_b16 a[244:245], v196 offset:8192\n"
"v_exp_f32 v113, v113\n"
"v_mfma_f32_16x16x32_bf16 v[124:127], a[208:211], v[0:3], v[124:127]\n"
"v_cvt_pk_bf16_f32 v154, v132, v133\n"
"v_add_f32 v180, v180, v113\n"
"v_mfma_f32_16x16x32_bf16 a[96:99], v[144:147], v[220:223], a[96:99]\n"
"v_exp_f32 v114, v114\n"
"v_mfma_f32_16x16x32_bf16 a[96:99], v[148:151], v[224:227], a[96:99]\n"
"v_cvt_pk_bf16_f32 v155, v134, v135\n"
"v_add_f32 v180, v180, v114\n"
"v_mfma_f32_16x16x32_bf16 a[100:103], v[148:151], v[232:235], a[100:103]\n"
"v_exp_f32 v115, v115\n"
"v_mfma_f32_16x16x32_bf16 a[100:103], v[144:147], v[228:231], a[100:103]\n"
"v_cvt_pk_bf16_f32 v156, v136, v137\n"
"v_add_f32 v180, v180, v115\n"
"v_mfma_f32_16x16x32_bf16 a[104:107], v[144:147], v[236:239], a[104:107]\n"
"ds_read_b64_tr_b16 a[246:247], v196 offset:12288\n"
"v_exp_f32 v116, v116\n"
"v_mfma_f32_16x16x32_bf16 a[104:107], v[148:151], v[240:243], a[104:107]\n"
"v_cvt_pk_bf16_f32 v157, v138, v139\n"
"v_add_f32 v180, v180, v116\n"
"v_mfma_f32_16x16x32_bf16 a[108:111], v[148:151], v[248:251], a[108:111]\n"
"v_exp_f32 v117, v117\n"
"v_mfma_f32_16x16x32_bf16 a[108:111], v[144:147], v[244:247], a[108:111]\n"
"v_cvt_pk_bf16_f32 v158, v140, v141\n"
"v_add_f32 v180, v180, v117\n"
"v_mfma_f32_16x16x32_bf16 a[112:115], v[144:147], v[80:83], a[112:115]\n"
"v_exp_f32 v118, v118\n"
"v_mfma_f32_16x16x32_bf16 a[112:115], v[148:151], v[84:87], a[112:115]\n"
"v_cvt_pk_bf16_f32 v159, v142, v143\n"
"v_add_f32 v180, v180, v118\n"
"v_mfma_f32_16x16x32_bf16 a[116:119], v[148:151], v[92:95], a[116:119]\n"
"ds_read_b64_tr_b16 a[252:253], v197 offset:8192\n"
"v_exp_f32 v119, v119\n"
"v_mfma_f32_16x16x32_bf16 a[116:119], v[144:147], v[88:91], a[116:119]\n"
"ds_read_b64_tr_b16 a[254:255], v197 offset:12288\n"
"v_add_f32 v180, v180, v119\n"
"v_mfma_f32_16x16x32_bf16 a[120:123], v[144:147], v[96:99], a[120:123]\n"
"v_exp_f32 v120, v120\n"
"v_mfma_f32_16x16x32_bf16 a[120:123], v[148:151], v[100:103], a[120:123]\n"
"ds_read_b64_tr_b16 a[248:249], v197 offset:0\n"
"v_add_f32 v180, v180, v120\n"
"v_mfma_f32_16x16x32_bf16 a[124:127], v[148:151], v[108:111], a[124:127]\n"
"v_exp_f32 v121, v121\n"
"v_mfma_f32_16x16x32_bf16 a[124:127], v[144:147], v[104:107], a[124:127]\n"
"ds_read_b64_tr_b16 a[250:251], v197 offset:4096\n"
"v_add_f32 v180, v180, v121\n"
"v_mfma_f32_16x16x32_bf16 v[128:131], a[160:163], v[16:19], v[164:167]\n"
"v_exp_f32 v122, v122\n"
"v_mfma_f32_16x16x32_bf16 v[128:131], a[164:167], v[20:23], v[128:131]\n"
"s_add_u32 m0, s78, 0\n"
"v_add_f32 v180, v180, v122\n"
"v_cvt_pk_bf16_f32 v144, v112, v113\n"
"v_mfma_f32_16x16x32_bf16 v[128:131], a[168:171], v[24:27], v[128:131]\n"
"buffer_load_dwordx4 v198, s[52:55], s75 offen offset:0 nt lds\n"
"v_exp_f32 v123, v123\n"
"v_mfma_f32_16x16x32_bf16 v[128:131], a[172:175], v[28:31], v[128:131]\n"
"v_add_f32 v180, v180, v123\n"
"v_cvt_pk_bf16_f32 v145, v114, v115\n"
"v_mfma_f32_16x16x32_bf16 v[132:135], a[188:191], v[28:31], v[164:167]\n"
"v_exp_f32 v124, v124\n"
"v_mfma_f32_16x16x32_bf16 v[132:135], a[184:187], v[24:27], v[132:135]\n"
"buffer_load_dwordx4 v198, s[52:55], s75 offen offset:2048 nt lds\n"
"v_add_f32 v180, v180, v124\n"
"v_mfma_f32_16x16x32_bf16 v[132:135], a[180:183], v[20:23], v[132:135]\n"
"v_exp_f32 v125, v125\n"
"v_mfma_f32_16x16x32_bf16 v[132:135], a[176:179], v[16:19], v[132:135]\n"
"s_add_u32 m0, s78, 0x380\n"
"v_add_f32 v180, v180, v125\n"
"v_cvt_pk_bf16_f32 v146, v116, v117\n"
"v_mfma_f32_16x16x32_bf16 v[136:139], a[192:195], v[16:19], v[164:167]\n"
"buffer_load_dwordx4 v198, s[52:55], s75 offen offset:128 nt lds\n"
"v_exp_f32 v126, v126\n"
"v_mfma_f32_16x16x32_bf16 v[136:139], a[196:199], v[20:23], v[136:139]\n"
"v_add_f32 v180, v180, v126\n"
"v_cvt_pk_bf16_f32 v147, v118, v119\n"
"v_mfma_f32_16x16x32_bf16 v[136:139], a[200:203], v[24:27], v[136:139]\n"
"v_exp_f32 v127, v127\n"
"v_mfma_f32_16x16x32_bf16 v[136:139], a[204:207], v[28:31], v[136:139]\n"
"buffer_load_dwordx4 v198, s[52:55], s75 offen offset:2176 nt lds\n"
"v_add_f32 v180, v180, v127\n"
"v_mfma_f32_16x16x32_bf16 v[140:143], a[220:223], v[28:31], v[164:167]\n"
"v_exp_f32 v128, v128\n"
"v_mfma_f32_16x16x32_bf16 v[140:143], a[216:219], v[24:27], v[140:143]\n"
"s_add_u32 m0, s78, 0x4000\n"
"v_add_f32 v181, v181, v128\n"
"v_cvt_pk_bf16_f32 v148, v120, v121\n"
"v_mfma_f32_16x16x32_bf16 v[140:143], a[212:215], v[20:23], v[140:143]\n"
"buffer_load_dwordx4 v198, s[56:59], s75 offen offset:0 nt lds\n"
"v_exp_f32 v129, v129\n"
"v_mfma_f32_16x16x32_bf16 v[140:143], a[208:211], v[16:19], v[140:143]\n"
"s_waitcnt lgkmcnt(0)\n"
"v_add_f32 v181, v181, v129\n"
"v_cvt_pk_bf16_f32 v149, v122, v123\n"
"v_mfma_f32_16x16x32_bf16 a[128:131], v[152:155], v[220:223], a[128:131]\n"
"v_exp_f32 v130, v130\n"
"v_mfma_f32_16x16x32_bf16 a[128:131], v[156:159], v[224:227], a[128:131]\n"
"buffer_load_dwordx4 v198, s[56:59], s75 offen offset:2048 nt lds\n"
"v_add_f32 v181, v181, v130\n"
"v_mfma_f32_16x16x32_bf16 a[132:135], v[156:159], v[232:235], a[132:135]\n"
"v_exp_f32 v131, v131\n"
"v_mfma_f32_16x16x32_bf16 a[132:135], v[152:155], v[228:231], a[132:135]\n"
"s_add_u32 m0, s78, 0x4380\n"
"v_add_f32 v181, v181, v131\n"
"v_cvt_pk_bf16_f32 v150, v124, v125\n"
"v_mfma_f32_16x16x32_bf16 a[136:139], v[152:155], v[236:239], a[136:139]\n"
"buffer_load_dwordx4 v198, s[56:59], s75 offen offset:128 nt lds\n"
"v_exp_f32 v132, v132\n"
"v_mfma_f32_16x16x32_bf16 a[136:139], v[156:159], v[240:243], a[136:139]\n"
"v_add_f32 v181, v181, v132\n"
"v_cvt_pk_bf16_f32 v151, v126, v127\n"
"v_mfma_f32_16x16x32_bf16 a[140:143], v[156:159], v[248:251], a[140:143]\n"
"v_exp_f32 v133, v133\n"
"v_mfma_f32_16x16x32_bf16 a[140:143], v[152:155], v[244:247], a[140:143]\n"
"buffer_load_dwordx4 v198, s[56:59], s75 offen offset:2176 nt lds\n"
"v_add_f32 v181, v181, v133\n"
"v_mfma_f32_16x16x32_bf16 a[144:147], v[152:155], v[80:83], a[144:147]\n"
"s_add_u32 s75, s75, 0x4000\n"
"v_exp_f32 v134, v134\n"
"v_mfma_f32_16x16x32_bf16 a[144:147], v[156:159], v[84:87], a[144:147]\n"
"ds_read_b64_tr_b16 v[80:81], v194 offset:1024\n"
"v_add_f32 v181, v181, v134\n"
"v_mfma_f32_16x16x32_bf16 a[148:151], v[156:159], v[92:95], a[148:151]\n"
"v_exp_f32 v135, v135\n"
"v_mfma_f32_16x16x32_bf16 a[148:151], v[152:155], v[88:91], a[148:151]\n"
"ds_read_b64_tr_b16 v[82:83], v194 offset:5120\n"
"v_add_f32 v181, v181, v135\n"
"v_mfma_f32_16x16x32_bf16 a[152:155], v[152:155], v[96:99], a[152:155]\n"
"v_exp_f32 v136, v136\n"
"v_mfma_f32_16x16x32_bf16 a[152:155], v[156:159], v[100:103], a[152:155]\n"
"ds_read_b64_tr_b16 v[84:85], v194 offset:9216\n"
"v_add_f32 v181, v181, v136\n"
"v_mfma_f32_16x16x32_bf16 a[156:159], v[156:159], v[108:111], a[156:159]\n"
"v_exp_f32 v137, v137\n"
"v_mfma_f32_16x16x32_bf16 a[156:159], v[152:155], v[104:107], a[156:159]\n"
"ds_read_b64_tr_b16 v[86:87], v194 offset:13312\n"
"v_add_f32 v181, v181, v137\n"
"v_mfma_f32_16x16x32_bf16 v[112:115], a[160:163], v[32:35], v[168:171]\n"
"v_exp_f32 v138, v138\n"
"v_mfma_f32_16x16x32_bf16 v[112:115], a[164:167], v[36:39], v[112:115]\n"
"ds_read_b64_tr_b16 v[92:93], v195 offset:9216\n"
"v_add_f32 v181, v181, v138\n"
"v_mfma_f32_16x16x32_bf16 v[112:115], a[168:171], v[40:43], v[112:115]\n"
"v_exp_f32 v139, v139\n"
"v_mfma_f32_16x16x32_bf16 v[112:115], a[172:175], v[44:47], v[112:115]\n"
"ds_read_b64_tr_b16 v[94:95], v195 offset:13312\n"
"v_add_f32 v181, v181, v139\n"
"v_mfma_f32_16x16x32_bf16 v[116:119], a[188:191], v[44:47], v[168:171]\n"
"v_exp_f32 v140, v140\n"
"v_mfma_f32_16x16x32_bf16 v[116:119], a[184:187], v[40:43], v[116:119]\n"
"ds_read_b64_tr_b16 v[88:89], v195 offset:1024\n"
"v_add_f32 v181, v181, v140\n"
"v_mfma_f32_16x16x32_bf16 v[116:119], a[180:183], v[36:39], v[116:119]\n"
"v_exp_f32 v141, v141\n"
"v_mfma_f32_16x16x32_bf16 v[116:119], a[176:179], v[32:35], v[116:119]\n"
"ds_read_b64_tr_b16 v[90:91], v195 offset:5120\n"
"v_add_f32 v181, v181, v141\n"
"v_mfma_f32_16x16x32_bf16 v[120:123], a[192:195], v[32:35], v[168:171]\n"
"v_exp_f32 v142, v142\n"
"v_mfma_f32_16x16x32_bf16 v[120:123], a[196:199], v[36:39], v[120:123]\n"
"ds_read_b64_tr_b16 v[96:97], v196 offset:1024\n"
"v_add_f32 v181, v181, v142\n"
"v_mfma_f32_16x16x32_bf16 v[120:123], a[200:203], v[40:43], v[120:123]\n"
"v_exp_f32 v143, v143\n"
"v_mfma_f32_16x16x32_bf16 v[120:123], a[204:207], v[44:47], v[120:123]\n"
"ds_read_b64_tr_b16 v[98:99], v196 offset:5120\n"
"v_add_f32 v181, v181, v143\n"
"v_mfma_f32_16x16x32_bf16 v[124:127], a[220:223], v[44:47], v[168:171]\n"
"v_exp_f32 v112, v112\n"
"v_mfma_f32_16x16x32_bf16 v[124:127], a[216:219], v[40:43], v[124:127]\n"
"ds_read_b64_tr_b16 v[100:101], v196 offset:9216\n"
"v_add_f32 v182, v182, v112\n"
"v_mfma_f32_16x16x32_bf16 v[124:127], a[212:215], v[36:39], v[124:127]\n"
"v_exp_f32 v113, v113\n"
"v_mfma_f32_16x16x32_bf16 v[124:127], a[208:211], v[32:35], v[124:127]\n"
"ds_read_b64_tr_b16 v[102:103], v196 offset:13312\n"
"v_add_f32 v182, v182, v113\n"
"v_mfma_f32_16x16x32_bf16 a[0:3], v[144:147], a[224:227], a[0:3]\n"
"v_exp_f32 v114, v114\n"
"v_mfma_f32_16x16x32_bf16 a[0:3], v[148:151], a[228:231], a[0:3]\n"
"ds_read_b64_tr_b16 v[108:109], v197 offset:9216\n"
"v_add_f32 v182, v182, v114\n"
"v_mfma_f32_16x16x32_bf16 a[4:7], v[148:151], a[236:239], a[4:7]\n"
"v_exp_f32 v115, v115\n"
"v_mfma_f32_16x16x32_bf16 a[4:7], v[144:147], a[232:235], a[4:7]\n"
"ds_read_b64_tr_b16 v[110:111], v197 offset:13312\n"
"v_add_f32 v182, v182, v115\n"
"v_mfma_f32_16x16x32_bf16 a[8:11], v[144:147], a[240:243], a[8:11]\n"
"v_exp_f32 v116, v116\n"
"v_mfma_f32_16x16x32_bf16 a[8:11], v[148:151], a[244:247], a[8:11]\n"
"ds_read_b64_tr_b16 v[104:105], v197 offset:1024\n"
"v_add_f32 v182, v182, v116\n"
"v_mfma_f32_16x16x32_bf16 a[12:15], v[148:151], a[252:255], a[12:15]\n"
"v_exp_f32 v117, v117\n"
"v_mfma_f32_16x16x32_bf16 a[12:15], v[144:147], a[248:251], a[12:15]\n"
"s_waitcnt lgkmcnt(3)\n"
"ds_read_b64_tr_b16 v[106:107], v197 offset:5120\n"
"v_add_f32 v182, v182, v117\n"
"v_mfma_f32_16x16x32_bf16 a[16:19], v[144:147], v[80:83], a[16:19]\n"
"v_exp_f32 v118, v118\n"
"v_mfma_f32_16x16x32_bf16 a[16:19], v[148:151], v[84:87], a[16:19]\n"
"v_add_f32 v182, v182, v118\n"
"v_cvt_pk_bf16_f32 v152, v128, v129\n"
"v_mfma_f32_16x16x32_bf16 a[20:23], v[148:151], v[92:95], a[20:23]\n"
"v_exp_f32 v119, v119\n"
"v_mfma_f32_16x16x32_bf16 a[20:23], v[144:147], v[88:91], a[20:23]\n"
"v_add_f32 v182, v182, v119\n"
"v_cvt_pk_bf16_f32 v153, v130, v131\n"
"v_mfma_f32_16x16x32_bf16 a[24:27], v[144:147], v[96:99], a[24:27]\n"
"v_exp_f32 v120, v120\n"
"v_mfma_f32_16x16x32_bf16 a[24:27], v[148:151], v[100:103], a[24:27]\n"
"s_waitcnt lgkmcnt(1)\n"
"v_add_f32 v182, v182, v120\n"
"v_cvt_pk_bf16_f32 v154, v132, v133\n"
"v_mfma_f32_16x16x32_bf16 a[28:31], v[148:151], v[108:111], a[28:31]\n"
"s_waitcnt lgkmcnt(0)\n"
"v_exp_f32 v121, v121\n"
"v_mfma_f32_16x16x32_bf16 a[28:31], v[144:147], v[104:107], a[28:31]\n"
"v_add_f32 v182, v182, v121\n"
"v_cvt_pk_bf16_f32 v155, v134, v135\n"
"v_mfma_f32_16x16x32_bf16 v[128:131], a[160:163], v[48:51], v[172:175]\n"
"s_waitcnt vmcnt(8)\n"
"v_exp_f32 v122, v122\n"
"v_mfma_f32_16x16x32_bf16 v[128:131], a[164:167], v[52:55], v[128:131]\n"
"s_barrier\n"
"v_add_f32 v182, v182, v122\n"
"v_cvt_pk_bf16_f32 v156, v136, v137\n"
"v_mfma_f32_16x16x32_bf16 v[128:131], a[168:171], v[56:59], v[128:131]\n"
"v_exp_f32 v123, v123\n"
"v_mfma_f32_16x16x32_bf16 v[128:131], a[172:175], v[60:63], v[128:131]\n"
"v_add_f32 v182, v182, v123\n"
"v_cvt_pk_bf16_f32 v157, v138, v139\n"
"v_mfma_f32_16x16x32_bf16 v[132:135], a[188:191], v[60:63], v[172:175]\n"
"ds_read_b64_tr_b16 v[220:221], v194 offset:32768\n"
"v_exp_f32 v124, v124\n"
"v_mfma_f32_16x16x32_bf16 v[132:135], a[184:187], v[56:59], v[132:135]\n"
"v_add_f32 v182, v182, v124\n"
"v_cvt_pk_bf16_f32 v158, v140, v141\n"
"v_mfma_f32_16x16x32_bf16 v[132:135], a[180:183], v[52:55], v[132:135]\n"
"v_exp_f32 v125, v125\n"
"v_mfma_f32_16x16x32_bf16 v[132:135], a[176:179], v[48:51], v[132:135]\n"
"v_add_f32 v182, v182, v125\n"
"v_cvt_pk_bf16_f32 v159, v142, v143\n"
"v_mfma_f32_16x16x32_bf16 v[136:139], a[192:195], v[48:51], v[172:175]\n"
"ds_read_b64_tr_b16 v[222:223], v194 offset:36864\n"
"v_exp_f32 v126, v126\n"
"v_mfma_f32_16x16x32_bf16 v[136:139], a[196:199], v[52:55], v[136:139]\n"
"v_add_f32 v182, v182, v126\n"
"v_cvt_pk_bf16_f32 v144, v112, v113\n"
"v_mfma_f32_16x16x32_bf16 v[136:139], a[200:203], v[56:59], v[136:139]\n"
"v_exp_f32 v127, v127\n"
"v_mfma_f32_16x16x32_bf16 v[136:139], a[204:207], v[60:63], v[136:139]\n"
"v_add_f32 v182, v182, v127\n"
"v_cvt_pk_bf16_f32 v145, v114, v115\n"
"v_mfma_f32_16x16x32_bf16 v[140:143], a[220:223], v[60:63], v[172:175]\n"
"ds_read_b64_tr_b16 v[224:225], v194 offset:40960\n"
"v_exp_f32 v128, v128\n"
"v_mfma_f32_16x16x32_bf16 v[140:143], a[216:219], v[56:59], v[140:143]\n"
"v_add_f32 v183, v183, v128\n"
"v_cvt_pk_bf16_f32 v146, v116, v117\n"
"v_mfma_f32_16x16x32_bf16 v[140:143], a[212:215], v[52:55], v[140:143]\n"
"v_exp_f32 v129, v129\n"
"v_mfma_f32_16x16x32_bf16 v[140:143], a[208:211], v[48:51], v[140:143]\n"
"v_add_f32 v183, v183, v129\n"
"v_cvt_pk_bf16_f32 v147, v118, v119\n"
"v_mfma_f32_16x16x32_bf16 a[32:35], v[152:155], a[224:227], a[32:35]\n"
"ds_read_b64_tr_b16 v[226:227], v194 offset:45056\n"
"v_exp_f32 v130, v130\n"
"v_mfma_f32_16x16x32_bf16 a[32:35], v[156:159], a[228:231], a[32:35]\n"
"v_add_f32 v183, v183, v130\n"
"v_cvt_pk_bf16_f32 v148, v120, v121\n"
"v_mfma_f32_16x16x32_bf16 a[36:39], v[156:159], a[236:239], a[36:39]\n"
"v_exp_f32 v131, v131\n"
"v_mfma_f32_16x16x32_bf16 a[36:39], v[152:155], a[232:235], a[36:39]\n"
"v_add_f32 v183, v183, v131\n"
"v_cvt_pk_bf16_f32 v149, v122, v123\n"
"v_mfma_f32_16x16x32_bf16 a[40:43], v[152:155], a[240:243], a[40:43]\n"
"ds_read_b64_tr_b16 v[232:233], v195 offset:40960\n"
"v_exp_f32 v132, v132\n"
"v_mfma_f32_16x16x32_bf16 a[40:43], v[156:159], a[244:247], a[40:43]\n"
"v_add_f32 v183, v183, v132\n"
"v_cvt_pk_bf16_f32 v150, v124, v125\n"
"v_mfma_f32_16x16x32_bf16 a[44:47], v[156:159], a[252:255], a[44:47]\n"
"v_exp_f32 v133, v133\n"
"v_mfma_f32_16x16x32_bf16 a[44:47], v[152:155], a[248:251], a[44:47]\n"
"v_add_f32 v183, v183, v133\n"
"v_cvt_pk_bf16_f32 v151, v126, v127\n"
"v_mfma_f32_16x16x32_bf16 a[48:51], v[152:155], v[80:83], a[48:51]\n"
"ds_read_b64_tr_b16 v[234:235], v195 offset:45056\n"
"v_exp_f32 v134, v134\n"
"v_mfma_f32_16x16x32_bf16 a[48:51], v[156:159], v[84:87], a[48:51]\n"
"ds_read_b64_tr_b16 v[228:229], v195 offset:32768\n"
"v_add_f32 v183, v183, v134\n"
"v_mfma_f32_16x16x32_bf16 a[52:55], v[156:159], v[92:95], a[52:55]\n"
"v_exp_f32 v135, v135\n"
"v_mfma_f32_16x16x32_bf16 a[52:55], v[152:155], v[88:91], a[52:55]\n"
"ds_read_b64_tr_b16 v[230:231], v195 offset:36864\n"
"v_add_f32 v183, v183, v135\n"
"v_mfma_f32_16x16x32_bf16 a[56:59], v[152:155], v[96:99], a[56:59]\n"
"v_exp_f32 v136, v136\n"
"v_mfma_f32_16x16x32_bf16 a[56:59], v[156:159], v[100:103], a[56:59]\n"
"ds_read_b64_tr_b16 v[236:237], v196 offset:32768\n"
"v_add_f32 v183, v183, v136\n"
"v_mfma_f32_16x16x32_bf16 a[60:63], v[156:159], v[108:111], a[60:63]\n"
"v_exp_f32 v137, v137\n"
"v_mfma_f32_16x16x32_bf16 a[60:63], v[152:155], v[104:107], a[60:63]\n"
"ds_read_b64_tr_b16 v[238:239], v196 offset:36864\n"
"v_add_f32 v183, v183, v137\n"
"v_mfma_f32_16x16x32_bf16 v[112:115], a[160:163], v[64:67], v[176:179]\n"
"v_exp_f32 v138, v138\n"
"v_mfma_f32_16x16x32_bf16 v[112:115], a[164:167], v[68:71], v[112:115]\n"
"ds_read_b128 a[160:163], v188 offset:32768\n"
"v_add_f32 v183, v183, v138\n"
"v_mfma_f32_16x16x32_bf16 v[112:115], a[168:171], v[72:75], v[112:115]\n"
"v_exp_f32 v139, v139\n"
"v_mfma_f32_16x16x32_bf16 v[112:115], a[172:175], v[76:79], v[112:115]\n"
"ds_read_b128 a[164:167], v189 offset:32768\n"
"v_add_f32 v183, v183, v139\n"
"v_mfma_f32_16x16x32_bf16 v[116:119], a[188:191], v[76:79], v[176:179]\n"
"v_exp_f32 v140, v140\n"
"v_mfma_f32_16x16x32_bf16 v[116:119], a[184:187], v[72:75], v[116:119]\n"
"ds_read_b128 a[168:171], v188 offset:33792\n"
"v_add_f32 v183, v183, v140\n"
"v_mfma_f32_16x16x32_bf16 v[116:119], a[180:183], v[68:71], v[116:119]\n"
"v_exp_f32 v141, v141\n"
"v_mfma_f32_16x16x32_bf16 v[116:119], a[176:179], v[64:67], v[116:119]\n"
"ds_read_b128 a[172:175], v189 offset:33792\n"
"v_add_f32 v183, v183, v141\n"
"v_mfma_f32_16x16x32_bf16 v[120:123], a[192:195], v[64:67], v[176:179]\n"
"v_exp_f32 v142, v142\n"
"v_mfma_f32_16x16x32_bf16 v[120:123], a[196:199], v[68:71], v[120:123]\n"
"ds_read_b128 a[188:191], v189 offset:37888\n"
"v_add_f32 v183, v183, v142\n"
"v_mfma_f32_16x16x32_bf16 v[120:123], a[200:203], v[72:75], v[120:123]\n"
"v_exp_f32 v143, v143\n"
"v_mfma_f32_16x16x32_bf16 v[120:123], a[204:207], v[76:79], v[120:123]\n"
"ds_read_b128 a[184:187], v188 offset:37888\n"
"v_add_f32 v183, v183, v143\n"
"v_mfma_f32_16x16x32_bf16 v[124:127], a[220:223], v[76:79], v[176:179]\n"
"ds_read_b128 a[180:183], v189 offset:36864\n"
"v_exp_f32 v112, v112\n"
"v_mfma_f32_16x16x32_bf16 v[124:127], a[216:219], v[72:75], v[124:127]\n"
"v_add_f32 v184, v184, v112\n"
"v_cvt_pk_bf16_f32 v152, v128, v129\n"
"v_mfma_f32_16x16x32_bf16 v[124:127], a[212:215], v[68:71], v[124:127]\n"
"v_exp_f32 v113, v113\n"
"v_mfma_f32_16x16x32_bf16 v[124:127], a[208:211], v[64:67], v[124:127]\n"
"v_add_f32 v184, v184, v113\n"
"v_cvt_pk_bf16_f32 v153, v130, v131\n"
"v_mfma_f32_16x16x32_bf16 a[64:67], v[144:147], a[224:227], a[64:67]\n"
"v_exp_f32 v114, v114\n"
"v_mfma_f32_16x16x32_bf16 a[64:67], v[148:151], a[228:231], a[64:67]\n"
"ds_read_b128 a[176:179], v188 offset:36864\n"
"v_add_f32 v184, v184, v114\n"
"v_mfma_f32_16x16x32_bf16 a[68:71], v[148:151], a[236:239], a[68:71]\n"
"v_exp_f32 v115, v115\n"
"v_mfma_f32_16x16x32_bf16 a[68:71], v[144:147], a[232:235], a[68:71]\n"
"ds_read_b128 a[192:195], v188 offset:40960\n"
"v_add_f32 v184, v184, v115\n"
"v_mfma_f32_16x16x32_bf16 a[72:75], v[144:147], a[240:243], a[72:75]\n"
"v_exp_f32 v116, v116\n"
"v_mfma_f32_16x16x32_bf16 a[72:75], v[148:151], a[244:247], a[72:75]\n"
"ds_read_b128 a[196:199], v189 offset:40960\n"
"v_add_f32 v184, v184, v116\n"
"v_mfma_f32_16x16x32_bf16 a[76:79], v[148:151], a[252:255], a[76:79]\n"
"v_exp_f32 v117, v117\n"
"v_mfma_f32_16x16x32_bf16 a[76:79], v[144:147], a[248:251], a[76:79]\n"
"ds_read_b128 a[200:203], v188 offset:41984\n"
"v_add_f32 v184, v184, v117\n"
"v_mfma_f32_16x16x32_bf16 a[80:83], v[144:147], v[80:83], a[80:83]\n"
"v_exp_f32 v118, v118\n"
"v_cvt_pk_bf16_f32 v154, v132, v133\n"
"v_mfma_f32_16x16x32_bf16 a[80:83], v[148:151], v[84:87], a[80:83]\n"
"v_add_f32 v184, v184, v118\n"
"v_cvt_pk_bf16_f32 v155, v134, v135\n"
"v_mfma_f32_16x16x32_bf16 a[84:87], v[148:151], v[92:95], a[84:87]\n"
"v_exp_f32 v119, v119\n"
"v_mfma_f32_16x16x32_bf16 a[84:87], v[144:147], v[88:91], a[84:87]\n"
"ds_read_b128 a[204:207], v189 offset:41984\n"
"v_add_f32 v184, v184, v119\n"
"v_mfma_f32_16x16x32_bf16 a[88:91], v[144:147], v[96:99], a[88:91]\n"
"v_exp_f32 v120, v120\n"
"v_mfma_f32_16x16x32_bf16 a[88:91], v[148:151], v[100:103], a[88:91]\n"
"ds_read_b128 a[220:223], v189 offset:46080\n"
"v_add_f32 v184, v184, v120\n"
"v_mfma_f32_16x16x32_bf16 a[92:95], v[148:151], v[108:111], a[92:95]\n"
"v_exp_f32 v121, v121\n"
"v_mfma_f32_16x16x32_bf16 a[92:95], v[144:147], v[104:107], a[92:95]\n"
"s_waitcnt lgkmcnt(2)\n"
"ds_read_b128 a[216:219], v188 offset:46080\n"
"v_add_f32 v184, v184, v121\n"
"v_mfma_f32_16x16x32_bf16 v[128:131], a[160:163], v[0:3], v[160:163]\n"
"v_exp_f32 v122, v122\n"
"v_mfma_f32_16x16x32_bf16 v[128:131], a[164:167], v[4:7], v[128:131]\n"
"ds_read_b128 a[212:215], v189 offset:45056\n"
"v_add_f32 v184, v184, v122\n"
"v_mfma_f32_16x16x32_bf16 v[128:131], a[168:171], v[8:11], v[128:131]\n"
"v_exp_f32 v123, v123\n"
"v_mfma_f32_16x16x32_bf16 v[128:131], a[172:175], v[12:15], v[128:131]\n"
"v_add_f32 v184, v184, v123\n"
"v_cvt_pk_bf16_f32 v156, v136, v137\n"
"v_mfma_f32_16x16x32_bf16 v[132:135], a[188:191], v[12:15], v[160:163]\n"
"v_exp_f32 v124, v124\n"
"v_mfma_f32_16x16x32_bf16 v[132:135], a[184:187], v[8:11], v[132:135]\n"
"v_add_f32 v184, v184, v124\n"
"v_cvt_pk_bf16_f32 v157, v138, v139\n"
"v_mfma_f32_16x16x32_bf16 v[132:135], a[180:183], v[4:7], v[132:135]\n"
"ds_read_b128 a[208:211], v188 offset:45056\n"
"v_exp_f32 v125, v125\n"
"v_mfma_f32_16x16x32_bf16 v[132:135], a[176:179], v[0:3], v[132:135]\n"
"v_add_f32 v184, v184, v125\n"
"v_cvt_pk_bf16_f32 v158, v140, v141\n"
"v_mfma_f32_16x16x32_bf16 v[136:139], a[192:195], v[0:3], v[160:163]\n"
"v_exp_f32 v126, v126\n"
"v_mfma_f32_16x16x32_bf16 v[136:139], a[196:199], v[4:7], v[136:139]\n"
"v_add_f32 v184, v184, v126\n"
"v_cvt_pk_bf16_f32 v159, v142, v143\n"
"v_mfma_f32_16x16x32_bf16 v[136:139], a[200:203], v[8:11], v[136:139]\n"
"s_waitcnt lgkmcnt(1)\n"
"v_exp_f32 v127, v127\n"
"v_mfma_f32_16x16x32_bf16 v[136:139], a[204:207], v[12:15], v[136:139]\n"
"v_add_f32 v184, v184, v127\n"
"v_cvt_pk_bf16_f32 v144, v112, v113\n"
"v_mfma_f32_16x16x32_bf16 v[140:143], a[220:223], v[12:15], v[160:163]\n"
"v_exp_f32 v128, v128\n"
"v_mfma_f32_16x16x32_bf16 v[140:143], a[216:219], v[8:11], v[140:143]\n"
"v_cvt_pk_bf16_f32 v145, v114, v115\n"
"v_add_f32 v180, v180, v128\n"
"v_mfma_f32_16x16x32_bf16 v[140:143], a[212:215], v[4:7], v[140:143]\n"
"s_waitcnt lgkmcnt(0)\n"
"ds_read_b64_tr_b16 v[240:241], v196 offset:40960\n"
"v_exp_f32 v129, v129\n"
"v_mfma_f32_16x16x32_bf16 v[140:143], a[208:211], v[0:3], v[140:143]\n"
"v_cvt_pk_bf16_f32 v146, v116, v117\n"
"v_add_f32 v180, v180, v129\n"
"v_mfma_f32_16x16x32_bf16 a[96:99], v[152:155], a[224:227], a[96:99]\n"
"v_exp_f32 v130, v130\n"
"v_mfma_f32_16x16x32_bf16 a[96:99], v[156:159], a[228:231], a[96:99]\n"
"v_cvt_pk_bf16_f32 v147, v118, v119\n"
"v_add_f32 v180, v180, v130\n"
"v_mfma_f32_16x16x32_bf16 a[100:103], v[156:159], a[236:239], a[100:103]\n"
"v_exp_f32 v131, v131\n"
"v_mfma_f32_16x16x32_bf16 a[100:103], v[152:155], a[232:235], a[100:103]\n"
"v_cvt_pk_bf16_f32 v148, v120, v121\n"
"v_add_f32 v180, v180, v131\n"
"v_mfma_f32_16x16x32_bf16 a[104:107], v[152:155], a[240:243], a[104:107]\n"
"ds_read_b64_tr_b16 v[242:243], v196 offset:45056\n"
"v_exp_f32 v132, v132\n"
"v_mfma_f32_16x16x32_bf16 a[104:107], v[156:159], a[244:247], a[104:107]\n"
"v_cvt_pk_bf16_f32 v149, v122, v123\n"
"v_add_f32 v180, v180, v132\n"
"v_mfma_f32_16x16x32_bf16 a[108:111], v[156:159], a[252:255], a[108:111]\n"
"v_exp_f32 v133, v133\n"
"v_mfma_f32_16x16x32_bf16 a[108:111], v[152:155], a[248:251], a[108:111]\n"
"v_cvt_pk_bf16_f32 v150, v124, v125\n"
"v_add_f32 v180, v180, v133\n"
"v_mfma_f32_16x16x32_bf16 a[112:115], v[152:155], v[80:83], a[112:115]\n"
"v_exp_f32 v134, v134\n"
"v_mfma_f32_16x16x32_bf16 a[112:115], v[156:159], v[84:87], a[112:115]\n"
"v_cvt_pk_bf16_f32 v151, v126, v127\n"
"v_add_f32 v180, v180, v134\n"
"v_mfma_f32_16x16x32_bf16 a[116:119], v[156:159], v[92:95], a[116:119]\n"
"ds_read_b64_tr_b16 v[248:249], v197 offset:40960\n"
"v_exp_f32 v135, v135\n"
"v_mfma_f32_16x16x32_bf16 a[116:119], v[152:155], v[88:91], a[116:119]\n"
"ds_read_b64_tr_b16 v[250:251], v197 offset:45056\n"
"v_add_f32 v180, v180, v135\n"
"v_mfma_f32_16x16x32_bf16 a[120:123], v[152:155], v[96:99], a[120:123]\n"
"v_exp_f32 v136, v136\n"
"v_mfma_f32_16x16x32_bf16 a[120:123], v[156:159], v[100:103], a[120:123]\n"
"ds_read_b64_tr_b16 v[244:245], v197 offset:32768\n"
"v_add_f32 v180, v180, v136\n"
"v_mfma_f32_16x16x32_bf16 a[124:127], v[156:159], v[108:111], a[124:127]\n"
"v_exp_f32 v137, v137\n"
"v_mfma_f32_16x16x32_bf16 a[124:127], v[152:155], v[104:107], a[124:127]\n"
"ds_read_b64_tr_b16 v[246:247], v197 offset:36864\n"
"v_add_f32 v180, v180, v137\n"
"v_mfma_f32_16x16x32_bf16 v[112:115], a[160:163], v[16:19], v[164:167]\n"
"v_exp_f32 v138, v138\n"
"v_mfma_f32_16x16x32_bf16 v[112:115], a[164:167], v[20:23], v[112:115]\n"
"s_add_u32 m0, s78, 0x8000\n"
"v_add_f32 v180, v180, v138\n"
"v_cvt_pk_bf16_f32 v152, v128, v129\n"
"v_mfma_f32_16x16x32_bf16 v[112:115], a[168:171], v[24:27], v[112:115]\n"
"buffer_load_dwordx4 v198, s[52:55], s75 offen offset:0 nt lds\n"
"v_exp_f32 v139, v139\n"
"v_mfma_f32_16x16x32_bf16 v[112:115], a[172:175], v[28:31], v[112:115]\n"
"v_add_f32 v180, v180, v139\n"
"v_cvt_pk_bf16_f32 v153, v130, v131\n"
"v_mfma_f32_16x16x32_bf16 v[116:119], a[188:191], v[28:31], v[164:167]\n"
"v_exp_f32 v140, v140\n"
"v_mfma_f32_16x16x32_bf16 v[116:119], a[184:187], v[24:27], v[116:119]\n"
"buffer_load_dwordx4 v198, s[52:55], s75 offen offset:2048 nt lds\n"
"v_add_f32 v180, v180, v140\n"
"v_mfma_f32_16x16x32_bf16 v[116:119], a[180:183], v[20:23], v[116:119]\n"
"v_exp_f32 v141, v141\n"
"v_mfma_f32_16x16x32_bf16 v[116:119], a[176:179], v[16:19], v[116:119]\n"
"s_add_u32 m0, s78, 0x8380\n"
"v_add_f32 v180, v180, v141\n"
"v_cvt_pk_bf16_f32 v154, v132, v133\n"
"v_mfma_f32_16x16x32_bf16 v[120:123], a[192:195], v[16:19], v[164:167]\n"
"buffer_load_dwordx4 v198, s[52:55], s75 offen offset:128 nt lds\n"
"v_exp_f32 v142, v142\n"
"v_mfma_f32_16x16x32_bf16 v[120:123], a[196:199], v[20:23], v[120:123]\n"
"v_add_f32 v180, v180, v142\n"
"v_cvt_pk_bf16_f32 v155, v134, v135\n"
"v_mfma_f32_16x16x32_bf16 v[120:123], a[200:203], v[24:27], v[120:123]\n"
"v_exp_f32 v143, v143\n"
"v_mfma_f32_16x16x32_bf16 v[120:123], a[204:207], v[28:31], v[120:123]\n"
"buffer_load_dwordx4 v198, s[52:55], s75 offen offset:2176 nt lds\n"
"v_add_f32 v180, v180, v143\n"
"v_mfma_f32_16x16x32_bf16 v[124:127], a[220:223], v[28:31], v[164:167]\n"
"v_exp_f32 v112, v112\n"
"v_mfma_f32_16x16x32_bf16 v[124:127], a[216:219], v[24:27], v[124:127]\n"
"s_add_u32 m0, s78, 0xc000\n"
"v_add_f32 v181, v181, v112\n"
"v_cvt_pk_bf16_f32 v156, v136, v137\n"
"v_mfma_f32_16x16x32_bf16 v[124:127], a[212:215], v[20:23], v[124:127]\n"
"buffer_load_dwordx4 v198, s[56:59], s75 offen offset:0 nt lds\n"
"v_exp_f32 v113, v113\n"
"v_mfma_f32_16x16x32_bf16 v[124:127], a[208:211], v[16:19], v[124:127]\n"
"s_waitcnt lgkmcnt(0)\n"
"v_add_f32 v181, v181, v113\n"
"v_cvt_pk_bf16_f32 v157, v138, v139\n"
"v_mfma_f32_16x16x32_bf16 a[128:131], v[144:147], a[224:227], a[128:131]\n"
"v_exp_f32 v114, v114\n"
"v_mfma_f32_16x16x32_bf16 a[128:131], v[148:151], a[228:231], a[128:131]\n"
"buffer_load_dwordx4 v198, s[56:59], s75 offen offset:2048 nt lds\n"
"v_add_f32 v181, v181, v114\n"
"v_mfma_f32_16x16x32_bf16 a[132:135], v[148:151], a[236:239], a[132:135]\n"
"v_exp_f32 v115, v115\n"
"v_mfma_f32_16x16x32_bf16 a[132:135], v[144:147], a[232:235], a[132:135]\n"
"s_add_u32 m0, s78, 0xc380\n"
"v_add_f32 v181, v181, v115\n"
"v_cvt_pk_bf16_f32 v158, v140, v141\n"
"v_mfma_f32_16x16x32_bf16 a[136:139], v[144:147], a[240:243], a[136:139]\n"
"buffer_load_dwordx4 v198, s[56:59], s75 offen offset:128 nt lds\n"
"v_exp_f32 v116, v116\n"
"v_mfma_f32_16x16x32_bf16 a[136:139], v[148:151], a[244:247], a[136:139]\n"
"v_add_f32 v181, v181, v116\n"
"v_cvt_pk_bf16_f32 v159, v142, v143\n"
"v_mfma_f32_16x16x32_bf16 a[140:143], v[148:151], a[252:255], a[140:143]\n"
"v_exp_f32 v117, v117\n"
"v_mfma_f32_16x16x32_bf16 a[140:143], v[144:147], a[248:251], a[140:143]\n"
"buffer_load_dwordx4 v198, s[56:59], s75 offen offset:2176 nt lds\n"
"v_add_f32 v181, v181, v117\n"
"v_mfma_f32_16x16x32_bf16 a[144:147], v[144:147], v[80:83], a[144:147]\n"
"s_add_u32 s75, s75, 0x4000\n"
"v_exp_f32 v118, v118\n"
"v_mfma_f32_16x16x32_bf16 a[144:147], v[148:151], v[84:87], a[144:147]\n"
"ds_read_b64_tr_b16 v[80:81], v194 offset:33792\n"
"v_add_f32 v181, v181, v118\n"
"v_mfma_f32_16x16x32_bf16 a[148:151], v[148:151], v[92:95], a[148:151]\n"
"v_exp_f32 v119, v119\n"
"v_mfma_f32_16x16x32_bf16 a[148:151], v[144:147], v[88:91], a[148:151]\n"
"ds_read_b64_tr_b16 v[82:83], v194 offset:37888\n"
"v_add_f32 v181, v181, v119\n"
"v_mfma_f32_16x16x32_bf16 a[152:155], v[144:147], v[96:99], a[152:155]\n"
"v_exp_f32 v120, v120\n"
"v_mfma_f32_16x16x32_bf16 a[152:155], v[148:151], v[100:103], a[152:155]\n"
"ds_read_b64_tr_b16 v[84:85], v194 offset:41984\n"
"v_add_f32 v181, v181, v120\n"
"v_mfma_f32_16x16x32_bf16 a[156:159], v[148:151], v[108:111], a[156:159]\n"
"v_exp_f32 v121, v121\n"
"v_mfma_f32_16x16x32_bf16 a[156:159], v[144:147], v[104:107], a[156:159]\n"
"ds_read_b64_tr_b16 v[86:87], v194 offset:46080\n"
"v_add_f32 v181, v181, v121\n"
"v_mfma_f32_16x16x32_bf16 v[128:131], a[160:163], v[32:35], v[168:171]\n"
"v_exp_f32 v122, v122\n"
"v_mfma_f32_16x16x32_bf16 v[128:131], a[164:167], v[36:39], v[128:131]\n"
"ds_read_b64_tr_b16 v[92:93], v195 offset:41984\n"
"v_add_f32 v181, v181, v122\n"
"v_mfma_f32_16x16x32_bf16 v[128:131], a[168:171], v[40:43], v[128:131]\n"
"v_exp_f32 v123, v123\n"
"v_mfma_f32_16x16x32_bf16 v[128:131], a[172:175], v[44:47], v[128:131]\n"
"ds_read_b64_tr_b16 v[94:95], v195 offset:46080\n"
"v_add_f32 v181, v181, v123\n"
"v_mfma_f32_16x16x32_bf16 v[132:135], a[188:191], v[44:47], v[168:171]\n"
"v_exp_f32 v124, v124\n"
"v_mfma_f32_16x16x32_bf16 v[132:135], a[184:187], v[40:43], v[132:135]\n"
"ds_read_b64_tr_b16 v[88:89], v195 offset:33792\n"
"v_add_f32 v181, v181, v124\n"
"v_mfma_f32_16x16x32_bf16 v[132:135], a[180:183], v[36:39], v[132:135]\n"
"v_exp_f32 v125, v125\n"
"v_mfma_f32_16x16x32_bf16 v[132:135], a[176:179], v[32:35], v[132:135]\n"
"ds_read_b64_tr_b16 v[90:91], v195 offset:37888\n"
"v_add_f32 v181, v181, v125\n"
"v_mfma_f32_16x16x32_bf16 v[136:139], a[192:195], v[32:35], v[168:171]\n"
"v_exp_f32 v126, v126\n"
"v_mfma_f32_16x16x32_bf16 v[136:139], a[196:199], v[36:39], v[136:139]\n"
"ds_read_b64_tr_b16 v[96:97], v196 offset:33792\n"
"v_add_f32 v181, v181, v126\n"
"v_mfma_f32_16x16x32_bf16 v[136:139], a[200:203], v[40:43], v[136:139]\n"
"v_exp_f32 v127, v127\n"
"v_mfma_f32_16x16x32_bf16 v[136:139], a[204:207], v[44:47], v[136:139]\n"
"ds_read_b64_tr_b16 v[98:99], v196 offset:37888\n"
"v_add_f32 v181, v181, v127\n"
"v_mfma_f32_16x16x32_bf16 v[140:143], a[220:223], v[44:47], v[168:171]\n"
"v_exp_f32 v128, v128\n"
"v_mfma_f32_16x16x32_bf16 v[140:143], a[216:219], v[40:43], v[140:143]\n"
"ds_read_b64_tr_b16 v[100:101], v196 offset:41984\n"
"v_add_f32 v182, v182, v128\n"
"v_mfma_f32_16x16x32_bf16 v[140:143], a[212:215], v[36:39], v[140:143]\n"
"v_exp_f32 v129, v129\n"
"v_mfma_f32_16x16x32_bf16 v[140:143], a[208:211], v[32:35], v[140:143]\n"
"ds_read_b64_tr_b16 v[102:103], v196 offset:46080\n"
"v_add_f32 v182, v182, v129\n"
"v_mfma_f32_16x16x32_bf16 a[0:3], v[152:155], v[220:223], a[0:3]\n"
"v_exp_f32 v130, v130\n"
"v_mfma_f32_16x16x32_bf16 a[0:3], v[156:159], v[224:227], a[0:3]\n"
"ds_read_b64_tr_b16 v[108:109], v197 offset:41984\n"
"v_add_f32 v182, v182, v130\n"
"v_mfma_f32_16x16x32_bf16 a[4:7], v[156:159], v[232:235], a[4:7]\n"
"v_exp_f32 v131, v131\n"
"v_mfma_f32_16x16x32_bf16 a[4:7], v[152:155], v[228:231], a[4:7]\n"
"ds_read_b64_tr_b16 v[110:111], v197 offset:46080\n"
"v_add_f32 v182, v182, v131\n"
"v_mfma_f32_16x16x32_bf16 a[8:11], v[152:155], v[236:239], a[8:11]\n"
"v_exp_f32 v132, v132\n"
"v_mfma_f32_16x16x32_bf16 a[8:11], v[156:159], v[240:243], a[8:11]\n"
"ds_read_b64_tr_b16 v[104:105], v197 offset:33792\n"
"v_add_f32 v182, v182, v132\n"
"v_mfma_f32_16x16x32_bf16 a[12:15], v[156:159], v[248:251], a[12:15]\n"
"v_exp_f32 v133, v133\n"
"v_mfma_f32_16x16x32_bf16 a[12:15], v[152:155], v[244:247], a[12:15]\n"
"s_waitcnt lgkmcnt(3)\n"
"ds_read_b64_tr_b16 v[106:107], v197 offset:37888\n"
"v_add_f32 v182, v182, v133\n"
"v_mfma_f32_16x16x32_bf16 a[16:19], v[152:155], v[80:83], a[16:19]\n"
"v_exp_f32 v134, v134\n"
"v_mfma_f32_16x16x32_bf16 a[16:19], v[156:159], v[84:87], a[16:19]\n"
"v_add_f32 v182, v182, v134\n"
"v_cvt_pk_bf16_f32 v144, v112, v113\n"
"v_mfma_f32_16x16x32_bf16 a[20:23], v[156:159], v[92:95], a[20:23]\n"
"v_exp_f32 v135, v135\n"
"v_mfma_f32_16x16x32_bf16 a[20:23], v[152:155], v[88:91], a[20:23]\n"
"v_add_f32 v182, v182, v135\n"
"v_cvt_pk_bf16_f32 v145, v114, v115\n"
"v_mfma_f32_16x16x32_bf16 a[24:27], v[152:155], v[96:99], a[24:27]\n"
"v_exp_f32 v136, v136\n"
"v_mfma_f32_16x16x32_bf16 a[24:27], v[156:159], v[100:103], a[24:27]\n"
"s_waitcnt lgkmcnt(1)\n"
"v_add_f32 v182, v182, v136\n"
"v_cvt_pk_bf16_f32 v146, v116, v117\n"
"v_mfma_f32_16x16x32_bf16 a[28:31], v[156:159], v[108:111], a[28:31]\n"
"s_waitcnt lgkmcnt(0)\n"
"v_exp_f32 v137, v137\n"
"v_mfma_f32_16x16x32_bf16 a[28:31], v[152:155], v[104:107], a[28:31]\n"
"v_add_f32 v182, v182, v137\n"
"v_cvt_pk_bf16_f32 v147, v118, v119\n"
"v_mfma_f32_16x16x32_bf16 v[112:115], a[160:163], v[48:51], v[172:175]\n"
"s_waitcnt vmcnt(8)\n"
"v_exp_f32 v138, v138\n"
"v_mfma_f32_16x16x32_bf16 v[112:115], a[164:167], v[52:55], v[112:115]\n"
"s_barrier\n"
"v_add_f32 v182, v182, v138\n"
"v_cvt_pk_bf16_f32 v148, v120, v121\n"
"v_mfma_f32_16x16x32_bf16 v[112:115], a[168:171], v[56:59], v[112:115]\n"
"v_exp_f32 v139, v139\n"
"v_mfma_f32_16x16x32_bf16 v[112:115], a[172:175], v[60:63], v[112:115]\n"
"v_add_f32 v182, v182, v139\n"
"v_cvt_pk_bf16_f32 v149, v122, v123\n"
"v_mfma_f32_16x16x32_bf16 v[116:119], a[188:191], v[60:63], v[172:175]\n"
"ds_read_b64_tr_b16 a[224:225], v190 offset:0\n"
"v_exp_f32 v140, v140\n"
"v_mfma_f32_16x16x32_bf16 v[116:119], a[184:187], v[56:59], v[116:119]\n"
"v_add_f32 v182, v182, v140\n"
"v_cvt_pk_bf16_f32 v150, v124, v125\n"
"v_mfma_f32_16x16x32_bf16 v[116:119], a[180:183], v[52:55], v[116:119]\n"
"v_exp_f32 v141, v141\n"
"v_mfma_f32_16x16x32_bf16 v[116:119], a[176:179], v[48:51], v[116:119]\n"
"v_add_f32 v182, v182, v141\n"
"v_cvt_pk_bf16_f32 v151, v126, v127\n"
"v_mfma_f32_16x16x32_bf16 v[120:123], a[192:195], v[48:51], v[172:175]\n"
"ds_read_b64_tr_b16 a[226:227], v190 offset:4096\n"
"v_exp_f32 v142, v142\n"
"v_mfma_f32_16x16x32_bf16 v[120:123], a[196:199], v[52:55], v[120:123]\n"
"v_add_f32 v182, v182, v142\n"
"v_cvt_pk_bf16_f32 v152, v128, v129\n"
"v_mfma_f32_16x16x32_bf16 v[120:123], a[200:203], v[56:59], v[120:123]\n"
"v_exp_f32 v143, v143\n"
"v_mfma_f32_16x16x32_bf16 v[120:123], a[204:207], v[60:63], v[120:123]\n"
"v_add_f32 v182, v182, v143\n"
"v_cvt_pk_bf16_f32 v153, v130, v131\n"
"v_mfma_f32_16x16x32_bf16 v[124:127], a[220:223], v[60:63], v[172:175]\n"
"ds_read_b64_tr_b16 a[228:229], v190 offset:8192\n"
"v_exp_f32 v112, v112\n"
"v_mfma_f32_16x16x32_bf16 v[124:127], a[216:219], v[56:59], v[124:127]\n"
"v_add_f32 v183, v183, v112\n"
"v_cvt_pk_bf16_f32 v154, v132, v133\n"
"v_mfma_f32_16x16x32_bf16 v[124:127], a[212:215], v[52:55], v[124:127]\n"
"v_exp_f32 v113, v113\n"
"v_mfma_f32_16x16x32_bf16 v[124:127], a[208:211], v[48:51], v[124:127]\n"
"v_add_f32 v183, v183, v113\n"
"v_cvt_pk_bf16_f32 v155, v134, v135\n"
"v_mfma_f32_16x16x32_bf16 a[32:35], v[144:147], v[220:223], a[32:35]\n"
"ds_read_b64_tr_b16 a[230:231], v190 offset:12288\n"
"v_exp_f32 v114, v114\n"
"v_mfma_f32_16x16x32_bf16 a[32:35], v[148:151], v[224:227], a[32:35]\n"
"v_add_f32 v183, v183, v114\n"
"v_cvt_pk_bf16_f32 v156, v136, v137\n"
"v_mfma_f32_16x16x32_bf16 a[36:39], v[148:151], v[232:235], a[36:39]\n"
"v_exp_f32 v115, v115\n"
"v_mfma_f32_16x16x32_bf16 a[36:39], v[144:147], v[228:231], a[36:39]\n"
"v_add_f32 v183, v183, v115\n"
"v_cvt_pk_bf16_f32 v157, v138, v139\n"
"v_mfma_f32_16x16x32_bf16 a[40:43], v[144:147], v[236:239], a[40:43]\n"
"ds_read_b64_tr_b16 a[236:237], v191 offset:8192\n"
"v_exp_f32 v116, v116\n"
"v_mfma_f32_16x16x32_bf16 a[40:43], v[148:151], v[240:243], a[40:43]\n"
"v_add_f32 v183, v183, v116\n"
"v_cvt_pk_bf16_f32 v158, v140, v141\n"
"v_mfma_f32_16x16x32_bf16 a[44:47], v[148:151], v[248:251], a[44:47]\n"
"v_exp_f32 v117, v117\n"
"v_mfma_f32_16x16x32_bf16 a[44:47], v[144:147], v[244:247], a[44:47]\n"
"v_add_f32 v183, v183, v117\n"
"v_cvt_pk_bf16_f32 v159, v142, v143\n"
"v_mfma_f32_16x16x32_bf16 a[48:51], v[144:147], v[80:83], a[48:51]\n"
"ds_read_b64_tr_b16 a[238:239], v191 offset:12288\n"
"v_exp_f32 v118, v118\n"
"v_mfma_f32_16x16x32_bf16 a[48:51], v[148:151], v[84:87], a[48:51]\n"
"ds_read_b64_tr_b16 a[232:233], v191 offset:0\n"
"v_add_f32 v183, v183, v118\n"
"v_mfma_f32_16x16x32_bf16 a[52:55], v[148:151], v[92:95], a[52:55]\n"
"v_exp_f32 v119, v119\n"
"v_mfma_f32_16x16x32_bf16 a[52:55], v[144:147], v[88:91], a[52:55]\n"
"ds_read_b64_tr_b16 a[234:235], v191 offset:4096\n"
"v_add_f32 v183, v183, v119\n"
"v_mfma_f32_16x16x32_bf16 a[56:59], v[144:147], v[96:99], a[56:59]\n"
"v_exp_f32 v120, v120\n"
"v_mfma_f32_16x16x32_bf16 a[56:59], v[148:151], v[100:103], a[56:59]\n"
"ds_read_b64_tr_b16 a[240:241], v192 offset:0\n"
"v_add_f32 v183, v183, v120\n"
"v_mfma_f32_16x16x32_bf16 a[60:63], v[148:151], v[108:111], a[60:63]\n"
"v_exp_f32 v121, v121\n"
"v_mfma_f32_16x16x32_bf16 a[60:63], v[144:147], v[104:107], a[60:63]\n"
"ds_read_b64_tr_b16 a[242:243], v192 offset:4096\n"
"v_add_f32 v183, v183, v121\n"
"v_mfma_f32_16x16x32_bf16 v[128:131], a[160:163], v[64:67], v[176:179]\n"
"v_exp_f32 v122, v122\n"
"v_mfma_f32_16x16x32_bf16 v[128:131], a[164:167], v[68:71], v[128:131]\n"
"ds_read_b128 a[160:163], v186 offset:0\n"
"v_add_f32 v183, v183, v122\n"
"v_mfma_f32_16x16x32_bf16 v[128:131], a[168:171], v[72:75], v[128:131]\n"
"v_exp_f32 v123, v123\n"
"v_mfma_f32_16x16x32_bf16 v[128:131], a[172:175], v[76:79], v[128:131]\n"
"ds_read_b128 a[164:167], v187 offset:0\n"
"v_add_f32 v183, v183, v123\n"
"v_mfma_f32_16x16x32_bf16 v[132:135], a[188:191], v[76:79], v[176:179]\n"
"v_exp_f32 v124, v124\n"
"v_mfma_f32_16x16x32_bf16 v[132:135], a[184:187], v[72:75], v[132:135]\n"
"ds_read_b128 a[168:171], v186 offset:1024\n"
"v_add_f32 v183, v183, v124\n"
"v_mfma_f32_16x16x32_bf16 v[132:135], a[180:183], v[68:71], v[132:135]\n"
"v_exp_f32 v125, v125\n"
"v_mfma_f32_16x16x32_bf16 v[132:135], a[176:179], v[64:67], v[132:135]\n"
"ds_read_b128 a[172:175], v187 offset:1024\n"
"v_add_f32 v183, v183, v125\n"
"v_mfma_f32_16x16x32_bf16 v[136:139], a[192:195], v[64:67], v[176:179]\n"
"v_exp_f32 v126, v126\n"
"v_mfma_f32_16x16x32_bf16 v[136:139], a[196:199], v[68:71], v[136:139]\n"
"ds_read_b128 a[188:191], v187 offset:5120\n"
"v_add_f32 v183, v183, v126\n"
"v_mfma_f32_16x16x32_bf16 v[136:139], a[200:203], v[72:75], v[136:139]\n"
"v_exp_f32 v127, v127\n"
"v_mfma_f32_16x16x32_bf16 v[136:139], a[204:207], v[76:79], v[136:139]\n"
"ds_read_b128 a[184:187], v186 offset:5120\n"
"v_add_f32 v183, v183, v127\n"
"v_mfma_f32_16x16x32_bf16 v[140:143], a[220:223], v[76:79], v[176:179]\n"
"ds_read_b128 a[180:183], v187 offset:4096\n"
"v_exp_f32 v128, v128\n"
"v_mfma_f32_16x16x32_bf16 v[140:143], a[216:219], v[72:75], v[140:143]\n"
"v_add_f32 v184, v184, v128\n"
"v_cvt_pk_bf16_f32 v144, v112, v113\n"
"v_mfma_f32_16x16x32_bf16 v[140:143], a[212:215], v[68:71], v[140:143]\n"
"v_exp_f32 v129, v129\n"
"v_mfma_f32_16x16x32_bf16 v[140:143], a[208:211], v[64:67], v[140:143]\n"
"v_add_f32 v184, v184, v129\n"
"v_cvt_pk_bf16_f32 v145, v114, v115\n"
"v_mfma_f32_16x16x32_bf16 a[64:67], v[152:155], v[220:223], a[64:67]\n"
"v_exp_f32 v130, v130\n"
"v_mfma_f32_16x16x32_bf16 a[64:67], v[156:159], v[224:227], a[64:67]\n"
"ds_read_b128 a[176:179], v186 offset:4096\n"
"v_add_f32 v184, v184, v130\n"
"v_mfma_f32_16x16x32_bf16 a[68:71], v[156:159], v[232:235], a[68:71]\n"
"v_exp_f32 v131, v131\n"
"v_mfma_f32_16x16x32_bf16 a[68:71], v[152:155], v[228:231], a[68:71]\n"
"ds_read_b128 a[192:195], v186 offset:8192\n"
"v_add_f32 v184, v184, v131\n"
"v_mfma_f32_16x16x32_bf16 a[72:75], v[152:155], v[236:239], a[72:75]\n"
"v_exp_f32 v132, v132\n"
"v_mfma_f32_16x16x32_bf16 a[72:75], v[156:159], v[240:243], a[72:75]\n"
"ds_read_b128 a[196:199], v187 offset:8192\n"
"v_add_f32 v184, v184, v132\n"
"v_mfma_f32_16x16x32_bf16 a[76:79], v[156:159], v[248:251], a[76:79]\n"
"v_exp_f32 v133, v133\n"
"v_mfma_f32_16x16x32_bf16 a[76:79], v[152:155], v[244:247], a[76:79]\n"
"ds_read_b128 a[200:203], v186 offset:9216\n"
"v_add_f32 v184, v184, v133\n"
"v_mfma_f32_16x16x32_bf16 a[80:83], v[152:155], v[80:83], a[80:83]\n"
"v_exp_f32 v134, v134\n"
"v_cvt_pk_bf16_f32 v146, v116, v117\n"
"v_mfma_f32_16x16x32_bf16 a[80:83], v[156:159], v[84:87], a[80:83]\n"
"v_add_f32 v184, v184, v134\n"
"v_cvt_pk_bf16_f32 v147, v118, v119\n"
"v_mfma_f32_16x16x32_bf16 a[84:87], v[156:159], v[92:95], a[84:87]\n"
"v_exp_f32 v135, v135\n"
"v_mfma_f32_16x16x32_bf16 a[84:87], v[152:155], v[88:91], a[84:87]\n"
"ds_read_b128 a[204:207], v187 offset:9216\n"
"v_add_f32 v184, v184, v135\n"
"v_mfma_f32_16x16x32_bf16 a[88:91], v[152:155], v[96:99], a[88:91]\n"
"v_exp_f32 v136, v136\n"
"v_mfma_f32_16x16x32_bf16 a[88:91], v[156:159], v[100:103], a[88:91]\n"
"ds_read_b128 a[220:223], v187 offset:13312\n"
"v_add_f32 v184, v184, v136\n"
"v_mfma_f32_16x16x32_bf16 a[92:95], v[156:159], v[108:111], a[92:95]\n"
"v_exp_f32 v137, v137\n"
"v_mfma_f32_16x16x32_bf16 a[92:95], v[152:155], v[104:107], a[92:95]\n"
"s_waitcnt lgkmcnt(2)\n"
"ds_read_b128 a[216:219], v186 offset:13312\n"
"v_add_f32 v184, v184, v137\n"
"v_mfma_f32_16x16x32_bf16 v[112:115], a[160:163], v[0:3], v[160:163]\n"
"v_exp_f32 v138, v138\n"
"v_mfma_f32_16x16x32_bf16 v[112:115], a[164:167], v[4:7], v[112:115]\n"
"ds_read_b128 a[212:215], v187 offset:12288\n"
"v_add_f32 v184, v184, v138\n"
"v_mfma_f32_16x16x32_bf16 v[112:115], a[168:171], v[8:11], v[112:115]\n"
"v_exp_f32 v139, v139\n"
"v_mfma_f32_16x16x32_bf16 v[112:115], a[172:175], v[12:15], v[112:115]\n"
"v_add_f32 v184, v184, v139\n"
"v_cvt_pk_bf16_f32 v148, v120, v121\n"
"v_mfma_f32_16x16x32_bf16 v[116:119], a[188:191], v[12:15], v[160:163]\n"
"v_exp_f32 v140, v140\n"
"v_mfma_f32_16x16x32_bf16 v[116:119], a[184:187], v[8:11], v[116:119]\n"
"v_add_f32 v184, v184, v140\n"
"v_cvt_pk_bf16_f32 v149, v122, v123\n"
"v_mfma_f32_16x16x32_bf16 v[116:119], a[180:183], v[4:7], v[116:119]\n"
"ds_read_b128 a[208:211], v186 offset:12288\n"
"v_exp_f32 v141, v141\n"
"v_mfma_f32_16x16x32_bf16 v[116:119], a[176:179], v[0:3], v[116:119]\n"
"v_add_f32 v184, v184, v141\n"
"v_cvt_pk_bf16_f32 v150, v124, v125\n"
"v_mfma_f32_16x16x32_bf16 v[120:123], a[192:195], v[0:3], v[160:163]\n"
"v_exp_f32 v142, v142\n"
"v_mfma_f32_16x16x32_bf16 v[120:123], a[196:199], v[4:7], v[120:123]\n"
"v_add_f32 v184, v184, v142\n"
"v_cvt_pk_bf16_f32 v151, v126, v127\n"
"v_mfma_f32_16x16x32_bf16 v[120:123], a[200:203], v[8:11], v[120:123]\n"
"s_waitcnt lgkmcnt(1)\n"
"v_exp_f32 v143, v143\n"
"v_mfma_f32_16x16x32_bf16 v[120:123], a[204:207], v[12:15], v[120:123]\n"
"v_add_f32 v184, v184, v143\n"
"v_cvt_pk_bf16_f32 v152, v128, v129\n"
"v_mfma_f32_16x16x32_bf16 v[124:127], a[220:223], v[12:15], v[160:163]\n"
"v_exp_f32 v112, v112\n"
"v_mfma_f32_16x16x32_bf16 v[124:127], a[216:219], v[8:11], v[124:127]\n"
"v_cvt_pk_bf16_f32 v153, v130, v131\n"
"v_add_f32 v180, v180, v112\n"
"v_mfma_f32_16x16x32_bf16 v[124:127], a[212:215], v[4:7], v[124:127]\n"
"s_waitcnt lgkmcnt(0)\n"
"ds_read_b64_tr_b16 a[244:245], v192 offset:8192\n"
"v_exp_f32 v113, v113\n"
"v_mfma_f32_16x16x32_bf16 v[124:127], a[208:211], v[0:3], v[124:127]\n"
"v_cvt_pk_bf16_f32 v154, v132, v133\n"
"v_add_f32 v180, v180, v113\n"
"v_mfma_f32_16x16x32_bf16 a[96:99], v[144:147], v[220:223], a[96:99]\n"
"v_exp_f32 v114, v114\n"
"v_mfma_f32_16x16x32_bf16 a[96:99], v[148:151], v[224:227], a[96:99]\n"
"v_cvt_pk_bf16_f32 v155, v134, v135\n"
"v_add_f32 v180, v180, v114\n"
"v_mfma_f32_16x16x32_bf16 a[100:103], v[148:151], v[232:235], a[100:103]\n"
"v_exp_f32 v115, v115\n"
"v_mfma_f32_16x16x32_bf16 a[100:103], v[144:147], v[228:231], a[100:103]\n"
"v_cvt_pk_bf16_f32 v156, v136, v137\n"
"v_add_f32 v180, v180, v115\n"
"v_mfma_f32_16x16x32_bf16 a[104:107], v[144:147], v[236:239], a[104:107]\n"
"ds_read_b64_tr_b16 a[246:247], v192 offset:12288\n"
"v_exp_f32 v116, v116\n"
"v_mfma_f32_16x16x32_bf16 a[104:107], v[148:151], v[240:243], a[104:107]\n"
"v_cvt_pk_bf16_f32 v157, v138, v139\n"
"v_add_f32 v180, v180, v116\n"
"v_mfma_f32_16x16x32_bf16 a[108:111], v[148:151], v[248:251], a[108:111]\n"
"v_exp_f32 v117, v117\n"
"v_mfma_f32_16x16x32_bf16 a[108:111], v[144:147], v[244:247], a[108:111]\n"
"v_cvt_pk_bf16_f32 v158, v140, v141\n"
"v_add_f32 v180, v180, v117\n"
"v_mfma_f32_16x16x32_bf16 a[112:115], v[144:147], v[80:83], a[112:115]\n"
"v_exp_f32 v118, v118\n"
"v_mfma_f32_16x16x32_bf16 a[112:115], v[148:151], v[84:87], a[112:115]\n"
"v_cvt_pk_bf16_f32 v159, v142, v143\n"
"v_add_f32 v180, v180, v118\n"
"v_mfma_f32_16x16x32_bf16 a[116:119], v[148:151], v[92:95], a[116:119]\n"
"ds_read_b64_tr_b16 a[252:253], v193 offset:8192\n"
"v_exp_f32 v119, v119\n"
"v_mfma_f32_16x16x32_bf16 a[116:119], v[144:147], v[88:91], a[116:119]\n"
"ds_read_b64_tr_b16 a[254:255], v193 offset:12288\n"
"v_add_f32 v180, v180, v119\n"
"v_mfma_f32_16x16x32_bf16 a[120:123], v[144:147], v[96:99], a[120:123]\n"
"v_exp_f32 v120, v120\n"
"v_mfma_f32_16x16x32_bf16 a[120:123], v[148:151], v[100:103], a[120:123]\n"
"ds_read_b64_tr_b16 a[248:249], v193 offset:0\n"
"v_add_f32 v180, v180, v120\n"
"v_mfma_f32_16x16x32_bf16 a[124:127], v[148:151], v[108:111], a[124:127]\n"
"v_exp_f32 v121, v121\n"
"v_mfma_f32_16x16x32_bf16 a[124:127], v[144:147], v[104:107], a[124:127]\n"
"ds_read_b64_tr_b16 a[250:251], v193 offset:4096\n"
"v_add_f32 v180, v180, v121\n"
"v_mfma_f32_16x16x32_bf16 v[128:131], a[160:163], v[16:19], v[164:167]\n"
"v_exp_f32 v122, v122\n"
"v_mfma_f32_16x16x32_bf16 v[128:131], a[164:167], v[20:23], v[128:131]\n"
"s_add_u32 m0, s78, 0x10000\n"
"v_add_f32 v180, v180, v122\n"
"v_cvt_pk_bf16_f32 v144, v112, v113\n"
"v_mfma_f32_16x16x32_bf16 v[128:131], a[168:171], v[24:27], v[128:131]\n"
"buffer_load_dwordx4 v198, s[52:55], s75 offen offset:0 nt lds\n"
"v_exp_f32 v123, v123\n"
"v_mfma_f32_16x16x32_bf16 v[128:131], a[172:175], v[28:31], v[128:131]\n"
"v_add_f32 v180, v180, v123\n"
"v_cvt_pk_bf16_f32 v145, v114, v115\n"
"v_mfma_f32_16x16x32_bf16 v[132:135], a[188:191], v[28:31], v[164:167]\n"
"v_exp_f32 v124, v124\n"
"v_mfma_f32_16x16x32_bf16 v[132:135], a[184:187], v[24:27], v[132:135]\n"
"buffer_load_dwordx4 v198, s[52:55], s75 offen offset:2048 nt lds\n"
"v_add_f32 v180, v180, v124\n"
"v_mfma_f32_16x16x32_bf16 v[132:135], a[180:183], v[20:23], v[132:135]\n"
"v_exp_f32 v125, v125\n"
"v_mfma_f32_16x16x32_bf16 v[132:135], a[176:179], v[16:19], v[132:135]\n"
"s_add_u32 m0, s78, 0x10380\n"
"v_add_f32 v180, v180, v125\n"
"v_cvt_pk_bf16_f32 v146, v116, v117\n"
"v_mfma_f32_16x16x32_bf16 v[136:139], a[192:195], v[16:19], v[164:167]\n"
"buffer_load_dwordx4 v198, s[52:55], s75 offen offset:128 nt lds\n"
"v_exp_f32 v126, v126\n"
"v_mfma_f32_16x16x32_bf16 v[136:139], a[196:199], v[20:23], v[136:139]\n"
"v_add_f32 v180, v180, v126\n"
"v_cvt_pk_bf16_f32 v147, v118, v119\n"
"v_mfma_f32_16x16x32_bf16 v[136:139], a[200:203], v[24:27], v[136:139]\n"
"v_exp_f32 v127, v127\n"
"v_mfma_f32_16x16x32_bf16 v[136:139], a[204:207], v[28:31], v[136:139]\n"
"buffer_load_dwordx4 v198, s[52:55], s75 offen offset:2176 nt lds\n"
"v_add_f32 v180, v180, v127\n"
"v_mfma_f32_16x16x32_bf16 v[140:143], a[220:223], v[28:31], v[164:167]\n"
"v_exp_f32 v128, v128\n"
"v_mfma_f32_16x16x32_bf16 v[140:143], a[216:219], v[24:27], v[140:143]\n"
"s_add_u32 m0, s78, 0x14000\n"
"v_add_f32 v181, v181, v128\n"
"v_cvt_pk_bf16_f32 v148, v120, v121\n"
"v_mfma_f32_16x16x32_bf16 v[140:143], a[212:215], v[20:23], v[140:143]\n"
"buffer_load_dwordx4 v198, s[56:59], s75 offen offset:0 nt lds\n"
"v_exp_f32 v129, v129\n"
"v_mfma_f32_16x16x32_bf16 v[140:143], a[208:211], v[16:19], v[140:143]\n"
"s_waitcnt lgkmcnt(0)\n"
"v_add_f32 v181, v181, v129\n"
"v_cvt_pk_bf16_f32 v149, v122, v123\n"
"v_mfma_f32_16x16x32_bf16 a[128:131], v[152:155], v[220:223], a[128:131]\n"
"v_exp_f32 v130, v130\n"
"v_mfma_f32_16x16x32_bf16 a[128:131], v[156:159], v[224:227], a[128:131]\n"
"buffer_load_dwordx4 v198, s[56:59], s75 offen offset:2048 nt lds\n"
"v_add_f32 v181, v181, v130\n"
"v_mfma_f32_16x16x32_bf16 a[132:135], v[156:159], v[232:235], a[132:135]\n"
"v_exp_f32 v131, v131\n"
"v_mfma_f32_16x16x32_bf16 a[132:135], v[152:155], v[228:231], a[132:135]\n"
"s_add_u32 m0, s78, 0x14380\n"
"v_add_f32 v181, v181, v131\n"
"v_cvt_pk_bf16_f32 v150, v124, v125\n"
"v_mfma_f32_16x16x32_bf16 a[136:139], v[152:155], v[236:239], a[136:139]\n"
"buffer_load_dwordx4 v198, s[56:59], s75 offen offset:128 nt lds\n"
"v_exp_f32 v132, v132\n"
"v_mfma_f32_16x16x32_bf16 a[136:139], v[156:159], v[240:243], a[136:139]\n"
"v_add_f32 v181, v181, v132\n"
"v_cvt_pk_bf16_f32 v151, v126, v127\n"
"v_mfma_f32_16x16x32_bf16 a[140:143], v[156:159], v[248:251], a[140:143]\n"
"v_exp_f32 v133, v133\n"
"v_mfma_f32_16x16x32_bf16 a[140:143], v[152:155], v[244:247], a[140:143]\n"
"buffer_load_dwordx4 v198, s[56:59], s75 offen offset:2176 nt lds\n"
"v_add_f32 v181, v181, v133\n"
"v_mfma_f32_16x16x32_bf16 a[144:147], v[152:155], v[80:83], a[144:147]\n"
"s_add_u32 s75, s75, 0x4000\n"
"v_exp_f32 v134, v134\n"
"v_mfma_f32_16x16x32_bf16 a[144:147], v[156:159], v[84:87], a[144:147]\n"
"ds_read_b64_tr_b16 v[80:81], v190 offset:1024\n"
"v_add_f32 v181, v181, v134\n"
"v_mfma_f32_16x16x32_bf16 a[148:151], v[156:159], v[92:95], a[148:151]\n"
"v_exp_f32 v135, v135\n"
"v_mfma_f32_16x16x32_bf16 a[148:151], v[152:155], v[88:91], a[148:151]\n"
"ds_read_b64_tr_b16 v[82:83], v190 offset:5120\n"
"v_add_f32 v181, v181, v135\n"
"v_mfma_f32_16x16x32_bf16 a[152:155], v[152:155], v[96:99], a[152:155]\n"
"v_exp_f32 v136, v136\n"
"v_mfma_f32_16x16x32_bf16 a[152:155], v[156:159], v[100:103], a[152:155]\n"
"ds_read_b64_tr_b16 v[84:85], v190 offset:9216\n"
"v_add_f32 v181, v181, v136\n"
"v_mfma_f32_16x16x32_bf16 a[156:159], v[156:159], v[108:111], a[156:159]\n"
"v_exp_f32 v137, v137\n"
"v_mfma_f32_16x16x32_bf16 a[156:159], v[152:155], v[104:107], a[156:159]\n"
"ds_read_b64_tr_b16 v[86:87], v190 offset:13312\n"
"v_add_f32 v181, v181, v137\n"
"s_sub_u32 s77, s77, 1\n"
"s_cmp_lg_u32 s77, 0\n"
"s_cbranch_scc1 L_att_%=\n"
"v_mfma_f32_16x16x32_bf16 v[112:115], a[160:163], v[32:35], v[168:171]\n"
"v_exp_f32 v138, v138\n"
"v_mfma_f32_16x16x32_bf16 v[112:115], a[164:167], v[36:39], v[112:115]\n"
"ds_read_b64_tr_b16 v[92:93], v191 offset:9216\n"
"v_add_f32 v181, v181, v138\n"
"v_mfma_f32_16x16x32_bf16 v[112:115], a[168:171], v[40:43], v[112:115]\n"
"v_exp_f32 v139, v139\n"
"v_mfma_f32_16x16x32_bf16 v[112:115], a[172:175], v[44:47], v[112:115]\n"
"ds_read_b64_tr_b16 v[94:95], v191 offset:13312\n"
"v_add_f32 v181, v181, v139\n"
"v_mfma_f32_16x16x32_bf16 v[116:119], a[188:191], v[44:47], v[168:171]\n"
"v_exp_f32 v140, v140\n"
"v_mfma_f32_16x16x32_bf16 v[116:119], a[184:187], v[40:43], v[116:119]\n"
"ds_read_b64_tr_b16 v[88:89], v191 offset:1024\n"
"v_add_f32 v181, v181, v140\n"
"v_mfma_f32_16x16x32_bf16 v[116:119], a[180:183], v[36:39], v[116:119]\n"
"v_exp_f32 v141, v141\n"
"v_mfma_f32_16x16x32_bf16 v[116:119], a[176:179], v[32:35], v[116:119]\n"
"ds_read_b64_tr_b16 v[90:91], v191 offset:5120\n"
"v_add_f32 v181, v181, v141\n"
"v_mfma_f32_16x16x32_bf16 v[120:123], a[192:195], v[32:35], v[168:171]\n"
"v_exp_f32 v142, v142\n"
"v_mfma_f32_16x16x32_bf16 v[120:123], a[196:199], v[36:39], v[120:123]\n"
"ds_read_b64_tr_b16 v[96:97], v192 offset:1024\n"
"v_add_f32 v181, v181, v142\n"
"v_mfma_f32_16x16x32_bf16 v[120:123], a[200:203], v[40:43], v[120:123]\n"
"v_exp_f32 v143, v143\n"
"v_mfma_f32_16x16x32_bf16 v[120:123], a[204:207], v[44:47], v[120:123]\n"
"ds_read_b64_tr_b16 v[98:99], v192 offset:5120\n"
"v_add_f32 v181, v181, v143\n"
"v_mfma_f32_16x16x32_bf16 v[124:127], a[220:223], v[44:47], v[168:171]\n"
"v_exp_f32 v112, v112\n"
"v_mfma_f32_16x16x32_bf16 v[124:127], a[216:219], v[40:43], v[124:127]\n"
"ds_read_b64_tr_b16 v[100:101], v192 offset:9216\n"
"v_add_f32 v182, v182, v112\n"
"v_mfma_f32_16x16x32_bf16 v[124:127], a[212:215], v[36:39], v[124:127]\n"
"v_exp_f32 v113, v113\n"
"v_mfma_f32_16x16x32_bf16 v[124:127], a[208:211], v[32:35], v[124:127]\n"
"ds_read_b64_tr_b16 v[102:103], v192 offset:13312\n"
"v_add_f32 v182, v182, v113\n"
"v_mfma_f32_16x16x32_bf16 a[0:3], v[144:147], a[224:227], a[0:3]\n"
"v_exp_f32 v114, v114\n"
"v_mfma_f32_16x16x32_bf16 a[0:3], v[148:151], a[228:231], a[0:3]\n"
"ds_read_b64_tr_b16 v[108:109], v193 offset:9216\n"
"v_add_f32 v182, v182, v114\n"
"v_mfma_f32_16x16x32_bf16 a[4:7], v[148:151], a[236:239], a[4:7]\n"
"v_exp_f32 v115, v115\n"
"v_mfma_f32_16x16x32_bf16 a[4:7], v[144:147], a[232:235], a[4:7]\n"
"ds_read_b64_tr_b16 v[110:111], v193 offset:13312\n"
"v_add_f32 v182, v182, v115\n"
"v_mfma_f32_16x16x32_bf16 a[8:11], v[144:147], a[240:243], a[8:11]\n"
"v_exp_f32 v116, v116\n"
"v_mfma_f32_16x16x32_bf16 a[8:11], v[148:151], a[244:247], a[8:11]\n"
"ds_read_b64_tr_b16 v[104:105], v193 offset:1024\n"
"v_add_f32 v182, v182, v116\n"
"v_mfma_f32_16x16x32_bf16 a[12:15], v[148:151], a[252:255], a[12:15]\n"
"v_exp_f32 v117, v117\n"
"v_mfma_f32_16x16x32_bf16 a[12:15], v[144:147], a[248:251], a[12:15]\n"
"s_waitcnt lgkmcnt(3)\n"
"ds_read_b64_tr_b16 v[106:107], v193 offset:5120\n"
"v_add_f32 v182, v182, v117\n"
"v_mfma_f32_16x16x32_bf16 a[16:19], v[144:147], v[80:83], a[16:19]\n"
"v_exp_f32 v118, v118\n"
"v_mfma_f32_16x16x32_bf16 a[16:19], v[148:151], v[84:87], a[16:19]\n"
"v_add_f32 v182, v182, v118\n"
"v_cvt_pk_bf16_f32 v152, v128, v129\n"
"v_mfma_f32_16x16x32_bf16 a[20:23], v[148:151], v[92:95], a[20:23]\n"
"v_exp_f32 v119, v119\n"
"v_mfma_f32_16x16x32_bf16 a[20:23], v[144:147], v[88:91], a[20:23]\n"
"v_add_f32 v182, v182, v119\n"
"v_cvt_pk_bf16_f32 v153, v130, v131\n"
"v_mfma_f32_16x16x32_bf16 a[24:27], v[144:147], v[96:99], a[24:27]\n"
"v_exp_f32 v120, v120\n"
"v_mfma_f32_16x16x32_bf16 a[24:27], v[148:151], v[100:103], a[24:27]\n"
"s_waitcnt lgkmcnt(1)\n"
"v_add_f32 v182, v182, v120\n"
"v_cvt_pk_bf16_f32 v154, v132, v133\n"
"v_mfma_f32_16x16x32_bf16 a[28:31], v[148:151], v[108:111], a[28:31]\n"
"s_waitcnt lgkmcnt(0)\n"
"v_exp_f32 v121, v121\n"
"v_mfma_f32_16x16x32_bf16 a[28:31], v[144:147], v[104:107], a[28:31]\n"
"v_add_f32 v182, v182, v121\n"
"v_cvt_pk_bf16_f32 v155, v134, v135\n"
"v_mfma_f32_16x16x32_bf16 v[128:131], a[160:163], v[48:51], v[172:175]\n"
"s_waitcnt vmcnt(8)\n"
"v_exp_f32 v122, v122\n"
"v_mfma_f32_16x16x32_bf16 v[128:131], a[164:167], v[52:55], v[128:131]\n"
"s_barrier\n"
"v_add_f32 v182, v182, v122\n"
"v_cvt_pk_bf16_f32 v156, v136, v137\n"
"v_mfma_f32_16x16x32_bf16 v[128:131], a[168:171], v[56:59], v[128:131]\n"
"v_exp_f32 v123, v123\n"
"v_mfma_f32_16x16x32_bf16 v[128:131], a[172:175], v[60:63], v[128:131]\n"
"v_add_f32 v182, v182, v123\n"
"v_cvt_pk_bf16_f32 v157, v138, v139\n"
"v_mfma_f32_16x16x32_bf16 v[132:135], a[188:191], v[60:63], v[172:175]\n"
"ds_read_b64_tr_b16 v[220:221], v190 offset:32768\n"
"v_exp_f32 v124, v124\n"
"v_mfma_f32_16x16x32_bf16 v[132:135], a[184:187], v[56:59], v[132:135]\n"
"v_add_f32 v182, v182, v124\n"
"v_cvt_pk_bf16_f32 v158, v140, v141\n"
"v_mfma_f32_16x16x32_bf16 v[132:135], a[180:183], v[52:55], v[132:135]\n"
"v_exp_f32 v125, v125\n"
"v_mfma_f32_16x16x32_bf16 v[132:135], a[176:179], v[48:51], v[132:135]\n"
"v_add_f32 v182, v182, v125\n"
"v_cvt_pk_bf16_f32 v159, v142, v143\n"
"v_mfma_f32_16x16x32_bf16 v[136:139], a[192:195], v[48:51], v[172:175]\n"
"ds_read_b64_tr_b16 v[222:223], v190 offset:36864\n"
"v_exp_f32 v126, v126\n"
"v_mfma_f32_16x16x32_bf16 v[136:139], a[196:199], v[52:55], v[136:139]\n"
"v_add_f32 v182, v182, v126\n"
"v_cvt_pk_bf16_f32 v144, v112, v113\n"
"v_mfma_f32_16x16x32_bf16 v[136:139], a[200:203], v[56:59], v[136:139]\n"
"v_exp_f32 v127, v127\n"
"v_mfma_f32_16x16x32_bf16 v[136:139], a[204:207], v[60:63], v[136:139]\n"
"v_add_f32 v182, v182, v127\n"
"v_cvt_pk_bf16_f32 v145, v114, v115\n"
"v_mfma_f32_16x16x32_bf16 v[140:143], a[220:223], v[60:63], v[172:175]\n"
"ds_read_b64_tr_b16 v[224:225], v190 offset:40960\n"
"v_exp_f32 v128, v128\n"
"v_mfma_f32_16x16x32_bf16 v[140:143], a[216:219], v[56:59], v[140:143]\n"
"v_add_f32 v183, v183, v128\n"
"v_cvt_pk_bf16_f32 v146, v116, v117\n"
"v_mfma_f32_16x16x32_bf16 v[140:143], a[212:215], v[52:55], v[140:143]\n"
"v_exp_f32 v129, v129\n"
"v_mfma_f32_16x16x32_bf16 v[140:143], a[208:211], v[48:51], v[140:143]\n"
"v_add_f32 v183, v183, v129\n"
"v_cvt_pk_bf16_f32 v147, v118, v119\n"
"v_mfma_f32_16x16x32_bf16 a[32:35], v[152:155], a[224:227], a[32:35]\n"
"ds_read_b64_tr_b16 v[226:227], v190 offset:45056\n"
"v_exp_f32 v130, v130\n"
"v_mfma_f32_16x16x32_bf16 a[32:35], v[156:159], a[228:231], a[32:35]\n"
"v_add_f32 v183, v183, v130\n"
"v_cvt_pk_bf16_f32 v148, v120, v121\n"
"v_mfma_f32_16x16x32_bf16 a[36:39], v[156:159], a[236:239], a[36:39]\n"
"v_exp_f32 v131, v131\n"
"v_mfma_f32_16x16x32_bf16 a[36:39], v[152:155], a[232:235], a[36:39]\n"
"v_add_f32 v183, v183, v131\n"
"v_cvt_pk_bf16_f32 v149, v122, v123\n"
"v_mfma_f32_16x16x32_bf16 a[40:43], v[152:155], a[240:243], a[40:43]\n"
"ds_read_b64_tr_b16 v[232:233], v191 offset:40960\n"
"v_exp_f32 v132, v132\n"
"v_mfma_f32_16x16x32_bf16 a[40:43], v[156:159], a[244:247], a[40:43]\n"
"v_add_f32 v183, v183, v132\n"
"v_cvt_pk_bf16_f32 v150, v124, v125\n"
"v_mfma_f32_16x16x32_bf16 a[44:47], v[156:159], a[252:255], a[44:47]\n"
"v_exp_f32 v133, v133\n"
"v_mfma_f32_16x16x32_bf16 a[44:47], v[152:155], a[248:251], a[44:47]\n"
"v_add_f32 v183, v183, v133\n"
"v_cvt_pk_bf16_f32 v151, v126, v127\n"
"v_mfma_f32_16x16x32_bf16 a[48:51], v[152:155], v[80:83], a[48:51]\n"
"ds_read_b64_tr_b16 v[234:235], v191 offset:45056\n"
"v_exp_f32 v134, v134\n"
"v_mfma_f32_16x16x32_bf16 a[48:51], v[156:159], v[84:87], a[48:51]\n"
"ds_read_b64_tr_b16 v[228:229], v191 offset:32768\n"
"v_add_f32 v183, v183, v134\n"
"v_mfma_f32_16x16x32_bf16 a[52:55], v[156:159], v[92:95], a[52:55]\n"
"v_exp_f32 v135, v135\n"
"v_mfma_f32_16x16x32_bf16 a[52:55], v[152:155], v[88:91], a[52:55]\n"
"ds_read_b64_tr_b16 v[230:231], v191 offset:36864\n"
"v_add_f32 v183, v183, v135\n"
"v_mfma_f32_16x16x32_bf16 a[56:59], v[152:155], v[96:99], a[56:59]\n"
"v_exp_f32 v136, v136\n"
"v_mfma_f32_16x16x32_bf16 a[56:59], v[156:159], v[100:103], a[56:59]\n"
"ds_read_b64_tr_b16 v[236:237], v192 offset:32768\n"
"v_add_f32 v183, v183, v136\n"
"v_mfma_f32_16x16x32_bf16 a[60:63], v[156:159], v[108:111], a[60:63]\n"
"v_exp_f32 v137, v137\n"
"v_mfma_f32_16x16x32_bf16 a[60:63], v[152:155], v[104:107], a[60:63]\n"
"ds_read_b64_tr_b16 v[238:239], v192 offset:36864\n"
"v_add_f32 v183, v183, v137\n"
"v_mfma_f32_16x16x32_bf16 v[112:115], a[160:163], v[64:67], v[176:179]\n"
"v_exp_f32 v138, v138\n"
"v_mfma_f32_16x16x32_bf16 v[112:115], a[164:167], v[68:71], v[112:115]\n"
"ds_read_b128 a[160:163], v186 offset:32768\n"
"v_add_f32 v183, v183, v138\n"
"v_mfma_f32_16x16x32_bf16 v[112:115], a[168:171], v[72:75], v[112:115]\n"
"v_exp_f32 v139, v139\n"
"v_mfma_f32_16x16x32_bf16 v[112:115], a[172:175], v[76:79], v[112:115]\n"
"ds_read_b128 a[164:167], v187 offset:32768\n"
"v_add_f32 v183, v183, v139\n"
"v_mfma_f32_16x16x32_bf16 v[116:119], a[188:191], v[76:79], v[176:179]\n"
"v_exp_f32 v140, v140\n"
"v_mfma_f32_16x16x32_bf16 v[116:119], a[184:187], v[72:75], v[116:119]\n"
"ds_read_b128 a[168:171], v186 offset:33792\n"
"v_add_f32 v183, v183, v140\n"
"v_mfma_f32_16x16x32_bf16 v[116:119], a[180:183], v[68:71], v[116:119]\n"
"v_exp_f32 v141, v141\n"
"v_mfma_f32_16x16x32_bf16 v[116:119], a[176:179], v[64:67], v[116:119]\n"
"ds_read_b128 a[172:175], v187 offset:33792\n"
"v_add_f32 v183, v183, v141\n"
"v_mfma_f32_16x16x32_bf16 v[120:123], a[192:195], v[64:67], v[176:179]\n"
"v_exp_f32 v142, v142\n"
"v_mfma_f32_16x16x32_bf16 v[120:123], a[196:199], v[68:71], v[120:123]\n"
"ds_read_b128 a[188:191], v187 offset:37888\n"
"v_add_f32 v183, v183, v142\n"
"v_mfma_f32_16x16x32_bf16 v[120:123], a[200:203], v[72:75], v[120:123]\n"
"v_exp_f32 v143, v143\n"
"v_mfma_f32_16x16x32_bf16 v[120:123], a[204:207], v[76:79], v[120:123]\n"
"ds_read_b128 a[184:187], v186 offset:37888\n"
"v_add_f32 v183, v183, v143\n"
"v_mfma_f32_16x16x32_bf16 v[124:127], a[220:223], v[76:79], v[176:179]\n"
"ds_read_b128 a[180:183], v187 offset:36864\n"
"v_exp_f32 v112, v112\n"
"v_mfma_f32_16x16x32_bf16 v[124:127], a[216:219], v[72:75], v[124:127]\n"
"v_add_f32 v184, v184, v112\n"
"v_cvt_pk_bf16_f32 v152, v128, v129\n"
"v_mfma_f32_16x16x32_bf16 v[124:127], a[212:215], v[68:71], v[124:127]\n"
"v_exp_f32 v113, v113\n"
"v_mfma_f32_16x16x32_bf16 v[124:127], a[208:211], v[64:67], v[124:127]\n"
"v_add_f32 v184, v184, v113\n"
"v_cvt_pk_bf16_f32 v153, v130, v131\n"
"v_mfma_f32_16x16x32_bf16 a[64:67], v[144:147], a[224:227], a[64:67]\n"
"v_exp_f32 v114, v114\n"
"v_mfma_f32_16x16x32_bf16 a[64:67], v[148:151], a[228:231], a[64:67]\n"
"ds_read_b128 a[176:179], v186 offset:36864\n"
"v_add_f32 v184, v184, v114\n"
"v_mfma_f32_16x16x32_bf16 a[68:71], v[148:151], a[236:239], a[68:71]\n"
"v_exp_f32 v115, v115\n"
"v_mfma_f32_16x16x32_bf16 a[68:71], v[144:147], a[232:235], a[68:71]\n"
"ds_read_b128 a[192:195], v186 offset:40960\n"
"v_add_f32 v184, v184, v115\n"
"v_mfma_f32_16x16x32_bf16 a[72:75], v[144:147], a[240:243], a[72:75]\n"
"v_exp_f32 v116, v116\n"
"v_mfma_f32_16x16x32_bf16 a[72:75], v[148:151], a[244:247], a[72:75]\n"
"ds_read_b128 a[196:199], v187 offset:40960\n"
"v_add_f32 v184, v184, v116\n"
"v_mfma_f32_16x16x32_bf16 a[76:79], v[148:151], a[252:255], a[76:79]\n"
"v_exp_f32 v117, v117\n"
"v_mfma_f32_16x16x32_bf16 a[76:79], v[144:147], a[248:251], a[76:79]\n"
"ds_read_b128 a[200:203], v186 offset:41984\n"
"v_add_f32 v184, v184, v117\n"
"v_mfma_f32_16x16x32_bf16 a[80:83], v[144:147], v[80:83], a[80:83]\n"
"v_exp_f32 v118, v118\n"
"v_cvt_pk_bf16_f32 v154, v132, v133\n"
"v_mfma_f32_16x16x32_bf16 a[80:83], v[148:151], v[84:87], a[80:83]\n"
"v_add_f32 v184, v184, v118\n"
"v_cvt_pk_bf16_f32 v155, v134, v135\n"
"v_mfma_f32_16x16x32_bf16 a[84:87], v[148:151], v[92:95], a[84:87]\n"
"v_exp_f32 v119, v119\n"
"v_mfma_f32_16x16x32_bf16 a[84:87], v[144:147], v[88:91], a[84:87]\n"
"ds_read_b128 a[204:207], v187 offset:41984\n"
"v_add_f32 v184, v184, v119\n"
"v_mfma_f32_16x16x32_bf16 a[88:91], v[144:147], v[96:99], a[88:91]\n"
"v_exp_f32 v120, v120\n"
"v_mfma_f32_16x16x32_bf16 a[88:91], v[148:151], v[100:103], a[88:91]\n"
"ds_read_b128 a[220:223], v187 offset:46080\n"
"v_add_f32 v184, v184, v120\n"
"v_mfma_f32_16x16x32_bf16 a[92:95], v[148:151], v[108:111], a[92:95]\n"
"v_exp_f32 v121, v121\n"
"v_mfma_f32_16x16x32_bf16 a[92:95], v[144:147], v[104:107], a[92:95]\n"
"s_waitcnt lgkmcnt(2)\n"
"ds_read_b128 a[216:219], v186 offset:46080\n"
"v_add_f32 v184, v184, v121\n"
"v_mfma_f32_16x16x32_bf16 v[128:131], a[160:163], v[0:3], v[160:163]\n"
"v_exp_f32 v122, v122\n"
"v_mfma_f32_16x16x32_bf16 v[128:131], a[164:167], v[4:7], v[128:131]\n"
"ds_read_b128 a[212:215], v187 offset:45056\n"
"v_add_f32 v184, v184, v122\n"
"v_mfma_f32_16x16x32_bf16 v[128:131], a[168:171], v[8:11], v[128:131]\n"
"v_exp_f32 v123, v123\n"
"v_mfma_f32_16x16x32_bf16 v[128:131], a[172:175], v[12:15], v[128:131]\n"
"v_add_f32 v184, v184, v123\n"
"v_cvt_pk_bf16_f32 v156, v136, v137\n"
"v_mfma_f32_16x16x32_bf16 v[132:135], a[188:191], v[12:15], v[160:163]\n"
"v_exp_f32 v124, v124\n"
"v_mfma_f32_16x16x32_bf16 v[132:135], a[184:187], v[8:11], v[132:135]\n"
"v_add_f32 v184, v184, v124\n"
"v_cvt_pk_bf16_f32 v157, v138, v139\n"
"v_mfma_f32_16x16x32_bf16 v[132:135], a[180:183], v[4:7], v[132:135]\n"
"ds_read_b128 a[208:211], v186 offset:45056\n"
"v_exp_f32 v125, v125\n"
"v_mfma_f32_16x16x32_bf16 v[132:135], a[176:179], v[0:3], v[132:135]\n"
"v_add_f32 v184, v184, v125\n"
"v_cvt_pk_bf16_f32 v158, v140, v141\n"
"v_mfma_f32_16x16x32_bf16 v[136:139], a[192:195], v[0:3], v[160:163]\n"
"v_exp_f32 v126, v126\n"
"v_mfma_f32_16x16x32_bf16 v[136:139], a[196:199], v[4:7], v[136:139]\n"
"v_add_f32 v184, v184, v126\n"
"v_cvt_pk_bf16_f32 v159, v142, v143\n"
"v_mfma_f32_16x16x32_bf16 v[136:139], a[200:203], v[8:11], v[136:139]\n"
"s_waitcnt lgkmcnt(1)\n"
"v_exp_f32 v127, v127\n"
"v_mfma_f32_16x16x32_bf16 v[136:139], a[204:207], v[12:15], v[136:139]\n"
"v_add_f32 v184, v184, v127\n"
"v_cvt_pk_bf16_f32 v144, v112, v113\n"
"v_mfma_f32_16x16x32_bf16 v[140:143], a[220:223], v[12:15], v[160:163]\n"
"v_exp_f32 v128, v128\n"
"v_mfma_f32_16x16x32_bf16 v[140:143], a[216:219], v[8:11], v[140:143]\n"
"v_cvt_pk_bf16_f32 v145, v114, v115\n"
"v_add_f32 v180, v180, v128\n"
"v_mfma_f32_16x16x32_bf16 v[140:143], a[212:215], v[4:7], v[140:143]\n"
"s_waitcnt lgkmcnt(0)\n"
"ds_read_b64_tr_b16 v[240:241], v192 offset:40960\n"
"v_exp_f32 v129, v129\n"
"v_mfma_f32_16x16x32_bf16 v[140:143], a[208:211], v[0:3], v[140:143]\n"
"v_cvt_pk_bf16_f32 v146, v116, v117\n"
"v_add_f32 v180, v180, v129\n"
"v_mfma_f32_16x16x32_bf16 a[96:99], v[152:155], a[224:227], a[96:99]\n"
"v_exp_f32 v130, v130\n"
"v_mfma_f32_16x16x32_bf16 a[96:99], v[156:159], a[228:231], a[96:99]\n"
"v_cvt_pk_bf16_f32 v147, v118, v119\n"
"v_add_f32 v180, v180, v130\n"
"v_mfma_f32_16x16x32_bf16 a[100:103], v[156:159], a[236:239], a[100:103]\n"
"v_exp_f32 v131, v131\n"
"v_mfma_f32_16x16x32_bf16 a[100:103], v[152:155], a[232:235], a[100:103]\n"
"v_cvt_pk_bf16_f32 v148, v120, v121\n"
"v_add_f32 v180, v180, v131\n"
"v_mfma_f32_16x16x32_bf16 a[104:107], v[152:155], a[240:243], a[104:107]\n"
"ds_read_b64_tr_b16 v[242:243], v192 offset:45056\n"
"v_exp_f32 v132, v132\n"
"v_mfma_f32_16x16x32_bf16 a[104:107], v[156:159], a[244:247], a[104:107]\n"
"v_cvt_pk_bf16_f32 v149, v122, v123\n"
"v_add_f32 v180, v180, v132\n"
"v_mfma_f32_16x16x32_bf16 a[108:111], v[156:159], a[252:255], a[108:111]\n"
"v_exp_f32 v133, v133\n"
"v_mfma_f32_16x16x32_bf16 a[108:111], v[152:155], a[248:251], a[108:111]\n"
"v_cvt_pk_bf16_f32 v150, v124, v125\n"
"v_add_f32 v180, v180, v133\n"
"v_mfma_f32_16x16x32_bf16 a[112:115], v[152:155], v[80:83], a[112:115]\n"
"v_exp_f32 v134, v134\n"
"v_mfma_f32_16x16x32_bf16 a[112:115], v[156:159], v[84:87], a[112:115]\n"
"v_cvt_pk_bf16_f32 v151, v126, v127\n"
"v_add_f32 v180, v180, v134\n"
"v_mfma_f32_16x16x32_bf16 a[116:119], v[156:159], v[92:95], a[116:119]\n"
"ds_read_b64_tr_b16 v[248:249], v193 offset:40960\n"
"v_exp_f32 v135, v135\n"
"v_mfma_f32_16x16x32_bf16 a[116:119], v[152:155], v[88:91], a[116:119]\n"
"ds_read_b64_tr_b16 v[250:251], v193 offset:45056\n"
"v_add_f32 v180, v180, v135\n"
"v_mfma_f32_16x16x32_bf16 a[120:123], v[152:155], v[96:99], a[120:123]\n"
"v_exp_f32 v136, v136\n"
"v_mfma_f32_16x16x32_bf16 a[120:123], v[156:159], v[100:103], a[120:123]\n"
"ds_read_b64_tr_b16 v[244:245], v193 offset:32768\n"
"v_add_f32 v180, v180, v136\n"
"v_mfma_f32_16x16x32_bf16 a[124:127], v[156:159], v[108:111], a[124:127]\n"
"v_exp_f32 v137, v137\n"
"v_mfma_f32_16x16x32_bf16 a[124:127], v[152:155], v[104:107], a[124:127]\n"
"ds_read_b64_tr_b16 v[246:247], v193 offset:36864\n"
"v_add_f32 v180, v180, v137\n"
"v_mfma_f32_16x16x32_bf16 v[112:115], a[160:163], v[16:19], v[164:167]\n"
"v_exp_f32 v138, v138\n"
"v_mfma_f32_16x16x32_bf16 v[112:115], a[164:167], v[20:23], v[112:115]\n"
"s_add_u32 m0, s78, 0x18000\n"
"v_add_f32 v180, v180, v138\n"
"v_cvt_pk_bf16_f32 v152, v128, v129\n"
"v_mfma_f32_16x16x32_bf16 v[112:115], a[168:171], v[24:27], v[112:115]\n"
"buffer_load_dwordx4 v198, s[52:55], s75 offen offset:0 nt lds\n"
"v_exp_f32 v139, v139\n"
"v_mfma_f32_16x16x32_bf16 v[112:115], a[172:175], v[28:31], v[112:115]\n"
"v_add_f32 v180, v180, v139\n"
"v_cvt_pk_bf16_f32 v153, v130, v131\n"
"v_mfma_f32_16x16x32_bf16 v[116:119], a[188:191], v[28:31], v[164:167]\n"
"v_exp_f32 v140, v140\n"
"v_mfma_f32_16x16x32_bf16 v[116:119], a[184:187], v[24:27], v[116:119]\n"
"buffer_load_dwordx4 v198, s[52:55], s75 offen offset:2048 nt lds\n"
"v_add_f32 v180, v180, v140\n"
"v_mfma_f32_16x16x32_bf16 v[116:119], a[180:183], v[20:23], v[116:119]\n"
"v_exp_f32 v141, v141\n"
"v_mfma_f32_16x16x32_bf16 v[116:119], a[176:179], v[16:19], v[116:119]\n"
"s_add_u32 m0, s78, 0x18380\n"
"v_add_f32 v180, v180, v141\n"
"v_cvt_pk_bf16_f32 v154, v132, v133\n"
"v_mfma_f32_16x16x32_bf16 v[120:123], a[192:195], v[16:19], v[164:167]\n"
"buffer_load_dwordx4 v198, s[52:55], s75 offen offset:128 nt lds\n"
"v_exp_f32 v142, v142\n"
"v_mfma_f32_16x16x32_bf16 v[120:123], a[196:199], v[20:23], v[120:123]\n"
"v_add_f32 v180, v180, v142\n"
"v_cvt_pk_bf16_f32 v155, v134, v135\n"
"v_mfma_f32_16x16x32_bf16 v[120:123], a[200:203], v[24:27], v[120:123]\n"
"v_exp_f32 v143, v143\n"
"v_mfma_f32_16x16x32_bf16 v[120:123], a[204:207], v[28:31], v[120:123]\n"
"buffer_load_dwordx4 v198, s[52:55], s75 offen offset:2176 nt lds\n"
"v_add_f32 v180, v180, v143\n"
"v_mfma_f32_16x16x32_bf16 v[124:127], a[220:223], v[28:31], v[164:167]\n"
"v_exp_f32 v112, v112\n"
"v_mfma_f32_16x16x32_bf16 v[124:127], a[216:219], v[24:27], v[124:127]\n"
"s_add_u32 m0, s78, 0x1c000\n"
"v_add_f32 v181, v181, v112\n"
"v_cvt_pk_bf16_f32 v156, v136, v137\n"
"v_mfma_f32_16x16x32_bf16 v[124:127], a[212:215], v[20:23], v[124:127]\n"
"buffer_load_dwordx4 v198, s[56:59], s75 offen offset:0 nt lds\n"
"v_exp_f32 v113, v113\n"
"v_mfma_f32_16x16x32_bf16 v[124:127], a[208:211], v[16:19], v[124:127]\n"
"s_waitcnt lgkmcnt(0)\n"
"v_add_f32 v181, v181, v113\n"
"v_cvt_pk_bf16_f32 v157, v138, v139\n"
"v_mfma_f32_16x16x32_bf16 a[128:131], v[144:147], a[224:227], a[128:131]\n"
"v_exp_f32 v114, v114\n"
"v_mfma_f32_16x16x32_bf16 a[128:131], v[148:151], a[228:231], a[128:131]\n"
"buffer_load_dwordx4 v198, s[56:59], s75 offen offset:2048 nt lds\n"
"v_add_f32 v181, v181, v114\n"
"v_mfma_f32_16x16x32_bf16 a[132:135], v[148:151], a[236:239], a[132:135]\n"
"v_exp_f32 v115, v115\n"
"v_mfma_f32_16x16x32_bf16 a[132:135], v[144:147], a[232:235], a[132:135]\n"
"s_add_u32 m0, s78, 0x1c380\n"
"v_add_f32 v181, v181, v115\n"
"v_cvt_pk_bf16_f32 v158, v140, v141\n"
"v_mfma_f32_16x16x32_bf16 a[136:139], v[144:147], a[240:243], a[136:139]\n"
"buffer_load_dwordx4 v198, s[56:59], s75 offen offset:128 nt lds\n"
"v_exp_f32 v116, v116\n"
"v_mfma_f32_16x16x32_bf16 a[136:139], v[148:151], a[244:247], a[136:139]\n"
"v_add_f32 v181, v181, v116\n"
"v_cvt_pk_bf16_f32 v159, v142, v143\n"
"v_mfma_f32_16x16x32_bf16 a[140:143], v[148:151], a[252:255], a[140:143]\n"
"v_exp_f32 v117, v117\n"
"v_mfma_f32_16x16x32_bf16 a[140:143], v[144:147], a[248:251], a[140:143]\n"
"buffer_load_dwordx4 v198, s[56:59], s75 offen offset:2176 nt lds\n"
"v_add_f32 v181, v181, v117\n"
"v_mfma_f32_16x16x32_bf16 a[144:147], v[144:147], v[80:83], a[144:147]\n"
"s_add_u32 s75, s75, 0x4000\n"
"v_exp_f32 v118, v118\n"
"v_mfma_f32_16x16x32_bf16 a[144:147], v[148:151], v[84:87], a[144:147]\n"
"ds_read_b64_tr_b16 v[80:81], v190 offset:33792\n"
"v_add_f32 v181, v181, v118\n"
"v_mfma_f32_16x16x32_bf16 a[148:151], v[148:151], v[92:95], a[148:151]\n"
"v_exp_f32 v119, v119\n"
"v_mfma_f32_16x16x32_bf16 a[148:151], v[144:147], v[88:91], a[148:151]\n"
"ds_read_b64_tr_b16 v[82:83], v190 offset:37888\n"
"v_add_f32 v181, v181, v119\n"
"v_mfma_f32_16x16x32_bf16 a[152:155], v[144:147], v[96:99], a[152:155]\n"
"v_exp_f32 v120, v120\n"
"v_mfma_f32_16x16x32_bf16 a[152:155], v[148:151], v[100:103], a[152:155]\n"
"ds_read_b64_tr_b16 v[84:85], v190 offset:41984\n"
"v_add_f32 v181, v181, v120\n"
"v_mfma_f32_16x16x32_bf16 a[156:159], v[148:151], v[108:111], a[156:159]\n"
"v_exp_f32 v121, v121\n"
"v_mfma_f32_16x16x32_bf16 a[156:159], v[144:147], v[104:107], a[156:159]\n"
"ds_read_b64_tr_b16 v[86:87], v190 offset:46080\n"
"v_add_f32 v181, v181, v121\n"
"v_mfma_f32_16x16x32_bf16 v[128:131], a[160:163], v[32:35], v[168:171]\n"
"v_exp_f32 v122, v122\n"
"v_mfma_f32_16x16x32_bf16 v[128:131], a[164:167], v[36:39], v[128:131]\n"
"ds_read_b64_tr_b16 v[92:93], v191 offset:41984\n"
"v_add_f32 v181, v181, v122\n"
"v_mfma_f32_16x16x32_bf16 v[128:131], a[168:171], v[40:43], v[128:131]\n"
"v_exp_f32 v123, v123\n"
"v_mfma_f32_16x16x32_bf16 v[128:131], a[172:175], v[44:47], v[128:131]\n"
"ds_read_b64_tr_b16 v[94:95], v191 offset:46080\n"
"v_add_f32 v181, v181, v123\n"
"v_mfma_f32_16x16x32_bf16 v[132:135], a[188:191], v[44:47], v[168:171]\n"
"v_exp_f32 v124, v124\n"
"v_mfma_f32_16x16x32_bf16 v[132:135], a[184:187], v[40:43], v[132:135]\n"
"ds_read_b64_tr_b16 v[88:89], v191 offset:33792\n"
"v_add_f32 v181, v181, v124\n"
"v_mfma_f32_16x16x32_bf16 v[132:135], a[180:183], v[36:39], v[132:135]\n"
"v_exp_f32 v125, v125\n"
"v_mfma_f32_16x16x32_bf16 v[132:135], a[176:179], v[32:35], v[132:135]\n"
"ds_read_b64_tr_b16 v[90:91], v191 offset:37888\n"
"v_add_f32 v181, v181, v125\n"
"v_mfma_f32_16x16x32_bf16 v[136:139], a[192:195], v[32:35], v[168:171]\n"
"v_exp_f32 v126, v126\n"
"v_mfma_f32_16x16x32_bf16 v[136:139], a[196:199], v[36:39], v[136:139]\n"
"ds_read_b64_tr_b16 v[96:97], v192 offset:33792\n"
"v_add_f32 v181, v181, v126\n"
"v_mfma_f32_16x16x32_bf16 v[136:139], a[200:203], v[40:43], v[136:139]\n"
"v_exp_f32 v127, v127\n"
"v_mfma_f32_16x16x32_bf16 v[136:139], a[204:207], v[44:47], v[136:139]\n"
"ds_read_b64_tr_b16 v[98:99], v192 offset:37888\n"
"v_add_f32 v181, v181, v127\n"
"v_mfma_f32_16x16x32_bf16 v[140:143], a[220:223], v[44:47], v[168:171]\n"
"v_exp_f32 v128, v128\n"
"v_mfma_f32_16x16x32_bf16 v[140:143], a[216:219], v[40:43], v[140:143]\n"
"ds_read_b64_tr_b16 v[100:101], v192 offset:41984\n"
"v_add_f32 v182, v182, v128\n"
"v_mfma_f32_16x16x32_bf16 v[140:143], a[212:215], v[36:39], v[140:143]\n"
"v_exp_f32 v129, v129\n"
"v_mfma_f32_16x16x32_bf16 v[140:143], a[208:211], v[32:35], v[140:143]\n"
"ds_read_b64_tr_b16 v[102:103], v192 offset:46080\n"
"v_add_f32 v182, v182, v129\n"
"v_mfma_f32_16x16x32_bf16 a[0:3], v[152:155], v[220:223], a[0:3]\n"
"v_exp_f32 v130, v130\n"
"v_mfma_f32_16x16x32_bf16 a[0:3], v[156:159], v[224:227], a[0:3]\n"
"ds_read_b64_tr_b16 v[108:109], v193 offset:41984\n"
"v_add_f32 v182, v182, v130\n"
"v_mfma_f32_16x16x32_bf16 a[4:7], v[156:159], v[232:235], a[4:7]\n"
"v_exp_f32 v131, v131\n"
"v_mfma_f32_16x16x32_bf16 a[4:7], v[152:155], v[228:231], a[4:7]\n"
"ds_read_b64_tr_b16 v[110:111], v193 offset:46080\n"
"v_add_f32 v182, v182, v131\n"
"v_mfma_f32_16x16x32_bf16 a[8:11], v[152:155], v[236:239], a[8:11]\n"
"v_exp_f32 v132, v132\n"
"v_mfma_f32_16x16x32_bf16 a[8:11], v[156:159], v[240:243], a[8:11]\n"
"ds_read_b64_tr_b16 v[104:105], v193 offset:33792\n"
"v_add_f32 v182, v182, v132\n"
"v_mfma_f32_16x16x32_bf16 a[12:15], v[156:159], v[248:251], a[12:15]\n"
"v_exp_f32 v133, v133\n"
"v_mfma_f32_16x16x32_bf16 a[12:15], v[152:155], v[244:247], a[12:15]\n"
"s_waitcnt lgkmcnt(3)\n"
"ds_read_b64_tr_b16 v[106:107], v193 offset:37888\n"
"v_add_f32 v182, v182, v133\n"
"v_mfma_f32_16x16x32_bf16 a[16:19], v[152:155], v[80:83], a[16:19]\n"
"v_exp_f32 v134, v134\n"
"v_mfma_f32_16x16x32_bf16 a[16:19], v[156:159], v[84:87], a[16:19]\n"
"v_add_f32 v182, v182, v134\n"
"v_cvt_pk_bf16_f32 v144, v112, v113\n"
"v_mfma_f32_16x16x32_bf16 a[20:23], v[156:159], v[92:95], a[20:23]\n"
"v_exp_f32 v135, v135\n"
"v_mfma_f32_16x16x32_bf16 a[20:23], v[152:155], v[88:91], a[20:23]\n"
"v_add_f32 v182, v182, v135\n"
"v_cvt_pk_bf16_f32 v145, v114, v115\n"
"v_mfma_f32_16x16x32_bf16 a[24:27], v[152:155], v[96:99], a[24:27]\n"
"v_exp_f32 v136, v136\n"
"v_mfma_f32_16x16x32_bf16 a[24:27], v[156:159], v[100:103], a[24:27]\n"
"s_waitcnt lgkmcnt(1)\n"
"v_add_f32 v182, v182, v136\n"
"v_cvt_pk_bf16_f32 v146, v116, v117\n"
"v_mfma_f32_16x16x32_bf16 a[28:31], v[156:159], v[108:111], a[28:31]\n"
"s_waitcnt lgkmcnt(0)\n"
"v_exp_f32 v137, v137\n"
"v_mfma_f32_16x16x32_bf16 a[28:31], v[152:155], v[104:107], a[28:31]\n"
"v_add_f32 v182, v182, v137\n"
"v_cvt_pk_bf16_f32 v147, v118, v119\n"
"v_mfma_f32_16x16x32_bf16 v[112:115], a[160:163], v[48:51], v[172:175]\n"
"s_waitcnt vmcnt(8)\n"
"v_exp_f32 v138, v138\n"
"v_mfma_f32_16x16x32_bf16 v[112:115], a[164:167], v[52:55], v[112:115]\n"
"s_barrier\n"
"v_add_f32 v182, v182, v138\n"
"v_cvt_pk_bf16_f32 v148, v120, v121\n"
"v_mfma_f32_16x16x32_bf16 v[112:115], a[168:171], v[56:59], v[112:115]\n"
"v_exp_f32 v139, v139\n"
"v_mfma_f32_16x16x32_bf16 v[112:115], a[172:175], v[60:63], v[112:115]\n"
"v_add_f32 v182, v182, v139\n"
"v_cvt_pk_bf16_f32 v149, v122, v123\n"
"v_mfma_f32_16x16x32_bf16 v[116:119], a[188:191], v[60:63], v[172:175]\n"
"s_add_u32 s76, s26, 0\n"
"ds_read_b64_tr_b16 a[224:225], v194 offset:0\n"
"v_exp_f32 v140, v140\n"
"v_mfma_f32_16x16x32_bf16 v[116:119], a[184:187], v[56:59], v[116:119]\n"
"s_add_u32 m0, s81, 0\n"
"v_add_f32 v182, v182, v140\n"
"v_cvt_pk_bf16_f32 v150, v124, v125\n"
"v_mfma_f32_16x16x32_bf16 v[116:119], a[180:183], v[52:55], v[116:119]\n"
"buffer_load_dwordx4 v199, s[60:63], s76 offen offset:0 lds\n"
"v_exp_f32 v141, v141\n"
"v_mfma_f32_16x16x32_bf16 v[116:119], a[176:179], v[48:51], v[116:119]\n"
"v_add_f32 v182, v182, v141\n"
"v_cvt_pk_bf16_f32 v151, v126, v127\n"
"v_mfma_f32_16x16x32_bf16 v[120:123], a[192:195], v[48:51], v[172:175]\n"
"ds_read_b64_tr_b16 a[226:227], v194 offset:4096\n"
"v_exp_f32 v142, v142\n"
"v_mfma_f32_16x16x32_bf16 v[120:123], a[196:199], v[52:55], v[120:123]\n"
"v_add_f32 v182, v182, v142\n"
"v_cvt_pk_bf16_f32 v152, v128, v129\n"
"v_mfma_f32_16x16x32_bf16 v[120:123], a[200:203], v[56:59], v[120:123]\n"
"v_exp_f32 v143, v143\n"
"v_mfma_f32_16x16x32_bf16 v[120:123], a[204:207], v[60:63], v[120:123]\n"
"v_add_f32 v182, v182, v143\n"
"v_cvt_pk_bf16_f32 v153, v130, v131\n"
"v_mfma_f32_16x16x32_bf16 v[124:127], a[220:223], v[60:63], v[172:175]\n"
"ds_read_b64_tr_b16 a[228:229], v194 offset:8192\n"
"v_exp_f32 v112, v112\n"
"v_mfma_f32_16x16x32_bf16 v[124:127], a[216:219], v[56:59], v[124:127]\n"
"v_add_f32 v183, v183, v112\n"
"v_cvt_pk_bf16_f32 v154, v132, v133\n"
"v_mfma_f32_16x16x32_bf16 v[124:127], a[212:215], v[52:55], v[124:127]\n"
"s_add_u32 s76, s26, 0x2400\n"
"v_exp_f32 v113, v113\n"
"v_mfma_f32_16x16x32_bf16 v[124:127], a[208:211], v[48:51], v[124:127]\n"
"s_add_u32 m0, s81, 0x400\n"
"v_add_f32 v183, v183, v113\n"
"v_cvt_pk_bf16_f32 v155, v134, v135\n"
"v_mfma_f32_16x16x32_bf16 a[32:35], v[144:147], v[220:223], a[32:35]\n"
"buffer_load_dwordx4 v199, s[60:63], s76 offen offset:0 lds\n"
"ds_read_b64_tr_b16 a[230:231], v194 offset:12288\n"
"v_exp_f32 v114, v114\n"
"v_mfma_f32_16x16x32_bf16 a[32:35], v[148:151], v[224:227], a[32:35]\n"
"v_add_f32 v183, v183, v114\n"
"v_cvt_pk_bf16_f32 v156, v136, v137\n"
"v_mfma_f32_16x16x32_bf16 a[36:39], v[148:151], v[232:235], a[36:39]\n"
"v_exp_f32 v115, v115\n"
"v_mfma_f32_16x16x32_bf16 a[36:39], v[144:147], v[228:231], a[36:39]\n"
"v_add_f32 v183, v183, v115\n"
"v_cvt_pk_bf16_f32 v157, v138, v139\n"
"v_mfma_f32_16x16x32_bf16 a[40:43], v[144:147], v[236:239], a[40:43]\n"
"ds_read_b64_tr_b16 a[236:237], v195 offset:8192\n"
"v_exp_f32 v116, v116\n"
"v_mfma_f32_16x16x32_bf16 a[40:43], v[148:151], v[240:243], a[40:43]\n"
"v_add_f32 v183, v183, v116\n"
"v_cvt_pk_bf16_f32 v158, v140, v141\n"
"v_mfma_f32_16x16x32_bf16 a[44:47], v[148:151], v[248:251], a[44:47]\n"
"v_exp_f32 v117, v117\n"
"v_mfma_f32_16x16x32_bf16 a[44:47], v[144:147], v[244:247], a[44:47]\n"
"v_add_f32 v183, v183, v117\n"
"v_cvt_pk_bf16_f32 v159, v142, v143\n"
"v_mfma_f32_16x16x32_bf16 a[48:51], v[144:147], v[80:83], a[48:51]\n"
"s_add_u32 s76, s26, 0x4800\n"
"ds_read_b64_tr_b16 a[238:239], v195 offset:12288\n"
"v_exp_f32 v118, v118\n"
"v_mfma_f32_16x16x32_bf16 a[48:51], v[148:151], v[84:87], a[48:51]\n"
"s_add_u32 m0, s81, 0x800\n"
"ds_read_b64_tr_b16 a[232:233], v195 offset:0\n"
"v_add_f32 v183, v183, v118\n"
"v_mfma_f32_16x16x32_bf16 a[52:55], v[148:151], v[92:95], a[52:55]\n"
"buffer_load_dwordx4 v199, s[60:63], s76 offen offset:0 lds\n"
"v_exp_f32 v119, v119\n"
"v_mfma_f32_16x16x32_bf16 a[52:55], v[144:147], v[88:91], a[52:55]\n"
"ds_read_b64_tr_b16 a[234:235], v195 offset:4096\n"
"v_add_f32 v183, v183, v119\n"
"v_mfma_f32_16x16x32_bf16 a[56:59], v[144:147], v[96:99], a[56:59]\n"
"v_exp_f32 v120, v120\n"
"v_mfma_f32_16x16x32_bf16 a[56:59], v[148:151], v[100:103], a[56:59]\n"
"ds_read_b64_tr_b16 a[240:241], v196 offset:0\n"
"v_add_f32 v183, v183, v120\n"
"v_mfma_f32_16x16x32_bf16 a[60:63], v[148:151], v[108:111], a[60:63]\n"
"v_exp_f32 v121, v121\n"
"v_mfma_f32_16x16x32_bf16 a[60:63], v[144:147], v[104:107], a[60:63]\n"
"ds_read_b64_tr_b16 a[242:243], v196 offset:4096\n"
"v_add_f32 v183, v183, v121\n"
"v_mfma_f32_16x16x32_bf16 v[128:131], a[160:163], v[64:67], v[176:179]\n"
"v_exp_f32 v122, v122\n"
"v_mfma_f32_16x16x32_bf16 v[128:131], a[164:167], v[68:71], v[128:131]\n"
"ds_read_b128 a[160:163], v188 offset:0\n"
"v_add_f32 v183, v183, v122\n"
"v_mfma_f32_16x16x32_bf16 v[128:131], a[168:171], v[72:75], v[128:131]\n"
"s_add_u32 s76, s26, 0x6c00\n"
"v_exp_f32 v123, v123\n"
"v_mfma_f32_16x16x32_bf16 v[128:131], a[172:175], v[76:79], v[128:131]\n"
"s_add_u32 m0, s81, 0xc00\n"
"ds_read_b128 a[164:167], v189 offset:0\n"
"v_add_f32 v183, v183, v123\n"
"v_mfma_f32_16x16x32_bf16 v[132:135], a[188:191], v[76:79], v[176:179]\n"
"buffer_load_dwordx4 v199, s[60:63], s76 offen offset:0 lds\n"
"v_exp_f32 v124, v124\n"
"v_mfma_f32_16x16x32_bf16 v[132:135], a[184:187], v[72:75], v[132:135]\n"
"ds_read_b128 a[168:171], v188 offset:1024\n"
"v_add_f32 v183, v183, v124\n"
"v_mfma_f32_16x16x32_bf16 v[132:135], a[180:183], v[68:71], v[132:135]\n"
"v_exp_f32 v125, v125\n"
"v_mfma_f32_16x16x32_bf16 v[132:135], a[176:179], v[64:67], v[132:135]\n"
"ds_read_b128 a[172:175], v189 offset:1024\n"
"v_add_f32 v183, v183, v125\n"
"v_mfma_f32_16x16x32_bf16 v[136:139], a[192:195], v[64:67], v[176:179]\n"
"v_exp_f32 v126, v126\n"
"v_mfma_f32_16x16x32_bf16 v[136:139], a[196:199], v[68:71], v[136:139]\n"
"ds_read_b128 a[188:191], v189 offset:5120\n"
"v_add_f32 v183, v183, v126\n"
"v_mfma_f32_16x16x32_bf16 v[136:139], a[200:203], v[72:75], v[136:139]\n"
"v_exp_f32 v127, v127\n"
"v_mfma_f32_16x16x32_bf16 v[136:139], a[204:207], v[76:79], v[136:139]\n"
"ds_read_b128 a[184:187], v188 offset:5120\n"
"v_add_f32 v183, v183, v127\n"
"v_mfma_f32_16x16x32_bf16 v[140:143], a[220:223], v[76:79], v[176:179]\n"
"s_add_u32 s76, s26, 0x9000\n"
"ds_read_b128 a[180:183], v189 offset:4096\n"
"v_exp_f32 v128, v128\n"
"v_mfma_f32_16x16x32_bf16 v[140:143], a[216:219], v[72:75], v[140:143]\n"
"s_add_u32 m0, s81, 0x1000\n"
"v_add_f32 v184, v184, v128\n"
"v_cvt_pk_bf16_f32 v144, v112, v113\n"
"v_mfma_f32_16x16x32_bf16 v[140:143], a[212:215], v[68:71], v[140:143]\n"
"buffer_load_dwordx4 v199, s[60:63], s76 offen offset:0 lds\n"
"v_exp_f32 v129, v129\n"
"v_mfma_f32_16x16x32_bf16 v[140:143], a[208:211], v[64:67], v[140:143]\n"
"v_add_f32 v184, v184, v129\n"
"v_cvt_pk_bf16_f32 v145, v114, v115\n"
"v_mfma_f32_16x16x32_bf16 a[64:67], v[152:155], v[220:223], a[64:67]\n"
"v_exp_f32 v130, v130\n"
"v_mfma_f32_16x16x32_bf16 a[64:67], v[156:159], v[224:227], a[64:67]\n"
"ds_read_b128 a[176:179], v188 offset:4096\n"
"v_add_f32 v184, v184, v130\n"
"v_mfma_f32_16x16x32_bf16 a[68:71], v[156:159], v[232:235], a[68:71]\n"
"v_exp_f32 v131, v131\n"
"v_mfma_f32_16x16x32_bf16 a[68:71], v[152:155], v[228:231], a[68:71]\n"
"ds_read_b128 a[192:195], v188 offset:8192\n"
"v_add_f32 v184, v184, v131\n"
"v_mfma_f32_16x16x32_bf16 a[72:75], v[152:155], v[236:239], a[72:75]\n"
"v_exp_f32 v132, v132\n"
"v_mfma_f32_16x16x32_bf16 a[72:75], v[156:159], v[240:243], a[72:75]\n"
"ds_read_b128 a[196:199], v189 offset:8192\n"
"v_add_f32 v184, v184, v132\n"
"v_mfma_f32_16x16x32_bf16 a[76:79], v[156:159], v[248:251], a[76:79]\n"
"s_add_u32 s76, s26, 0xb400\n"
"v_exp_f32 v133, v133\n"
"v_mfma_f32_16x16x32_bf16 a[76:79], v[152:155], v[244:247], a[76:79]\n"
"s_add_u32 m0, s81, 0x1400\n"
"ds_read_b128 a[200:203], v188 offset:9216\n"
"v_add_f32 v184, v184, v133\n"
"v_mfma_f32_16x16x32_bf16 a[80:83], v[152:155], v[80:83], a[80:83]\n"
"buffer_load_dwordx4 v199, s[60:63], s76 offen offset:0 lds\n"
"v_exp_f32 v134, v134\n"
"v_cvt_pk_bf16_f32 v146, v116, v117\n"
"v_mfma_f32_16x16x32_bf16 a[80:83], v[156:159], v[84:87], a[80:83]\n"
"v_add_f32 v184, v184, v134\n"
"v_cvt_pk_bf16_f32 v147, v118, v119\n"
"v_mfma_f32_16x16x32_bf16 a[84:87], v[156:159], v[92:95], a[84:87]\n"
"v_exp_f32 v135, v135\n"
"v_mfma_f32_16x16x32_bf16 a[84:87], v[152:155], v[88:91], a[84:87]\n"
"ds_read_b128 a[204:207], v189 offset:9216\n"
"v_add_f32 v184, v184, v135\n"
"v_mfma_f32_16x16x32_bf16 a[88:91], v[152:155], v[96:99], a[88:91]\n"
"v_exp_f32 v136, v136\n"
"v_mfma_f32_16x16x32_bf16 a[88:91], v[156:159], v[100:103], a[88:91]\n"
"ds_read_b128 a[220:223], v189 offset:13312\n"
"v_add_f32 v184, v184, v136\n"
"v_mfma_f32_16x16x32_bf16 a[92:95], v[156:159], v[108:111], a[92:95]\n"
"v_exp_f32 v137, v137\n"
"v_mfma_f32_16x16x32_bf16 a[92:95], v[152:155], v[104:107], a[92:95]\n"
"s_waitcnt lgkmcnt(2)\n"
"ds_read_b128 a[216:219], v188 offset:13312\n"
"v_add_f32 v184, v184, v137\n"
"v_mfma_f32_16x16x32_bf16 v[112:115], a[160:163], v[0:3], v[160:163]\n"
"s_add_u32 s76, s26, 0xd800\n"
"v_exp_f32 v138, v138\n"
"v_mfma_f32_16x16x32_bf16 v[112:115], a[164:167], v[4:7], v[112:115]\n"
"s_add_u32 m0, s81, 0x1800\n"
"ds_read_b128 a[212:215], v189 offset:12288\n"
"v_add_f32 v184, v184, v138\n"
"v_mfma_f32_16x16x32_bf16 v[112:115], a[168:171], v[8:11], v[112:115]\n"
"buffer_load_dwordx4 v199, s[60:63], s76 offen offset:0 lds\n"
"v_exp_f32 v139, v139\n"
"v_mfma_f32_16x16x32_bf16 v[112:115], a[172:175], v[12:15], v[112:115]\n"
"v_add_f32 v184, v184, v139\n"
"v_cvt_pk_bf16_f32 v148, v120, v121\n"
"v_mfma_f32_16x16x32_bf16 v[116:119], a[188:191], v[12:15], v[160:163]\n"
"v_exp_f32 v140, v140\n"
"v_mfma_f32_16x16x32_bf16 v[116:119], a[184:187], v[8:11], v[116:119]\n"
"v_add_f32 v184, v184, v140\n"
"v_cvt_pk_bf16_f32 v149, v122, v123\n"
"v_mfma_f32_16x16x32_bf16 v[116:119], a[180:183], v[4:7], v[116:119]\n"
"ds_read_b128 a[208:211], v188 offset:12288\n"
"v_exp_f32 v141, v141\n"
"v_mfma_f32_16x16x32_bf16 v[116:119], a[176:179], v[0:3], v[116:119]\n"
"v_add_f32 v184, v184, v141\n"
"v_cvt_pk_bf16_f32 v150, v124, v125\n"
"v_mfma_f32_16x16x32_bf16 v[120:123], a[192:195], v[0:3], v[160:163]\n"
"v_exp_f32 v142, v142\n"
"v_mfma_f32_16x16x32_bf16 v[120:123], a[196:199], v[4:7], v[120:123]\n"
"s_waitcnt lgkmcnt(1)\n"
"v_add_f32 v184, v184, v142\n"
"v_cvt_pk_bf16_f32 v151, v126, v127\n"
"v_mfma_f32_16x16x32_bf16 v[120:123], a[200:203], v[8:11], v[120:123]\n"
"s_add_u32 s76, s26, 0xfc00\n"
"v_exp_f32 v143, v143\n"
"v_mfma_f32_16x16x32_bf16 v[120:123], a[204:207], v[12:15], v[120:123]\n"
"s_add_u32 m0, s81, 0x1c00\n"
"v_add_f32 v184, v184, v143\n"
"v_cvt_pk_bf16_f32 v152, v128, v129\n"
"v_mfma_f32_16x16x32_bf16 v[124:127], a[220:223], v[12:15], v[160:163]\n"
"buffer_load_dwordx4 v199, s[60:63], s76 offen offset:0 lds\n"
"v_exp_f32 v112, v112\n"
"v_mfma_f32_16x16x32_bf16 v[124:127], a[216:219], v[8:11], v[124:127]\n"
"v_cvt_pk_bf16_f32 v153, v130, v131\n"
"v_add_f32 v180, v180, v112\n"
"v_mfma_f32_16x16x32_bf16 v[124:127], a[212:215], v[4:7], v[124:127]\n"
"s_waitcnt lgkmcnt(0)\n"
"ds_read_b64_tr_b16 a[244:245], v196 offset:8192\n"
"v_exp_f32 v113, v113\n"
"v_mfma_f32_16x16x32_bf16 v[124:127], a[208:211], v[0:3], v[124:127]\n"
"v_cvt_pk_bf16_f32 v154, v132, v133\n"
"v_add_f32 v180, v180, v113\n"
"v_mfma_f32_16x16x32_bf16 a[96:99], v[144:147], v[220:223], a[96:99]\n"
"v_exp_f32 v114, v114\n"
"v_mfma_f32_16x16x32_bf16 a[96:99], v[148:151], v[224:227], a[96:99]\n"
"v_cvt_pk_bf16_f32 v155, v134, v135\n"
"v_add_f32 v180, v180, v114\n"
"v_mfma_f32_16x16x32_bf16 a[100:103], v[148:151], v[232:235], a[100:103]\n"
"v_exp_f32 v115, v115\n"
"v_mfma_f32_16x16x32_bf16 a[100:103], v[144:147], v[228:231], a[100:103]\n"
"v_cvt_pk_bf16_f32 v156, v136, v137\n"
"v_add_f32 v180, v180, v115\n"
"v_mfma_f32_16x16x32_bf16 a[104:107], v[144:147], v[236:239], a[104:107]\n"
"ds_read_b64_tr_b16 a[246:247], v196 offset:12288\n"
"v_exp_f32 v116, v116\n"
"v_mfma_f32_16x16x32_bf16 a[104:107], v[148:151], v[240:243], a[104:107]\n"
"v_cvt_pk_bf16_f32 v157, v138, v139\n"
"v_add_f32 v180, v180, v116\n"
"v_mfma_f32_16x16x32_bf16 a[108:111], v[148:151], v[248:251], a[108:111]\n"
"v_exp_f32 v117, v117\n"
"v_mfma_f32_16x16x32_bf16 a[108:111], v[144:147], v[244:247], a[108:111]\n"
"v_cvt_pk_bf16_f32 v158, v140, v141\n"
"v_add_f32 v180, v180, v117\n"
"v_mfma_f32_16x16x32_bf16 a[112:115], v[144:147], v[80:83], a[112:115]\n"
"v_exp_f32 v118, v118\n"
"v_mfma_f32_16x16x32_bf16 a[112:115], v[148:151], v[84:87], a[112:115]\n"
"v_cvt_pk_bf16_f32 v159, v142, v143\n"
"v_add_f32 v180, v180, v118\n"
"v_mfma_f32_16x16x32_bf16 a[116:119], v[148:151], v[92:95], a[116:119]\n"
"ds_read_b64_tr_b16 a[252:253], v197 offset:8192\n"
"v_exp_f32 v119, v119\n"
"v_mfma_f32_16x16x32_bf16 a[116:119], v[144:147], v[88:91], a[116:119]\n"
"ds_read_b64_tr_b16 a[254:255], v197 offset:12288\n"
"v_add_f32 v180, v180, v119\n"
"v_mfma_f32_16x16x32_bf16 a[120:123], v[144:147], v[96:99], a[120:123]\n"
"v_exp_f32 v120, v120\n"
"v_mfma_f32_16x16x32_bf16 a[120:123], v[148:151], v[100:103], a[120:123]\n"
"ds_read_b64_tr_b16 a[248:249], v197 offset:0\n"
"v_add_f32 v180, v180, v120\n"
"v_mfma_f32_16x16x32_bf16 a[124:127], v[148:151], v[108:111], a[124:127]\n"
"v_exp_f32 v121, v121\n"
"v_mfma_f32_16x16x32_bf16 a[124:127], v[144:147], v[104:107], a[124:127]\n"
"ds_read_b64_tr_b16 a[250:251], v197 offset:4096\n"
"v_add_f32 v180, v180, v121\n"
"v_mfma_f32_16x16x32_bf16 v[128:131], a[160:163], v[16:19], v[164:167]\n"
"s_add_u32 s76, s26, 0x80\n"
"v_exp_f32 v122, v122\n"
"v_mfma_f32_16x16x32_bf16 v[128:131], a[164:167], v[20:23], v[128:131]\n"
"s_add_u32 m0, s81, 0x8000\n"
"v_add_f32 v180, v180, v122\n"
"v_cvt_pk_bf16_f32 v144, v112, v113\n"
"v_mfma_f32_16x16x32_bf16 v[128:131], a[168:171], v[24:27], v[128:131]\n"
"buffer_load_dwordx4 v199, s[60:63], s76 offen offset:0 lds\n"
"v_exp_f32 v123, v123\n"
"v_mfma_f32_16x16x32_bf16 v[128:131], a[172:175], v[28:31], v[128:131]\n"
"s_add_u32 s76, s26, 0x2480\n"
"v_add_f32 v180, v180, v123\n"
"v_cvt_pk_bf16_f32 v145, v114, v115\n"
"v_mfma_f32_16x16x32_bf16 v[132:135], a[188:191], v[28:31], v[164:167]\n"
"s_add_u32 m0, s81, 0x8400\n"
"v_exp_f32 v124, v124\n"
"v_mfma_f32_16x16x32_bf16 v[132:135], a[184:187], v[24:27], v[132:135]\n"
"buffer_load_dwordx4 v199, s[60:63], s76 offen offset:0 lds\n"
"v_add_f32 v180, v180, v124\n"
"v_mfma_f32_16x16x32_bf16 v[132:135], a[180:183], v[20:23], v[132:135]\n"
"s_add_u32 s76, s26, 0x4880\n"
"v_exp_f32 v125, v125\n"
"v_mfma_f32_16x16x32_bf16 v[132:135], a[176:179], v[16:19], v[132:135]\n"
"s_add_u32 m0, s81, 0x8800\n"
"v_add_f32 v180, v180, v125\n"
"v_cvt_pk_bf16_f32 v146, v116, v117\n"
"v_mfma_f32_16x16x32_bf16 v[136:139], a[192:195], v[16:19], v[164:167]\n"
"buffer_load_dwordx4 v199, s[60:63], s76 offen offset:0 lds\n"
"v_exp_f32 v126, v126\n"
"v_mfma_f32_16x16x32_bf16 v[136:139], a[196:199], v[20:23], v[136:139]\n"
"s_add_u32 s76, s26, 0x6c80\n"
"v_add_f32 v180, v180, v126\n"
"v_cvt_pk_bf16_f32 v147, v118, v119\n"
"v_mfma_f32_16x16x32_bf16 v[136:139], a[200:203], v[24:27], v[136:139]\n"
"s_add_u32 m0, s81, 0x8c00\n"
"v_exp_f32 v127, v127\n"
"v_mfma_f32_16x16x32_bf16 v[136:139], a[204:207], v[28:31], v[136:139]\n"
"buffer_load_dwordx4 v199, s[60:63], s76 offen offset:0 lds\n"
"v_add_f32 v180, v180, v127\n"
"v_mfma_f32_16x16x32_bf16 v[140:143], a[220:223], v[28:31], v[164:167]\n"
"s_add_u32 s76, s26, 0x9080\n"
"v_exp_f32 v128, v128\n"
"v_mfma_f32_16x16x32_bf16 v[140:143], a[216:219], v[24:27], v[140:143]\n"
"s_add_u32 m0, s81, 0x9000\n"
"v_add_f32 v181, v181, v128\n"
"v_cvt_pk_bf16_f32 v148, v120, v121\n"
"v_mfma_f32_16x16x32_bf16 v[140:143], a[212:215], v[20:23], v[140:143]\n"
"buffer_load_dwordx4 v199, s[60:63], s76 offen offset:0 lds\n"
"v_exp_f32 v129, v129\n"
"v_mfma_f32_16x16x32_bf16 v[140:143], a[208:211], v[16:19], v[140:143]\n"
"s_waitcnt lgkmcnt(0)\n"
"v_add_f32 v181, v181, v129\n"
"v_cvt_pk_bf16_f32 v149, v122, v123\n"
"v_mfma_f32_16x16x32_bf16 a[128:131], v[152:155], v[220:223], a[128:131]\n"
"s_add_u32 s76, s26, 0xb480\n"
"v_exp_f32 v130, v130\n"
"v_mfma_f32_16x16x32_bf16 a[128:131], v[156:159], v[224:227], a[128:131]\n"
"s_add_u32 m0, s81, 0x9400\n"
"v_add_f32 v181, v181, v130\n"
"v_mfma_f32_16x16x32_bf16 a[132:135], v[156:159], v[232:235], a[132:135]\n"
"buffer_load_dwordx4 v199, s[60:63], s76 offen offset:0 lds\n"
"v_exp_f32 v131, v131\n"
"v_mfma_f32_16x16x32_bf16 a[132:135], v[152:155], v[228:231], a[132:135]\n"
"s_add_u32 s76, s26, 0xd880\n"
"v_add_f32 v181, v181, v131\n"
"v_cvt_pk_bf16_f32 v150, v124, v125\n"
"v_mfma_f32_16x16x32_bf16 a[136:139], v[152:155], v[236:239], a[136:139]\n"
"s_add_u32 m0, s81, 0x9800\n"
"v_exp_f32 v132, v132\n"
"v_mfma_f32_16x16x32_bf16 a[136:139], v[156:159], v[240:243], a[136:139]\n"
"buffer_load_dwordx4 v199, s[60:63], s76 offen offset:0 lds\n"
"v_add_f32 v181, v181, v132\n"
"v_cvt_pk_bf16_f32 v151, v126, v127\n"
"v_mfma_f32_16x16x32_bf16 a[140:143], v[156:159], v[248:251], a[140:143]\n"
"s_add_u32 s76, s26, 0xfc80\n"
"v_exp_f32 v133, v133\n"
"v_mfma_f32_16x16x32_bf16 a[140:143], v[152:155], v[244:247], a[140:143]\n"
"s_add_u32 m0, s81, 0x9c00\n"
"v_add_f32 v181, v181, v133\n"
"v_mfma_f32_16x16x32_bf16 a[144:147], v[152:155], v[80:83], a[144:147]\n"
"buffer_load_dwordx4 v199, s[60:63], s76 offen offset:0 lds\n"
"v_exp_f32 v134, v134\n"
"v_mfma_f32_16x16x32_bf16 a[144:147], v[156:159], v[84:87], a[144:147]\n"
"ds_read_b64_tr_b16 v[80:81], v194 offset:1024\n"
"v_add_f32 v181, v181, v134\n"
"v_mfma_f32_16x16x32_bf16 a[148:151], v[156:159], v[92:95], a[148:151]\n"
"v_exp_f32 v135, v135\n"
"v_mfma_f32_16x16x32_bf16 a[148:151], v[152:155], v[88:91], a[148:151]\n"
"ds_read_b64_tr_b16 v[82:83], v194 offset:5120\n"
"v_add_f32 v181, v181, v135\n"
"v_mfma_f32_16x16x32_bf16 a[152:155], v[152:155], v[96:99], a[152:155]\n"
"v_exp_f32 v136, v136\n"
"v_mfma_f32_16x16x32_bf16 a[152:155], v[156:159], v[100:103], a[152:155]\n"
"ds_read_b64_tr_b16 v[84:85], v194 offset:9216\n"
"v_add_f32 v181, v181, v136\n"
"v_mfma_f32_16x16x32_bf16 a[156:159], v[156:159], v[108:111], a[156:159]\n"
"s_and_b32 s40, s42, 1\n"
"v_exp_f32 v137, v137\n"
"v_mfma_f32_16x16x32_bf16 a[156:159], v[152:155], v[104:107], a[156:159]\n"
"s_lshr_b32 s87, s42, 1\n"
"ds_read_b64_tr_b16 v[86:87], v194 offset:13312\n"
"v_add_f32 v181, v181, v137\n"
"v_mfma_f32_16x16x32_bf16 v[112:115], a[160:163], v[32:35], v[168:171]\n"
"s_lshl_b32 s86, s41, 3\n"
"v_exp_f32 v138, v138\n"
"v_mfma_f32_16x16x32_bf16 v[112:115], a[164:167], v[36:39], v[112:115]\n"
"s_add_u32 s86, s86, s43\n"
"ds_read_b64_tr_b16 v[92:93], v195 offset:9216\n"
"v_add_f32 v181, v181, v138\n"
"v_mfma_f32_16x16x32_bf16 v[112:115], a[168:171], v[40:43], v[112:115]\n"
"s_lshl_b32 s86, s86, 4\n"
"v_exp_f32 v139, v139\n"
"v_mfma_f32_16x16x32_bf16 v[112:115], a[172:175], v[44:47], v[112:115]\n"
"s_add_u32 s86, s86, s42\n"
"ds_read_b64_tr_b16 v[94:95], v195 offset:13312\n"
"v_add_f32 v181, v181, v139\n"
"v_mfma_f32_16x16x32_bf16 v[116:119], a[188:191], v[44:47], v[168:171]\n"
"s_lshl_b32 s89, s19, 9\n"
"v_exp_f32 v140, v140\n"
"v_mfma_f32_16x16x32_bf16 v[116:119], a[184:187], v[40:43], v[116:119]\n"
"s_add_u32 s89, s89, 0x27000\n"
"ds_read_b64_tr_b16 v[88:89], v195 offset:1024\n"
"v_add_f32 v181, v181, v140\n"
"v_mfma_f32_16x16x32_bf16 v[116:119], a[180:183], v[36:39], v[116:119]\n"
"s_lshr_b32 s88, s19, 1\n"
"v_exp_f32 v141, v141\n"
"v_mfma_f32_16x16x32_bf16 v[116:119], a[176:179], v[32:35], v[116:119]\n"
"s_xor_b32 s88, s88, s40\n"
"ds_read_b64_tr_b16 v[90:91], v195 offset:5120\n"
"v_add_f32 v181, v181, v141\n"
"v_mfma_f32_16x16x32_bf16 v[120:123], a[192:195], v[32:35], v[168:171]\n"
"s_and_b32 s90, s19, 1\n"
"v_exp_f32 v142, v142\n"
"v_mfma_f32_16x16x32_bf16 v[120:123], a[196:199], v[36:39], v[120:123]\n"
"s_mul_i32 s90, s90, 0x5000\n"
"ds_read_b64_tr_b16 v[96:97], v196 offset:1024\n"
"v_add_f32 v181, v181, v142\n"
"v_mfma_f32_16x16x32_bf16 v[120:123], a[200:203], v[40:43], v[120:123]\n"
"s_and_b32 s22, s19, 1\n"
"v_exp_f32 v143, v143\n"
"v_mfma_f32_16x16x32_bf16 v[120:123], a[204:207], v[44:47], v[120:123]\n"
"s_mul_i32 s96, s22, 0x1800\n"
"ds_read_b64_tr_b16 v[98:99], v196 offset:5120\n"
"v_add_f32 v181, v181, v143\n"
"v_mfma_f32_16x16x32_bf16 v[124:127], a[220:223], v[44:47], v[168:171]\n"
"s_mul_i32 s97, s22, 0x1000\n"
"v_exp_f32 v112, v112\n"
"v_mfma_f32_16x16x32_bf16 v[124:127], a[216:219], v[40:43], v[124:127]\n"
"s_add_u32 s97, s97, 0x3000\n"
"ds_read_b64_tr_b16 v[100:101], v196 offset:9216\n"
"v_add_f32 v182, v182, v112\n"
"v_mfma_f32_16x16x32_bf16 v[124:127], a[212:215], v[36:39], v[124:127]\n"
"s_lshr_b32 s21, s19, 1\n"
"v_exp_f32 v113, v113\n"
"v_mfma_f32_16x16x32_bf16 v[124:127], a[208:211], v[32:35], v[124:127]\n"
"s_mul_i32 s21, s21, 0x5000\n"
"ds_read_b64_tr_b16 v[102:103], v196 offset:13312\n"
"v_add_f32 v182, v182, v113\n"
"v_mfma_f32_16x16x32_bf16 a[0:3], v[144:147], a[224:227], a[0:3]\n"
"s_add_u32 s91, s21, 0x18000\n"
"v_exp_f32 v114, v114\n"
"v_mfma_f32_16x16x32_bf16 a[0:3], v[148:151], a[228:231], a[0:3]\n"
"s_mul_i32 s23, s86, 0xa000\n"
"ds_read_b64_tr_b16 v[108:109], v197 offset:9216\n"
"v_add_f32 v182, v182, v114\n"
"v_mfma_f32_16x16x32_bf16 a[4:7], v[148:151], a[236:239], a[4:7]\n"
"s_add_u32 s23, s23, 0\n"
"v_exp_f32 v115, v115\n"
"v_mfma_f32_16x16x32_bf16 a[4:7], v[144:147], a[232:235], a[4:7]\n"
"s_add_u32 s23, s23, s90\n"
"ds_read_b64_tr_b16 v[110:111], v197 offset:13312\n"
"v_add_f32 v182, v182, v115\n"
"v_mfma_f32_16x16x32_bf16 a[8:11], v[144:147], a[240:243], a[8:11]\n"
"s_add_u32 s44, s72, s23\ns_addc_u32 s45, s73, 0\n"
"v_exp_f32 v116, v116\n"
"v_mfma_f32_16x16x32_bf16 a[8:11], v[148:151], a[244:247], a[8:11]\n"
"s_and_b32 s45, s45, 0xffff\n"
"ds_read_b64_tr_b16 v[104:105], v197 offset:1024\n"
"v_add_f32 v182, v182, v116\n"
"v_mfma_f32_16x16x32_bf16 a[12:15], v[148:151], a[252:255], a[12:15]\n"
"s_mov_b32 s46, 0x5000\n"
"v_exp_f32 v117, v117\n"
"v_mfma_f32_16x16x32_bf16 a[12:15], v[144:147], a[248:251], a[12:15]\n"
"s_waitcnt lgkmcnt(3)\n"
"s_mov_b32 s47, 0x20000\n"
"ds_read_b64_tr_b16 v[106:107], v197 offset:5120\n"
"v_add_f32 v182, v182, v117\n"
"v_mfma_f32_16x16x32_bf16 a[16:19], v[144:147], v[80:83], a[16:19]\n"
"s_add_u32 s90, s90, 0x18000\n"
"v_exp_f32 v118, v118\n"
"v_mfma_f32_16x16x32_bf16 a[16:19], v[148:151], v[84:87], a[16:19]\n"
"s_xor_b32 s23, s86, 1\n"
"v_add_f32 v182, v182, v118\n"
"v_cvt_pk_bf16_f32 v152, v128, v129\n"
"v_mfma_f32_16x16x32_bf16 a[20:23], v[148:151], v[92:95], a[20:23]\n"
"s_mul_i32 s23, s23, 0xa000\n"
"v_exp_f32 v119, v119\n"
"v_mfma_f32_16x16x32_bf16 a[20:23], v[144:147], v[88:91], a[20:23]\n"
"s_add_u32 s23, s23, s21\n"
"v_add_f32 v182, v182, v119\n"
"v_cvt_pk_bf16_f32 v153, v130, v131\n"
"v_mfma_f32_16x16x32_bf16 a[24:27], v[144:147], v[96:99], a[24:27]\n"
"s_add_u32 s23, s23, 0\n"
"v_exp_f32 v120, v120\n"
"v_mfma_f32_16x16x32_bf16 a[24:27], v[148:151], v[100:103], a[24:27]\n"
"s_waitcnt lgkmcnt(1)\n"
"s_add_u32 s48, s72, s23\ns_addc_u32 s49, s73, 0\n"
"v_add_f32 v182, v182, v120\n"
"v_cvt_pk_bf16_f32 v154, v132, v133\n"
"v_mfma_f32_16x16x32_bf16 a[28:31], v[148:151], v[108:111], a[28:31]\n"
"s_waitcnt lgkmcnt(0)\n"
"s_and_b32 s49, s49, 0xffff\n"
"v_exp_f32 v121, v121\n"
"v_mfma_f32_16x16x32_bf16 a[28:31], v[144:147], v[104:107], a[28:31]\n"
"s_mov_b32 s50, 0x5000\n"
"v_add_f32 v182, v182, v121\n"
"v_cvt_pk_bf16_f32 v155, v134, v135\n"
"v_mfma_f32_16x16x32_bf16 v[128:131], a[160:163], v[48:51], v[172:175]\n"
"s_waitcnt vmcnt(16)\n"
"v_exp_f32 v122, v122\n"
"v_mfma_f32_16x16x32_bf16 v[128:131], a[164:167], v[52:55], v[128:131]\n"
"s_barrier\n"
"v_add_f32 v182, v182, v122\n"
"v_cvt_pk_bf16_f32 v156, v136, v137\n"
"v_mfma_f32_16x16x32_bf16 v[128:131], a[168:171], v[56:59], v[128:131]\n"
"s_mov_b32 s51, 0x20000\n"
"buffer_load_dword v212, v185, s[60:63], 0 offen offset:0 \n"
"v_exp_f32 v123, v123\n"
"v_mfma_f32_16x16x32_bf16 v[128:131], a[172:175], v[60:63], v[128:131]\n"
"s_lshl_b32 s92, s19, 1\n"
"v_add_f32 v182, v182, v123\n"
"v_cvt_pk_bf16_f32 v157, v138, v139\n"
"v_mfma_f32_16x16x32_bf16 v[132:135], a[188:191], v[60:63], v[172:175]\n"
"s_movk_i32 s93, 0x900\n"
"ds_read_b64_tr_b16 v[220:221], v194 offset:32768\n"
"v_exp_f32 v124, v124\n"
"v_mfma_f32_16x16x32_bf16 v[132:135], a[184:187], v[56:59], v[132:135]\n"
"s_lshl_b32 s94, s87, 2\n"
"v_add_f32 v182, v182, v124\n"
"v_cvt_pk_bf16_f32 v158, v140, v141\n"
"v_mfma_f32_16x16x32_bf16 v[132:135], a[180:183], v[52:55], v[132:135]\n"
"s_add_u32 s94, s94, 32\n"
"v_exp_f32 v125, v125\n"
"v_mfma_f32_16x16x32_bf16 v[132:135], a[176:179], v[48:51], v[132:135]\n"
"s_add_u32 s25, s87, 1\n"
"v_add_f32 v182, v182, v125\n"
"v_cvt_pk_bf16_f32 v159, v142, v143\n"
"v_mfma_f32_16x16x32_bf16 v[136:139], a[192:195], v[48:51], v[172:175]\n"
"s_lshl_b32 s37, s19, 10\n"
"ds_read_b64_tr_b16 v[222:223], v194 offset:36864\n"
"v_exp_f32 v126, v126\n"
"v_mfma_f32_16x16x32_bf16 v[136:139], a[196:199], v[52:55], v[136:139]\n"
"v_add_f32 v182, v182, v126\n"
"v_cvt_pk_bf16_f32 v144, v112, v113\n"
"v_mfma_f32_16x16x32_bf16 v[136:139], a[200:203], v[56:59], v[136:139]\n"
"v_exp_f32 v127, v127\n"
"v_mfma_f32_16x16x32_bf16 v[136:139], a[204:207], v[60:63], v[136:139]\n"
"v_add_f32 v182, v182, v127\n"
"v_cvt_pk_bf16_f32 v145, v114, v115\n"
"v_mfma_f32_16x16x32_bf16 v[140:143], a[220:223], v[60:63], v[172:175]\n"
"ds_read_b64_tr_b16 v[224:225], v194 offset:40960\n"
"v_exp_f32 v128, v128\n"
"v_mfma_f32_16x16x32_bf16 v[140:143], a[216:219], v[56:59], v[140:143]\n"
"v_add_f32 v183, v183, v128\n"
"v_cvt_pk_bf16_f32 v146, v116, v117\n"
"v_mfma_f32_16x16x32_bf16 v[140:143], a[212:215], v[52:55], v[140:143]\n"
"v_exp_f32 v129, v129\n"
"v_mfma_f32_16x16x32_bf16 v[140:143], a[208:211], v[48:51], v[140:143]\n"
"v_add_f32 v183, v183, v129\n"
"v_cvt_pk_bf16_f32 v147, v118, v119\n"
"v_mfma_f32_16x16x32_bf16 a[32:35], v[152:155], a[224:227], a[32:35]\n"
"ds_read_b64_tr_b16 v[226:227], v194 offset:45056\n"
"v_exp_f32 v130, v130\n"
"v_mfma_f32_16x16x32_bf16 a[32:35], v[156:159], a[228:231], a[32:35]\n"
"v_add_f32 v183, v183, v130\n"
"v_cvt_pk_bf16_f32 v148, v120, v121\n"
"v_mfma_f32_16x16x32_bf16 a[36:39], v[156:159], a[236:239], a[36:39]\n"
"v_exp_f32 v131, v131\n"
"v_mfma_f32_16x16x32_bf16 a[36:39], v[152:155], a[232:235], a[36:39]\n"
"v_add_f32 v183, v183, v131\n"
"v_cvt_pk_bf16_f32 v149, v122, v123\n"
"v_mfma_f32_16x16x32_bf16 a[40:43], v[152:155], a[240:243], a[40:43]\n"
"ds_read_b64_tr_b16 v[232:233], v195 offset:40960\n"
"v_exp_f32 v132, v132\n"
"v_mfma_f32_16x16x32_bf16 a[40:43], v[156:159], a[244:247], a[40:43]\n"
"v_add_f32 v183, v183, v132\n"
"v_cvt_pk_bf16_f32 v150, v124, v125\n"
"v_mfma_f32_16x16x32_bf16 a[44:47], v[156:159], a[252:255], a[44:47]\n"
"v_exp_f32 v133, v133\n"
"v_mfma_f32_16x16x32_bf16 a[44:47], v[152:155], a[248:251], a[44:47]\n"
"v_add_f32 v183, v183, v133\n"
"v_cvt_pk_bf16_f32 v151, v126, v127\n"
"v_mfma_f32_16x16x32_bf16 a[48:51], v[152:155], v[80:83], a[48:51]\n"
"ds_read_b64_tr_b16 v[234:235], v195 offset:45056\n"
"v_exp_f32 v134, v134\n"
"v_mfma_f32_16x16x32_bf16 a[48:51], v[156:159], v[84:87], a[48:51]\n"
"ds_read_b64_tr_b16 v[228:229], v195 offset:32768\n"
"v_add_f32 v183, v183, v134\n"
"v_mfma_f32_16x16x32_bf16 a[52:55], v[156:159], v[92:95], a[52:55]\n"
"v_exp_f32 v135, v135\n"
"v_mfma_f32_16x16x32_bf16 a[52:55], v[152:155], v[88:91], a[52:55]\n"
"ds_read_b64_tr_b16 v[230:231], v195 offset:36864\n"
"v_add_f32 v183, v183, v135\n"
"v_mfma_f32_16x16x32_bf16 a[56:59], v[152:155], v[96:99], a[56:59]\n"
"v_exp_f32 v136, v136\n"
"v_mfma_f32_16x16x32_bf16 a[56:59], v[156:159], v[100:103], a[56:59]\n"
"ds_read_b64_tr_b16 v[236:237], v196 offset:32768\n"
"v_add_f32 v183, v183, v136\n"
"v_mfma_f32_16x16x32_bf16 a[60:63], v[156:159], v[108:111], a[60:63]\n"
"v_exp_f32 v137, v137\n"
"v_mfma_f32_16x16x32_bf16 a[60:63], v[152:155], v[104:107], a[60:63]\n"
"ds_read_b64_tr_b16 v[238:239], v196 offset:36864\n"
"v_add_f32 v183, v183, v137\n"
"v_mfma_f32_16x16x32_bf16 v[112:115], a[160:163], v[64:67], v[176:179]\n"
"v_exp_f32 v138, v138\n"
"v_mfma_f32_16x16x32_bf16 v[112:115], a[164:167], v[68:71], v[112:115]\n"
"ds_read_b128 a[160:163], v188 offset:32768\n"
"v_add_f32 v183, v183, v138\n"
"v_mfma_f32_16x16x32_bf16 v[112:115], a[168:171], v[72:75], v[112:115]\n"
"v_exp_f32 v139, v139\n"
"v_mfma_f32_16x16x32_bf16 v[112:115], a[172:175], v[76:79], v[112:115]\n"
"ds_read_b128 a[164:167], v189 offset:32768\n"
"v_add_f32 v183, v183, v139\n"
"v_mfma_f32_16x16x32_bf16 v[116:119], a[188:191], v[76:79], v[176:179]\n"
"v_exp_f32 v140, v140\n"
"v_mfma_f32_16x16x32_bf16 v[116:119], a[184:187], v[72:75], v[116:119]\n"
"ds_read_b128 a[168:171], v188 offset:33792\n"
"v_add_f32 v183, v183, v140\n"
"v_mfma_f32_16x16x32_bf16 v[116:119], a[180:183], v[68:71], v[116:119]\n"
"v_exp_f32 v141, v141\n"
"v_mfma_f32_16x16x32_bf16 v[116:119], a[176:179], v[64:67], v[116:119]\n"
"ds_read_b128 a[172:175], v189 offset:33792\n"
"v_add_f32 v183, v183, v141\n"
"v_mfma_f32_16x16x32_bf16 v[120:123], a[192:195], v[64:67], v[176:179]\n"
"v_exp_f32 v142, v142\n"
"v_mfma_f32_16x16x32_bf16 v[120:123], a[196:199], v[68:71], v[120:123]\n"
"ds_read_b128 a[188:191], v189 offset:37888\n"
"v_add_f32 v183, v183, v142\n"
"v_mfma_f32_16x16x32_bf16 v[120:123], a[200:203], v[72:75], v[120:123]\n"
"v_exp_f32 v143, v143\n"
"v_mfma_f32_16x16x32_bf16 v[120:123], a[204:207], v[76:79], v[120:123]\n"
"ds_read_b128 a[184:187], v188 offset:37888\n"
"v_add_f32 v183, v183, v143\n"
"v_mfma_f32_16x16x32_bf16 v[124:127], a[220:223], v[76:79], v[176:179]\n"
"ds_read_b128 a[180:183], v189 offset:36864\n"
"v_exp_f32 v112, v112\n"
"v_mfma_f32_16x16x32_bf16 v[124:127], a[216:219], v[72:75], v[124:127]\n"
"v_add_f32 v184, v184, v112\n"
"v_cvt_pk_bf16_f32 v152, v128, v129\n"
"v_mfma_f32_16x16x32_bf16 v[124:127], a[212:215], v[68:71], v[124:127]\n"
"v_exp_f32 v113, v113\n"
"v_mfma_f32_16x16x32_bf16 v[124:127], a[208:211], v[64:67], v[124:127]\n"
"v_add_f32 v184, v184, v113\n"
"v_cvt_pk_bf16_f32 v153, v130, v131\n"
"v_mfma_f32_16x16x32_bf16 a[64:67], v[144:147], a[224:227], a[64:67]\n"
"v_exp_f32 v114, v114\n"
"v_mfma_f32_16x16x32_bf16 a[64:67], v[148:151], a[228:231], a[64:67]\n"
"ds_read_b128 a[176:179], v188 offset:36864\n"
"v_add_f32 v184, v184, v114\n"
"v_mfma_f32_16x16x32_bf16 a[68:71], v[148:151], a[236:239], a[68:71]\n"
"v_exp_f32 v115, v115\n"
"v_mfma_f32_16x16x32_bf16 a[68:71], v[144:147], a[232:235], a[68:71]\n"
"ds_read_b128 a[192:195], v188 offset:40960\n"
"v_add_f32 v184, v184, v115\n"
"v_mfma_f32_16x16x32_bf16 a[72:75], v[144:147], a[240:243], a[72:75]\n"
"v_exp_f32 v116, v116\n"
"v_mfma_f32_16x16x32_bf16 a[72:75], v[148:151], a[244:247], a[72:75]\n"
"ds_read_b128 a[196:199], v189 offset:40960\n"
"v_add_f32 v184, v184, v116\n"
"v_mfma_f32_16x16x32_bf16 a[76:79], v[148:151], a[252:255], a[76:79]\n"
"v_exp_f32 v117, v117\n"
"v_mfma_f32_16x16x32_bf16 a[76:79], v[144:147], a[248:251], a[76:79]\n"
"ds_read_b128 a[200:203], v188 offset:41984\n"
"v_add_f32 v184, v184, v117\n"
"v_mfma_f32_16x16x32_bf16 a[80:83], v[144:147], v[80:83], a[80:83]\n"
"v_exp_f32 v118, v118\n"
"v_cvt_pk_bf16_f32 v154, v132, v133\n"
"v_mfma_f32_16x16x32_bf16 a[80:83], v[148:151], v[84:87], a[80:83]\n"
"v_add_f32 v184, v184, v118\n"
"v_cvt_pk_bf16_f32 v155, v134, v135\n"
"v_mfma_f32_16x16x32_bf16 a[84:87], v[148:151], v[92:95], a[84:87]\n"
"v_exp_f32 v119, v119\n"
"v_mfma_f32_16x16x32_bf16 a[84:87], v[144:147], v[88:91], a[84:87]\n"
"ds_read_b128 a[204:207], v189 offset:41984\n"
"v_add_f32 v184, v184, v119\n"
"v_mfma_f32_16x16x32_bf16 a[88:91], v[144:147], v[96:99], a[88:91]\n"
"v_exp_f32 v120, v120\n"
"v_mfma_f32_16x16x32_bf16 a[88:91], v[148:151], v[100:103], a[88:91]\n"
"ds_read_b128 a[220:223], v189 offset:46080\n"
"v_add_f32 v184, v184, v120\n"
"v_mfma_f32_16x16x32_bf16 a[92:95], v[148:151], v[108:111], a[92:95]\n"
"v_exp_f32 v121, v121\n"
"v_mfma_f32_16x16x32_bf16 a[92:95], v[144:147], v[104:107], a[92:95]\n"
"s_waitcnt lgkmcnt(2)\n"
"ds_read_b128 a[216:219], v188 offset:46080\n"
"v_add_f32 v184, v184, v121\n"
"v_mfma_f32_16x16x32_bf16 v[128:131], a[160:163], v[0:3], v[160:163]\n"
"v_exp_f32 v122, v122\n"
"v_mfma_f32_16x16x32_bf16 v[128:131], a[164:167], v[4:7], v[128:131]\n"
"ds_read_b128 a[212:215], v189 offset:45056\n"
"v_add_f32 v184, v184, v122\n"
"v_mfma_f32_16x16x32_bf16 v[128:131], a[168:171], v[8:11], v[128:131]\n"
"v_exp_f32 v123, v123\n"
"v_mfma_f32_16x16x32_bf16 v[128:131], a[172:175], v[12:15], v[128:131]\n"
"v_add_f32 v184, v184, v123\n"
"v_cvt_pk_bf16_f32 v156, v136, v137\n"
"v_mfma_f32_16x16x32_bf16 v[132:135], a[188:191], v[12:15], v[160:163]\n"
"v_exp_f32 v124, v124\n"
"v_mfma_f32_16x16x32_bf16 v[132:135], a[184:187], v[8:11], v[132:135]\n"
"v_add_f32 v184, v184, v124\n"
"v_cvt_pk_bf16_f32 v157, v138, v139\n"
"v_mfma_f32_16x16x32_bf16 v[132:135], a[180:183], v[4:7], v[132:135]\n"
"ds_read_b128 a[208:211], v188 offset:45056\n"
"v_exp_f32 v125, v125\n"
"v_mfma_f32_16x16x32_bf16 v[132:135], a[176:179], v[0:3], v[132:135]\n"
"v_add_f32 v184, v184, v125\n"
"v_cvt_pk_bf16_f32 v158, v140, v141\n"
"v_mfma_f32_16x16x32_bf16 v[136:139], a[192:195], v[0:3], v[160:163]\n"
"v_exp_f32 v126, v126\n"
"v_mfma_f32_16x16x32_bf16 v[136:139], a[196:199], v[4:7], v[136:139]\n"
"v_add_f32 v184, v184, v126\n"
"v_cvt_pk_bf16_f32 v159, v142, v143\n"
"v_mfma_f32_16x16x32_bf16 v[136:139], a[200:203], v[8:11], v[136:139]\n"
"s_waitcnt lgkmcnt(1)\n"
"v_exp_f32 v127, v127\n"
"v_mfma_f32_16x16x32_bf16 v[136:139], a[204:207], v[12:15], v[136:139]\n"
"v_add_f32 v184, v184, v127\n"
"v_cvt_pk_bf16_f32 v144, v112, v113\n"
"v_mfma_f32_16x16x32_bf16 v[140:143], a[220:223], v[12:15], v[160:163]\n"
"v_exp_f32 v128, v128\n"
"v_mfma_f32_16x16x32_bf16 v[140:143], a[216:219], v[8:11], v[140:143]\n"
"v_cvt_pk_bf16_f32 v145, v114, v115\n"
"v_add_f32 v180, v180, v128\n"
"v_mfma_f32_16x16x32_bf16 v[140:143], a[212:215], v[4:7], v[140:143]\n"
"s_waitcnt lgkmcnt(0)\n"
"ds_read_b64_tr_b16 v[240:241], v196 offset:40960\n"
"v_exp_f32 v129, v129\n"
"v_mfma_f32_16x16x32_bf16 v[140:143], a[208:211], v[0:3], v[140:143]\n"
"v_cvt_pk_bf16_f32 v146, v116, v117\n"
"v_add_f32 v180, v180, v129\n"
"v_mfma_f32_16x16x32_bf16 a[96:99], v[152:155], a[224:227], a[96:99]\n"
"v_exp_f32 v130, v130\n"
"v_mfma_f32_16x16x32_bf16 a[96:99], v[156:159], a[228:231], a[96:99]\n"
"v_cvt_pk_bf16_f32 v147, v118, v119\n"
"v_add_f32 v180, v180, v130\n"
"v_mfma_f32_16x16x32_bf16 a[100:103], v[156:159], a[236:239], a[100:103]\n"
"v_exp_f32 v131, v131\n"
"v_mfma_f32_16x16x32_bf16 a[100:103], v[152:155], a[232:235], a[100:103]\n"
"v_cvt_pk_bf16_f32 v148, v120, v121\n"
"v_add_f32 v180, v180, v131\n"
"v_mfma_f32_16x16x32_bf16 a[104:107], v[152:155], a[240:243], a[104:107]\n"
"ds_read_b64_tr_b16 v[242:243], v196 offset:45056\n"
"v_exp_f32 v132, v132\n"
"v_mfma_f32_16x16x32_bf16 a[104:107], v[156:159], a[244:247], a[104:107]\n"
"v_cvt_pk_bf16_f32 v149, v122, v123\n"
"v_add_f32 v180, v180, v132\n"
"v_mfma_f32_16x16x32_bf16 a[108:111], v[156:159], a[252:255], a[108:111]\n"
"v_exp_f32 v133, v133\n"
"v_mfma_f32_16x16x32_bf16 a[108:111], v[152:155], a[248:251], a[108:111]\n"
"v_cvt_pk_bf16_f32 v150, v124, v125\n"
"v_add_f32 v180, v180, v133\n"
"v_mfma_f32_16x16x32_bf16 a[112:115], v[152:155], v[80:83], a[112:115]\n"
"v_exp_f32 v134, v134\n"
"v_mfma_f32_16x16x32_bf16 a[112:115], v[156:159], v[84:87], a[112:115]\n"
"v_cvt_pk_bf16_f32 v151, v126, v127\n"
"v_add_f32 v180, v180, v134\n"
"v_mfma_f32_16x16x32_bf16 a[116:119], v[156:159], v[92:95], a[116:119]\n"
"ds_read_b64_tr_b16 v[248:249], v197 offset:40960\n"
"v_exp_f32 v135, v135\n"
"v_mfma_f32_16x16x32_bf16 a[116:119], v[152:155], v[88:91], a[116:119]\n"
"ds_read_b64_tr_b16 v[250:251], v197 offset:45056\n"
"v_add_f32 v180, v180, v135\n"
"v_mfma_f32_16x16x32_bf16 a[120:123], v[152:155], v[96:99], a[120:123]\n"
"v_exp_f32 v136, v136\n"
"v_mfma_f32_16x16x32_bf16 a[120:123], v[156:159], v[100:103], a[120:123]\n"
"ds_read_b64_tr_b16 v[244:245], v197 offset:32768\n"
"v_add_f32 v180, v180, v136\n"
"v_mfma_f32_16x16x32_bf16 a[124:127], v[156:159], v[108:111], a[124:127]\n"
"v_exp_f32 v137, v137\n"
"v_mfma_f32_16x16x32_bf16 a[124:127], v[152:155], v[104:107], a[124:127]\n"
"ds_read_b64_tr_b16 v[246:247], v197 offset:36864\n"
"v_add_f32 v180, v180, v137\n"
"v_mfma_f32_16x16x32_bf16 v[112:115], a[160:163], v[16:19], v[164:167]\n"
"s_add_u32 s76, s26, 0x100\n"
"v_exp_f32 v138, v138\n"
"v_mfma_f32_16x16x32_bf16 v[112:115], a[164:167], v[20:23], v[112:115]\n"
"s_add_u32 m0, s81, 0x10000\n"
"v_add_f32 v180, v180, v138\n"
"v_cvt_pk_bf16_f32 v152, v128, v129\n"
"v_mfma_f32_16x16x32_bf16 v[112:115], a[168:171], v[24:27], v[112:115]\n"
"buffer_load_dwordx4 v199, s[60:63], s76 offen offset:0 lds\n"
"v_exp_f32 v139, v139\n"
"v_mfma_f32_16x16x32_bf16 v[112:115], a[172:175], v[28:31], v[112:115]\n"
"s_add_u32 s76, s26, 0x2500\n"
"v_add_f32 v180, v180, v139\n"
"v_cvt_pk_bf16_f32 v153, v130, v131\n"
"v_mfma_f32_16x16x32_bf16 v[116:119], a[188:191], v[28:31], v[164:167]\n"
"s_add_u32 m0, s81, 0x10400\n"
"v_exp_f32 v140, v140\n"
"v_mfma_f32_16x16x32_bf16 v[116:119], a[184:187], v[24:27], v[116:119]\n"
"buffer_load_dwordx4 v199, s[60:63], s76 offen offset:0 lds\n"
"v_add_f32 v180, v180, v140\n"
"v_mfma_f32_16x16x32_bf16 v[116:119], a[180:183], v[20:23], v[116:119]\n"
"s_add_u32 s76, s26, 0x4900\n"
"v_exp_f32 v141, v141\n"
"v_mfma_f32_16x16x32_bf16 v[116:119], a[176:179], v[16:19], v[116:119]\n"
"s_add_u32 m0, s81, 0x10800\n"
"v_add_f32 v180, v180, v141\n"
"v_cvt_pk_bf16_f32 v154, v132, v133\n"
"v_mfma_f32_16x16x32_bf16 v[120:123], a[192:195], v[16:19], v[164:167]\n"
"buffer_load_dwordx4 v199, s[60:63], s76 offen offset:0 lds\n"
"v_exp_f32 v142, v142\n"
"v_mfma_f32_16x16x32_bf16 v[120:123], a[196:199], v[20:23], v[120:123]\n"
"s_add_u32 s76, s26, 0x6d00\n"
"v_add_f32 v180, v180, v142\n"
"v_cvt_pk_bf16_f32 v155, v134, v135\n"
"v_mfma_f32_16x16x32_bf16 v[120:123], a[200:203], v[24:27], v[120:123]\n"
"s_add_u32 m0, s81, 0x10c00\n"
"v_exp_f32 v143, v143\n"
"v_mfma_f32_16x16x32_bf16 v[120:123], a[204:207], v[28:31], v[120:123]\n"
"buffer_load_dwordx4 v199, s[60:63], s76 offen offset:0 lds\n"
"v_add_f32 v180, v180, v143\n"
"v_mfma_f32_16x16x32_bf16 v[124:127], a[220:223], v[28:31], v[164:167]\n"
"s_add_u32 s76, s26, 0x9100\n"
"v_exp_f32 v112, v112\n"
"v_mfma_f32_16x16x32_bf16 v[124:127], a[216:219], v[24:27], v[124:127]\n"
"s_add_u32 m0, s81, 0x11000\n"
"v_add_f32 v181, v181, v112\n"
"v_cvt_pk_bf16_f32 v156, v136, v137\n"
"v_mfma_f32_16x16x32_bf16 v[124:127], a[212:215], v[20:23], v[124:127]\n"
"buffer_load_dwordx4 v199, s[60:63], s76 offen offset:0 lds\n"
"v_exp_f32 v113, v113\n"
"v_mfma_f32_16x16x32_bf16 v[124:127], a[208:211], v[16:19], v[124:127]\n"
"s_waitcnt lgkmcnt(0)\n"
"v_add_f32 v181, v181, v113\n"
"v_cvt_pk_bf16_f32 v157, v138, v139\n"
"v_mfma_f32_16x16x32_bf16 a[128:131], v[144:147], a[224:227], a[128:131]\n"
"s_add_u32 s76, s26, 0xb500\n"
"v_exp_f32 v114, v114\n"
"v_mfma_f32_16x16x32_bf16 a[128:131], v[148:151], a[228:231], a[128:131]\n"
"s_add_u32 m0, s81, 0x11400\n"
"v_add_f32 v181, v181, v114\n"
"v_mfma_f32_16x16x32_bf16 a[132:135], v[148:151], a[236:239], a[132:135]\n"
"buffer_load_dwordx4 v199, s[60:63], s76 offen offset:0 lds\n"
"v_exp_f32 v115, v115\n"
"v_mfma_f32_16x16x32_bf16 a[132:135], v[144:147], a[232:235], a[132:135]\n"
"s_add_u32 s76, s26, 0xd900\n"
"v_add_f32 v181, v181, v115\n"
"v_cvt_pk_bf16_f32 v158, v140, v141\n"
"v_mfma_f32_16x16x32_bf16 a[136:139], v[144:147], a[240:243], a[136:139]\n"
"s_add_u32 m0, s81, 0x11800\n"
"v_exp_f32 v116, v116\n"
"v_mfma_f32_16x16x32_bf16 a[136:139], v[148:151], a[244:247], a[136:139]\n"
"buffer_load_dwordx4 v199, s[60:63], s76 offen offset:0 lds\n"
"v_add_f32 v181, v181, v116\n"
"v_cvt_pk_bf16_f32 v159, v142, v143\n"
"v_mfma_f32_16x16x32_bf16 a[140:143], v[148:151], a[252:255], a[140:143]\n"
"s_add_u32 s76, s26, 0xfd00\n"
"v_exp_f32 v117, v117\n"
"v_mfma_f32_16x16x32_bf16 a[140:143], v[144:147], a[248:251], a[140:143]\n"
"s_add_u32 m0, s81, 0x11c00\n"
"v_add_f32 v181, v181, v117\n"
"v_mfma_f32_16x16x32_bf16 a[144:147], v[144:147], v[80:83], a[144:147]\n"
"buffer_load_dwordx4 v199, s[60:63], s76 offen offset:0 lds\n"
"v_exp_f32 v118, v118\n"
"v_mfma_f32_16x16x32_bf16 a[144:147], v[148:151], v[84:87], a[144:147]\n"
"ds_read_b64_tr_b16 v[80:81], v194 offset:33792\n"
"v_add_f32 v181, v181, v118\n"
"v_mfma_f32_16x16x32_bf16 a[148:151], v[148:151], v[92:95], a[148:151]\n"
"buffer_load_dword v253, v252, s[68:71], 0 offen offset:128 sc1\n"
"v_exp_f32 v119, v119\n"
"v_mfma_f32_16x16x32_bf16 a[148:151], v[144:147], v[88:91], a[148:151]\n"
"ds_read_b64_tr_b16 v[82:83], v194 offset:37888\n"
"v_add_f32 v181, v181, v119\n"
"v_mfma_f32_16x16x32_bf16 a[152:155], v[144:147], v[96:99], a[152:155]\n"
"v_exp_f32 v120, v120\n"
"v_mfma_f32_16x16x32_bf16 a[152:155], v[148:151], v[100:103], a[152:155]\n"
"ds_read_b64_tr_b16 v[84:85], v194 offset:41984\n"
"v_add_f32 v181, v181, v120\n"
"v_mfma_f32_16x16x32_bf16 a[156:159], v[148:151], v[108:111], a[156:159]\n"
"v_exp_f32 v121, v121\n"
"v_mfma_f32_16x16x32_bf16 a[156:159], v[144:147], v[104:107], a[156:159]\n"
"ds_read_b64_tr_b16 v[86:87], v194 offset:46080\n"
"v_add_f32 v181, v181, v121\n"
"v_mfma_f32_16x16x32_bf16 v[128:131], a[160:163], v[32:35], v[168:171]\n"
"v_exp_f32 v122, v122\n"
"v_mfma_f32_16x16x32_bf16 v[128:131], a[164:167], v[36:39], v[128:131]\n"
"ds_read_b64_tr_b16 v[92:93], v195 offset:41984\n"
"v_add_f32 v181, v181, v122\n"
"v_mfma_f32_16x16x32_bf16 v[128:131], a[168:171], v[40:43], v[128:131]\n"
"v_exp_f32 v123, v123\n"
"v_mfma_f32_16x16x32_bf16 v[128:131], a[172:175], v[44:47], v[128:131]\n"
"ds_read_b64_tr_b16 v[94:95], v195 offset:46080\n"
"v_add_f32 v181, v181, v123\n"
"v_mfma_f32_16x16x32_bf16 v[132:135], a[188:191], v[44:47], v[168:171]\n"
"v_exp_f32 v124, v124\n"
"v_mfma_f32_16x16x32_bf16 v[132:135], a[184:187], v[40:43], v[132:135]\n"
"ds_read_b64_tr_b16 v[88:89], v195 offset:33792\n"
"v_add_f32 v181, v181, v124\n"
"v_mfma_f32_16x16x32_bf16 v[132:135], a[180:183], v[36:39], v[132:135]\n"
"v_exp_f32 v125, v125\n"
"v_mfma_f32_16x16x32_bf16 v[132:135], a[176:179], v[32:35], v[132:135]\n"
"ds_read_b64_tr_b16 v[90:91], v195 offset:37888\n"
"v_add_f32 v181, v181, v125\n"
"v_mfma_f32_16x16x32_bf16 v[136:139], a[192:195], v[32:35], v[168:171]\n"
"v_exp_f32 v126, v126\n"
"v_mfma_f32_16x16x32_bf16 v[136:139], a[196:199], v[36:39], v[136:139]\n"
"ds_read_b64_tr_b16 v[96:97], v196 offset:33792\n"
"v_add_f32 v181, v181, v126\n"
"v_mfma_f32_16x16x32_bf16 v[136:139], a[200:203], v[40:43], v[136:139]\n"
"v_exp_f32 v127, v127\n"
"v_mfma_f32_16x16x32_bf16 v[136:139], a[204:207], v[44:47], v[136:139]\n"
"ds_read_b64_tr_b16 v[98:99], v196 offset:37888\n"
"v_add_f32 v181, v181, v127\n"
"v_mfma_f32_16x16x32_bf16 v[140:143], a[220:223], v[44:47], v[168:171]\n"
"v_exp_f32 v128, v128\n"
"v_mfma_f32_16x16x32_bf16 v[140:143], a[216:219], v[40:43], v[140:143]\n"
"ds_read_b64_tr_b16 v[100:101], v196 offset:41984\n"
"v_add_f32 v182, v182, v128\n"
"v_mfma_f32_16x16x32_bf16 v[140:143], a[212:215], v[36:39], v[140:143]\n"
"v_exp_f32 v129, v129\n"
"v_mfma_f32_16x16x32_bf16 v[140:143], a[208:211], v[32:35], v[140:143]\n"
"ds_read_b64_tr_b16 v[102:103], v196 offset:46080\n"
"v_add_f32 v182, v182, v129\n"
"v_mfma_f32_16x16x32_bf16 a[0:3], v[152:155], v[220:223], a[0:3]\n"
"v_exp_f32 v130, v130\n"
"v_mfma_f32_16x16x32_bf16 a[0:3], v[156:159], v[224:227], a[0:3]\n"
"ds_read_b64_tr_b16 v[108:109], v197 offset:41984\n"
"v_add_f32 v182, v182, v130\n"
"v_mfma_f32_16x16x32_bf16 a[4:7], v[156:159], v[232:235], a[4:7]\n"
"v_exp_f32 v131, v131\n"
"v_mfma_f32_16x16x32_bf16 a[4:7], v[152:155], v[228:231], a[4:7]\n"
"ds_read_b64_tr_b16 v[110:111], v197 offset:46080\n"
"v_add_f32 v182, v182, v131\n"
"v_mfma_f32_16x16x32_bf16 a[8:11], v[152:155], v[236:239], a[8:11]\n"
"v_exp_f32 v132, v132\n"
"v_mfma_f32_16x16x32_bf16 a[8:11], v[156:159], v[240:243], a[8:11]\n"
"ds_read_b64_tr_b16 v[104:105], v197 offset:33792\n"
"v_add_f32 v182, v182, v132\n"
"v_mfma_f32_16x16x32_bf16 a[12:15], v[156:159], v[248:251], a[12:15]\n"
"v_exp_f32 v133, v133\n"
"v_mfma_f32_16x16x32_bf16 a[12:15], v[152:155], v[244:247], a[12:15]\n"
"s_waitcnt lgkmcnt(3)\n"
"ds_read_b64_tr_b16 v[106:107], v197 offset:37888\n"
"v_add_f32 v182, v182, v133\n"
"v_mfma_f32_16x16x32_bf16 a[16:19], v[152:155], v[80:83], a[16:19]\n"
"v_exp_f32 v134, v134\n"
"v_mfma_f32_16x16x32_bf16 a[16:19], v[156:159], v[84:87], a[16:19]\n"
"v_add_f32 v182, v182, v134\n"
"v_cvt_pk_bf16_f32 v144, v112, v113\n"
"v_mfma_f32_16x16x32_bf16 a[20:23], v[156:159], v[92:95], a[20:23]\n"
"v_exp_f32 v135, v135\n"
"v_mfma_f32_16x16x32_bf16 a[20:23], v[152:155], v[88:91], a[20:23]\n"
"v_add_f32 v182, v182, v135\n"
"v_cvt_pk_bf16_f32 v145, v114, v115\n"
"v_mfma_f32_16x16x32_bf16 a[24:27], v[152:155], v[96:99], a[24:27]\n"
"v_exp_f32 v136, v136\n"
"v_mfma_f32_16x16x32_bf16 a[24:27], v[156:159], v[100:103], a[24:27]\n"
"s_waitcnt lgkmcnt(1)\n"
"v_add_f32 v182, v182, v136\n"
"v_cvt_pk_bf16_f32 v146, v116, v117\n"
"v_mfma_f32_16x16x32_bf16 a[28:31], v[156:159], v[108:111], a[28:31]\n"
"s_waitcnt lgkmcnt(0)\n"
"v_exp_f32 v137, v137\n"
"v_mfma_f32_16x16x32_bf16 a[28:31], v[152:155], v[104:107], a[28:31]\n"
"v_add_f32 v182, v182, v137\n"
"v_cvt_pk_bf16_f32 v147, v118, v119\n"
"v_mfma_f32_16x16x32_bf16 v[112:115], a[160:163], v[48:51], v[172:175]\n"
"s_waitcnt vmcnt(10)\n"
"v_exp_f32 v138, v138\n"
"v_mfma_f32_16x16x32_bf16 v[112:115], a[164:167], v[52:55], v[112:115]\n"
"s_barrier\n"
"v_add_f32 v182, v182, v138\n"
"v_cvt_pk_bf16_f32 v148, v120, v121\n"
"v_mfma_f32_16x16x32_bf16 v[112:115], a[168:171], v[56:59], v[112:115]\n"
"v_exp_f32 v139, v139\n"
"v_mfma_f32_16x16x32_bf16 v[112:115], a[172:175], v[60:63], v[112:115]\n"
"v_add_f32 v182, v182, v139\n"
"v_cvt_pk_bf16_f32 v149, v122, v123\n"
"v_mfma_f32_16x16x32_bf16 v[116:119], a[188:191], v[60:63], v[172:175]\n"
"v_exp_f32 v140, v140\n"
"v_mfma_f32_16x16x32_bf16 v[116:119], a[184:187], v[56:59], v[116:119]\n"
"v_add_f32 v182, v182, v140\n"
"v_cvt_pk_bf16_f32 v150, v124, v125\n"
"v_mfma_f32_16x16x32_bf16 v[116:119], a[180:183], v[52:55], v[116:119]\n"
"v_exp_f32 v141, v141\n"
"v_mfma_f32_16x16x32_bf16 v[116:119], a[176:179], v[48:51], v[116:119]\n"
"v_add_f32 v182, v182, v141\n"
"v_cvt_pk_bf16_f32 v151, v126, v127\n"
"s_waitcnt vmcnt(0)\n"
"v_readlane_b32 s80, v253, s25\n"
"s_cmp_lg_u32 s88, 0\n"
"s_cbranch_scc1 L_snd_%=\n"
"v_mfma_f32_16x16x32_bf16 v[120:123], a[192:195], v[48:51], v[172:175]\n"
"v_cvt_pk_bf16_f32 v152, v128, v129\n"
"v_cvt_pk_bf16_f32 v153, v130, v131\n"
"v_mfma_f32_16x16x32_bf16 v[120:123], a[196:199], v[52:55], v[120:123]\n"
"v_cvt_pk_bf16_f32 v154, v132, v133\n"
"v_cvt_pk_bf16_f32 v155, v134, v135\n"
"v_mfma_f32_16x16x32_bf16 v[120:123], a[200:203], v[56:59], v[120:123]\n"
"v_cvt_pk_bf16_f32 v156, v136, v137\n"
"v_cvt_pk_bf16_f32 v157, v138, v139\n"
"v_mfma_f32_16x16x32_bf16 v[120:123], a[204:207], v[60:63], v[120:123]\n"
"v_exp_f32 v142, v142\n"
"v_mfma_f32_16x16x32_bf16 v[124:127], a[220:223], v[60:63], v[172:175]\n"
"v_exp_f32 v143, v143\n"
"v_mfma_f32_16x16x32_bf16 v[124:127], a[216:219], v[56:59], v[124:127]\n"
"v_add_f32 v182, v182, v142\n"
"v_add_f32 v182, v182, v143\n"
"v_mfma_f32_16x16x32_bf16 v[124:127], a[212:215], v[52:55], v[124:127]\n"
"v_cvt_pk_bf16_f32 v158, v140, v141\n"
"v_cvt_pk_bf16_f32 v159, v142, v143\n"
"v_mfma_f32_16x16x32_bf16 v[124:127], a[208:211], v[48:51], v[124:127]\n"
"v_exp_f32 v112, v112\n"
"v_mfma_f32_16x16x32_bf16 a[32:35], v[144:147], v[220:223], a[32:35]\n"
"v_exp_f32 v113, v113\n"
"v_mfma_f32_16x16x32_bf16 a[32:35], v[148:151], v[224:227], a[32:35]\n"
"v_exp_f32 v114, v114\n"
"v_mfma_f32_16x16x32_bf16 a[36:39], v[148:151], v[232:235], a[36:39]\n"
"v_exp_f32 v115, v115\n"
"v_mfma_f32_16x16x32_bf16 a[36:39], v[144:147], v[228:231], a[36:39]\n"
"v_exp_f32 v116, v116\n"
"v_mfma_f32_16x16x32_bf16 a[40:43], v[144:147], v[236:239], a[40:43]\n"
"v_exp_f32 v117, v117\n"
"v_mfma_f32_16x16x32_bf16 a[40:43], v[148:151], v[240:243], a[40:43]\n"
"v_exp_f32 v118, v118\n"
"v_mfma_f32_16x16x32_bf16 a[44:47], v[148:151], v[248:251], a[44:47]\n"
"v_exp_f32 v119, v119\n"
"v_mfma_f32_16x16x32_bf16 a[44:47], v[144:147], v[244:247], a[44:47]\n"
"v_mov_b32 v20, v180\n"
"v_and_b32 v217, 15, v213\n"
"v_mfma_f32_16x16x32_bf16 a[48:51], v[144:147], v[80:83], a[48:51]\n"
"v_exp_f32 v120, v120\n"
"v_mfma_f32_16x16x32_bf16 a[48:51], v[148:151], v[84:87], a[48:51]\n"
"v_exp_f32 v121, v121\n"
"v_mfma_f32_16x16x32_bf16 a[52:55], v[148:151], v[92:95], a[52:55]\n"
"v_exp_f32 v122, v122\n"
"v_mfma_f32_16x16x32_bf16 a[52:55], v[144:147], v[88:91], a[52:55]\n"
"v_exp_f32 v123, v123\n"
"v_mfma_f32_16x16x32_bf16 a[56:59], v[144:147], v[96:99], a[56:59]\n"
"v_exp_f32 v124, v124\n"
"v_mfma_f32_16x16x32_bf16 a[56:59], v[148:151], v[100:103], a[56:59]\n"
"v_exp_f32 v125, v125\n"
"v_mfma_f32_16x16x32_bf16 a[60:63], v[148:151], v[108:111], a[60:63]\n"
"v_exp_f32 v126, v126\n"
"v_mfma_f32_16x16x32_bf16 a[60:63], v[144:147], v[104:107], a[60:63]\n"
"v_cvt_pk_bf16_f32 v144, v112, v113\n"
"v_cvt_pk_bf16_f32 v145, v114, v115\n"
"v_mfma_f32_16x16x32_bf16 v[128:131], a[160:163], v[64:67], v[176:179]\n"
"v_cvt_pk_bf16_f32 v146, v116, v117\n"
"v_cvt_pk_bf16_f32 v147, v118, v119\n"
"v_mfma_f32_16x16x32_bf16 v[128:131], a[164:167], v[68:71], v[128:131]\n"
"v_exp_f32 v127, v127\n"
"v_mfma_f32_16x16x32_bf16 v[128:131], a[168:171], v[72:75], v[128:131]\n"
"v_permlane16_swap_b32 v180, v20\n"
"v_mfma_f32_16x16x32_bf16 v[128:131], a[172:175], v[76:79], v[128:131]\n"
"v_cvt_pk_bf16_f32 v148, v120, v121\n"
"v_cvt_pk_bf16_f32 v149, v122, v123\n"
"v_mfma_f32_16x16x32_bf16 v[132:135], a[188:191], v[76:79], v[176:179]\n"
"v_cvt_pk_bf16_f32 v150, v124, v125\n"
"v_cvt_pk_bf16_f32 v151, v126, v127\n"
"v_mfma_f32_16x16x32_bf16 v[132:135], a[184:187], v[72:75], v[132:135]\n"
"v_add_f32 v180, v180, v20\n"
"v_mov_b32 v20, v180\n"
"v_mfma_f32_16x16x32_bf16 v[132:135], a[180:183], v[68:71], v[132:135]\n"
"s_nop 0\n"
"v_permlane32_swap_b32 v180, v20\n"
"v_mfma_f32_16x16x32_bf16 v[132:135], a[176:179], v[64:67], v[132:135]\n"
"v_add_f32 v180, v180, v20\n"
"v_lshrrev_b32 v214, 4, v213\n"
"v_mfma_f32_16x16x32_bf16 v[136:139], a[192:195], v[64:67], v[176:179]\n"
"v_rcp_f32 v20, v180\n"
"v_mfma_f32_16x16x32_bf16 v[136:139], a[196:199], v[68:71], v[136:139]\n"
"v_lshl_add_u32 v208, v217, 2, s89\n"
"v_lshl_add_u32 v209, v214, 4, s89\n"
"v_mfma_f32_16x16x32_bf16 v[136:139], a[200:203], v[72:75], v[136:139]\n"
"ds_write_b32 v208, v20 offset:0\n"
"v_mov_b32 v21, v181\n"
"v_mfma_f32_16x16x32_bf16 v[136:139], a[204:207], v[76:79], v[136:139]\n"
"ds_read_b128 v[0:3], v209 offset:0\n"
"v_bfe_u32 v215, v213, 2, 2\n"
"v_mfma_f32_16x16x32_bf16 v[140:143], a[220:223], v[76:79], v[176:179]\n"
"v_permlane16_swap_b32 v181, v21\n"
"v_mfma_f32_16x16x32_bf16 v[140:143], a[216:219], v[72:75], v[140:143]\n"
"v_add_f32 v181, v181, v21\n"
"v_mov_b32 v21, v181\n"
"v_mfma_f32_16x16x32_bf16 v[140:143], a[212:215], v[68:71], v[140:143]\n"
"s_nop 0\n"
"v_permlane32_swap_b32 v181, v21\n"
"v_mfma_f32_16x16x32_bf16 v[140:143], a[208:211], v[64:67], v[140:143]\n"
"v_add_f32 v181, v181, v21\n"
"v_xor_b32 v218, v214, v215\n"
"v_mfma_f32_16x16x32_bf16 a[64:67], v[152:155], v[220:223], a[64:67]\n"
"v_rcp_f32 v21, v181\n"
"v_mfma_f32_16x16x32_bf16 a[64:67], v[156:159], v[224:227], a[64:67]\n"
"ds_write_b32 v208, v21 offset:64\n"
"v_accvgpr_read_b32 v32, a0\n"
"v_mfma_f32_16x16x32_bf16 a[68:71], v[156:159], v[232:235], a[68:71]\n"
"ds_read_b128 v[4:7], v209 offset:64\n"
"v_accvgpr_read_b32 v33, a1\n"
"v_mfma_f32_16x16x32_bf16 a[68:71], v[152:155], v[228:231], a[68:71]\n"
"v_lshlrev_b32 v218, 3, v218\n"
"v_accvgpr_read_b32 v34, a2\n"
"v_mfma_f32_16x16x32_bf16 a[72:75], v[152:155], v[236:239], a[72:75]\n"
"v_accvgpr_read_b32 v35, a3\n"
"s_waitcnt lgkmcnt(2)\n"
"v_mul_f32 v32, v32, v0\n"
"v_mfma_f32_16x16x32_bf16 a[72:75], v[156:159], v[240:243], a[72:75]\n"
"v_mul_f32 v33, v33, v1\n"
"v_mov_b32 v22, v182\n"
"v_mfma_f32_16x16x32_bf16 a[76:79], v[156:159], v[248:251], a[76:79]\n"
"v_lshl_add_u32 v218, v217, 5, v218\n"
"v_accvgpr_read_b32 v36, a4\n"
"v_mfma_f32_16x16x32_bf16 a[76:79], v[152:155], v[244:247], a[76:79]\n"
"v_accvgpr_read_b32 v37, a5\n"
"v_accvgpr_read_b32 v38, a6\n"
"v_mfma_f32_16x16x32_bf16 a[80:83], v[152:155], v[80:83], a[80:83]\n"
"v_accvgpr_read_b32 v39, a7\n"
"v_mul_f32 v34, v34, v2\n"
"v_mfma_f32_16x16x32_bf16 a[80:83], v[156:159], v[84:87], a[80:83]\n"
"v_mul_f32 v35, v35, v3\n"
"v_cvt_pk_bf16_f32 v32, v32, v33\n"
"v_mfma_f32_16x16x32_bf16 a[84:87], v[156:159], v[92:95], a[84:87]\n"
"v_accvgpr_read_b32 v40, a8\n"
"v_accvgpr_read_b32 v41, a9\n"
"v_mfma_f32_16x16x32_bf16 a[84:87], v[152:155], v[88:91], a[84:87]\n"
"v_add_u32 v207, s90, v218\n"
"v_mul_f32 v36, v36, v0\n"
"v_mfma_f32_16x16x32_bf16 a[88:91], v[152:155], v[96:99], a[88:91]\n"
"v_mul_f32 v37, v37, v1\n"
"v_mul_f32 v38, v38, v2\n"
"v_mfma_f32_16x16x32_bf16 a[88:91], v[156:159], v[100:103], a[88:91]\n"
"v_mul_f32 v39, v39, v3\n"
"v_cvt_pk_bf16_f32 v33, v34, v35\n"
"v_mfma_f32_16x16x32_bf16 a[92:95], v[156:159], v[108:111], a[92:95]\n"
"ds_write_b64 v207, v[32:33] offset:0\n"
"v_accvgpr_read_b32 v42, a10\n"
"v_mfma_f32_16x16x32_bf16 a[92:95], v[152:155], v[104:107], a[92:95]\n"
"v_accvgpr_read_b32 v43, a11\n"
"v_mul_f32 v40, v40, v0\n"
"v_mfma_f32_16x16x32_bf16 a[96:99], v[144:147], v[220:223], a[96:99]\n"
"v_mul_f32 v41, v41, v1\n"
"v_cvt_pk_bf16_f32 v34, v36, v37\n"
"v_mfma_f32_16x16x32_bf16 a[96:99], v[148:151], v[224:227], a[96:99]\n"
"v_cvt_pk_bf16_f32 v35, v38, v39\n"
"v_accvgpr_read_b32 v44, a12\n"
"v_mfma_f32_16x16x32_bf16 a[100:103], v[148:151], v[232:235], a[100:103]\n"
"ds_write_b64 v207, v[34:35] offset:512\n"
"v_accvgpr_read_b32 v45, a13\n"
"v_mfma_f32_16x16x32_bf16 a[100:103], v[144:147], v[228:231], a[100:103]\n"
"v_accvgpr_read_b32 v46, a14\n"
"v_accvgpr_read_b32 v47, a15\n"
"v_mfma_f32_16x16x32_bf16 a[104:107], v[144:147], v[236:239], a[104:107]\n"
"v_mul_f32 v42, v42, v2\n"
"v_mul_f32 v43, v43, v3\n"
"v_mfma_f32_16x16x32_bf16 a[104:107], v[148:151], v[240:243], a[104:107]\n"
"v_cvt_pk_bf16_f32 v40, v40, v41\n"
"v_mul_f32 v44, v44, v0\n"
"v_mfma_f32_16x16x32_bf16 a[108:111], v[148:151], v[248:251], a[108:111]\n"
"v_permlane16_swap_b32 v182, v22\n"
"v_mfma_f32_16x16x32_bf16 a[108:111], v[144:147], v[244:247], a[108:111]\n"
"v_mul_f32 v45, v45, v1\n"
"v_mul_f32 v46, v46, v2\n"
"v_mfma_f32_16x16x32_bf16 a[112:115], v[144:147], v[80:83], a[112:115]\n"
"v_mul_f32 v47, v47, v3\n"
"v_cvt_pk_bf16_f32 v41, v42, v43\n"
"v_mfma_f32_16x16x32_bf16 a[112:115], v[148:151], v[84:87], a[112:115]\n"
"ds_write_b64 v207, v[40:41] offset:1024\n"
"v_accvgpr_read_b32 v32, a16\n"
"v_mfma_f32_16x16x32_bf16 a[116:119], v[148:151], v[92:95], a[116:119]\n"
"v_accvgpr_read_b32 v33, a17\n"
"v_add_f32 v182, v182, v22\n"
"v_mfma_f32_16x16x32_bf16 a[116:119], v[144:147], v[88:91], a[116:119]\n"
"v_cvt_pk_bf16_f32 v42, v44, v45\n"
"v_cvt_pk_bf16_f32 v43, v46, v47\n"
"v_mfma_f32_16x16x32_bf16 a[120:123], v[144:147], v[96:99], a[120:123]\n"
"ds_write_b64 v207, v[42:43] offset:1536\n"
"v_accvgpr_read_b32 v34, a18\n"
"v_mfma_f32_16x16x32_bf16 a[120:123], v[148:151], v[100:103], a[120:123]\n"
"v_accvgpr_read_b32 v35, a19\n"
"v_mul_f32 v32, v32, v0\n"
"v_mfma_f32_16x16x32_bf16 a[124:127], v[148:151], v[108:111], a[124:127]\n"
"v_mul_f32 v33, v33, v1\n"
"v_mov_b32 v22, v182\n"
"v_mfma_f32_16x16x32_bf16 a[124:127], v[144:147], v[104:107], a[124:127]\n"
"v_accvgpr_read_b32 v36, a20\n"
"v_accvgpr_read_b32 v37, a21\n"
"v_accvgpr_read_b32 v38, a22\n"
"v_accvgpr_read_b32 v39, a23\n"
"v_mul_f32 v34, v34, v2\n"
"v_mul_f32 v35, v35, v3\n"
"v_cvt_pk_bf16_f32 v32, v32, v33\n"
"v_accvgpr_read_b32 v40, a24\n"
"v_accvgpr_read_b32 v41, a25\n"
"v_mul_f32 v36, v36, v0\n"
"v_mul_f32 v37, v37, v1\n"
"v_mul_f32 v38, v38, v2\n"
"v_mul_f32 v39, v39, v3\n"
"v_cvt_pk_bf16_f32 v33, v34, v35\n"
"v_accvgpr_read_b32 v42, a26\n"
"v_accvgpr_read_b32 v43, a27\n"
"v_mul_f32 v40, v40, v0\n"
"v_mul_f32 v41, v41, v1\n"
"v_cvt_pk_bf16_f32 v34, v36, v37\n"
"v_cvt_pk_bf16_f32 v35, v38, v39\n"
"ds_write_b64 v207, v[32:33] offset:2048\n"
"v_accvgpr_read_b32 v44, a28\n"
"v_accvgpr_read_b32 v45, a29\n"
"v_accvgpr_read_b32 v46, a30\n"
"v_accvgpr_read_b32 v47, a31\n"
"v_mul_f32 v42, v42, v2\n"
"v_mul_f32 v43, v43, v3\n"
"v_cvt_pk_bf16_f32 v40, v40, v41\n"
"v_permlane32_swap_b32 v182, v22\n"
"ds_write_b64 v207, v[34:35] offset:2560\n"
"v_mul_f32 v44, v44, v0\n"
"v_mul_f32 v45, v45, v1\n"
"v_mul_f32 v46, v46, v2\n"
"v_mul_f32 v47, v47, v3\n"
"v_cvt_pk_bf16_f32 v41, v42, v43\n"
"v_add_f32 v182, v182, v22\n"
"v_cvt_pk_bf16_f32 v42, v44, v45\n"
"v_cvt_pk_bf16_f32 v43, v46, v47\n"
"ds_write_b64 v207, v[40:41] offset:3072\n"
"v_rcp_f32 v22, v182\n"
"ds_write_b64 v207, v[42:43] offset:3584\n"
"ds_write_b32 v208, v22 offset:128\n"
"ds_read_b128 v[8:11], v209 offset:128\n"
"v_add_f32 v183, v183, v112\n"
"v_add_f32 v183, v183, v113\n"
"v_add_f32 v183, v183, v114\n"
"v_exp_f32 v128, v128\n"
"v_exp_f32 v129, v129\n"
"v_exp_f32 v130, v130\n"
"v_exp_f32 v131, v131\n"
"v_exp_f32 v132, v132\n"
"v_exp_f32 v133, v133\n"
"v_exp_f32 v134, v134\n"
"v_exp_f32 v135, v135\n"
"v_add_f32 v183, v183, v115\n"
"v_add_f32 v183, v183, v116\n"
"v_cvt_pk_bf16_f32 v152, v128, v129\n"
"v_cvt_pk_bf16_f32 v153, v130, v131\n"
"v_cvt_pk_bf16_f32 v154, v132, v133\n"
"v_cvt_pk_bf16_f32 v155, v134, v135\n"
"v_add_f32 v183, v183, v117\n"
"v_add_f32 v183, v183, v118\n"
"v_mfma_f32_16x16x32_bf16 a[128:131], v[152:155], v[220:223], a[128:131]\n"
"v_exp_f32 v136, v136\n"
"v_exp_f32 v137, v137\n"
"v_exp_f32 v138, v138\n"
"v_exp_f32 v139, v139\n"
"v_exp_f32 v140, v140\n"
"v_exp_f32 v141, v141\n"
"v_exp_f32 v142, v142\n"
"v_exp_f32 v143, v143\n"
"v_add_f32 v183, v183, v119\n"
"v_add_f32 v183, v183, v120\n"
"v_cvt_pk_bf16_f32 v156, v136, v137\n"
"v_cvt_pk_bf16_f32 v157, v138, v139\n"
"v_cvt_pk_bf16_f32 v158, v140, v141\n"
"v_cvt_pk_bf16_f32 v159, v142, v143\n"
"v_add_f32 v183, v183, v121\n"
"v_add_f32 v183, v183, v122\n"
"v_mfma_f32_16x16x32_bf16 a[128:131], v[156:159], v[224:227], a[128:131]\n"
"v_add_f32 v184, v184, v128\n"
"v_add_f32 v183, v183, v123\n"
"v_mfma_f32_16x16x32_bf16 a[132:135], v[156:159], v[232:235], a[132:135]\n"
"v_add_f32 v184, v184, v129\n"
"v_add_f32 v183, v183, v124\n"
"v_mfma_f32_16x16x32_bf16 a[132:135], v[152:155], v[228:231], a[132:135]\n"
"v_add_f32 v184, v184, v130\n"
"v_and_b32 v216, 3, v213\n"
"v_mfma_f32_16x16x32_bf16 a[136:139], v[152:155], v[236:239], a[136:139]\n"
"v_add3_u32 v204, s91, v218, 0\n"
"v_add_f32 v183, v183, v125\n"
"v_mfma_f32_16x16x32_bf16 a[136:139], v[156:159], v[240:243], a[136:139]\n"
"v_add_f32 v184, v184, v131\n"
"v_xor_b32 v218, v216, v214\n"
"v_mfma_f32_16x16x32_bf16 a[140:143], v[156:159], v[248:251], a[140:143]\n"
"v_add_f32 v183, v183, v126\n"
"v_add_f32 v184, v184, v132\n"
"v_mfma_f32_16x16x32_bf16 a[140:143], v[152:155], v[244:247], a[140:143]\n"
"v_lshlrev_b32 v218, 3, v218\n"
"v_add_f32 v183, v183, v127\n"
"v_mfma_f32_16x16x32_bf16 a[144:147], v[152:155], v[80:83], a[144:147]\n"
"v_add_f32 v184, v184, v133\n"
"v_lshl_add_u32 v218, v215, 5, v218\n"
"v_mfma_f32_16x16x32_bf16 a[144:147], v[156:159], v[84:87], a[144:147]\n"
"v_accvgpr_read_b32 v48, a32\n"
"v_accvgpr_read_b32 v49, a33\n"
"v_mfma_f32_16x16x32_bf16 a[148:151], v[156:159], v[92:95], a[148:151]\n"
"v_mov_b32 v23, v183\n"
"v_add_f32 v184, v184, v134\n"
"v_mfma_f32_16x16x32_bf16 a[148:151], v[152:155], v[88:91], a[148:151]\n"
"v_lshl_add_u32 v218, v214, 7, v218\n"
"v_accvgpr_read_b32 v50, a34\n"
"v_mfma_f32_16x16x32_bf16 a[152:155], v[152:155], v[96:99], a[152:155]\n"
"v_accvgpr_read_b32 v51, a35\n"
"s_waitcnt lgkmcnt(10)\n"
"v_mul_f32 v48, v48, v4\n"
"v_mfma_f32_16x16x32_bf16 a[152:155], v[156:159], v[100:103], a[152:155]\n"
"v_mul_f32 v49, v49, v5\n"
"v_add_f32 v184, v184, v135\n"
"v_mfma_f32_16x16x32_bf16 a[156:159], v[156:159], v[108:111], a[156:159]\n"
"v_add_u32 v202, 0x18000, v218\n"
"v_accvgpr_read_b32 v52, a36\n"
"v_mfma_f32_16x16x32_bf16 a[156:159], v[152:155], v[104:107], a[156:159]\n"
"v_accvgpr_read_b32 v53, a37\n"
"v_accvgpr_read_b32 v54, a38\n"
"v_accvgpr_read_b32 v55, a39\n"
"v_mul_f32 v50, v50, v6\n"
"v_mul_f32 v51, v51, v7\n"
"v_cvt_pk_bf16_f32 v48, v48, v49\n"
"v_accvgpr_read_b32 v56, a40\n"
"v_accvgpr_read_b32 v57, a41\n"
"v_add_f32 v184, v184, v136\n"
"v_lshlrev_b32 v218, 3, v214\n"
"v_mul_f32 v52, v52, v4\n"
"v_mul_f32 v53, v53, v5\n"
"v_mul_f32 v54, v54, v6\n"
"v_mul_f32 v55, v55, v7\n"
"v_cvt_pk_bf16_f32 v49, v50, v51\n"
"v_accvgpr_read_b32 v58, a42\n"
"v_accvgpr_read_b32 v59, a43\n"
"v_mul_f32 v56, v56, v4\n"
"v_mul_f32 v57, v57, v5\n"
"v_permlane16_swap_b32 v183, v23\n"
"v_add_f32 v184, v184, v137\n"
"v_mad_u32_u24 v205, v217, s93, v218\n"
"v_cvt_pk_bf16_f32 v50, v52, v53\n"
"v_cvt_pk_bf16_f32 v51, v54, v55\n"
"ds_write_b64 v207, v[48:49] offset:4096\n"
"v_accvgpr_read_b32 v60, a44\n"
"v_accvgpr_read_b32 v61, a45\n"
"v_accvgpr_read_b32 v62, a46\n"
"v_accvgpr_read_b32 v63, a47\n"
"v_mul_f32 v58, v58, v6\n"
"v_mul_f32 v59, v59, v7\n"
"v_cvt_pk_bf16_f32 v56, v56, v57\n"
"v_accvgpr_read_b32 v32, a48\n"
"v_accvgpr_read_b32 v33, a49\n"
"v_add_f32 v183, v183, v23\n"
"v_add_f32 v184, v184, v138\n"
"v_lshl_add_u32 v217, v214, 2, v215\n"
"ds_write_b64 v207, v[50:51] offset:4608\n"
"v_mul_f32 v60, v60, v4\n"
"v_mul_f32 v61, v61, v5\n"
"v_mul_f32 v62, v62, v6\n"
"v_mul_f32 v63, v63, v7\n"
"v_cvt_pk_bf16_f32 v57, v58, v59\n"
"v_accvgpr_read_b32 v34, a50\n"
"v_accvgpr_read_b32 v35, a51\n"
"v_mul_f32 v32, v32, v4\n"
"v_mul_f32 v33, v33, v5\n"
"v_mov_b32 v23, v183\n"
"v_add_f32 v184, v184, v139\n"
"v_lshlrev_b32 v218, 7, v217\n"
"v_and_b32 v219, 6, v217\n"
"v_cvt_pk_bf16_f32 v58, v60, v61\n"
"v_cvt_pk_bf16_f32 v59, v62, v63\n"
"ds_write_b64 v207, v[56:57] offset:5120\n"
"v_accvgpr_read_b32 v36, a52\n"
"v_accvgpr_read_b32 v37, a53\n"
"v_accvgpr_read_b32 v38, a54\n"
"v_accvgpr_read_b32 v39, a55\n"
"v_mul_f32 v34, v34, v6\n"
"v_mul_f32 v35, v35, v7\n"
"v_cvt_pk_bf16_f32 v32, v32, v33\n"
"v_accvgpr_read_b32 v40, a56\n"
"v_accvgpr_read_b32 v41, a57\n"
"v_add_f32 v184, v184, v140\n"
"v_and_b32 v217, 1, v216\n"
"ds_write_b64 v207, v[58:59] offset:5632\n"
"v_mul_f32 v36, v36, v4\n"
"v_mul_f32 v37, v37, v5\n"
"v_mul_f32 v38, v38, v6\n"
"v_mul_f32 v39, v39, v7\n"
"v_cvt_pk_bf16_f32 v33, v34, v35\n"
"v_accvgpr_read_b32 v42, a58\n"
"v_accvgpr_read_b32 v43, a59\n"
"v_mul_f32 v40, v40, v4\n"
"v_mul_f32 v41, v41, v5\n"
"v_add_f32 v184, v184, v141\n"
"v_lshl_add_u32 v218, v217, 3, v218\n"
"v_cvt_pk_bf16_f32 v34, v36, v37\n"
"v_cvt_pk_bf16_f32 v35, v38, v39\n"
"ds_write_b64 v207, v[32:33] offset:6144\n"
"v_accvgpr_read_b32 v44, a60\n"
"v_accvgpr_read_b32 v45, a61\n"
"v_accvgpr_read_b32 v46, a62\n"
"v_accvgpr_read_b32 v47, a63\n"
"v_mul_f32 v42, v42, v6\n"
"v_mul_f32 v43, v43, v7\n"
"v_cvt_pk_bf16_f32 v40, v40, v41\n"
"v_permlane32_swap_b32 v183, v23\n"
"v_add_f32 v184, v184, v142\n"
"v_lshrrev_b32 v217, 1, v216\n"
"ds_write_b64 v207, v[34:35] offset:6656\n"
"v_mul_f32 v44, v44, v4\n"
"v_mul_f32 v45, v45, v5\n"
"v_mul_f32 v46, v46, v6\n"
"v_mul_f32 v47, v47, v7\n"
"v_cvt_pk_bf16_f32 v41, v42, v43\n"
"v_add_f32 v183, v183, v23\n"
"v_add_f32 v184, v184, v143\n"
"v_xor_b32 v219, v219, v217\n"
"v_cvt_pk_bf16_f32 v42, v44, v45\n"
"v_cvt_pk_bf16_f32 v43, v46, v47\n"
"ds_write_b64 v207, v[40:41] offset:7168\n"
"v_rcp_f32 v23, v183\n"
"v_accvgpr_read_b32 v48, a64\n"
"v_accvgpr_read_b32 v49, a65\n"
"v_mov_b32 v24, v184\n"
"v_xor_b32 v219, s92, v219\n"
"ds_write_b64 v207, v[42:43] offset:7680\n"
"v_accvgpr_read_b32 v50, a66\n"
"v_accvgpr_read_b32 v51, a67\n"
"s_waitcnt lgkmcnt(8)\n"
"v_mul_f32 v48, v48, v8\n"
"v_mul_f32 v49, v49, v9\n"
"v_lshl_add_u32 v200, v219, 4, v218\n"
"ds_write_b32 v208, v23 offset:192\n"
"v_accvgpr_read_b32 v52, a68\n"
"v_accvgpr_read_b32 v53, a69\n"
"v_accvgpr_read_b32 v54, a70\n"
"v_accvgpr_read_b32 v55, a71\n"
"v_mul_f32 v50, v50, v10\n"
"v_mul_f32 v51, v51, v11\n"
"v_cvt_pk_bf16_f32 v48, v48, v49\n"
"v_accvgpr_read_b32 v56, a72\n"
"v_accvgpr_read_b32 v57, a73\n"
"ds_read_b128 v[12:15], v209 offset:192\n"
"ds_read_b64_tr_b16 a[160:161], v200 offset:0\n"
"ds_read_b64_tr_b16 a[162:163], v200 offset:2048\n"
"ds_read_b64_tr_b16 a[164:165], v200 offset:4096\n"
"ds_read_b64_tr_b16 a[166:167], v200 offset:6144\n"
"ds_read_b64_tr_b16 a[168:169], v200 offset:8192\n"
"ds_read_b64_tr_b16 a[170:171], v200 offset:10240\n"
"ds_read_b64_tr_b16 a[172:173], v200 offset:12288\n"
"ds_read_b64_tr_b16 a[174:175], v200 offset:14336\n"
"ds_read_b64_tr_b16 a[176:177], v200 offset:16384\n"
"ds_read_b64_tr_b16 a[178:179], v200 offset:18432\n"
"ds_read_b64_tr_b16 a[180:181], v200 offset:20480\n"
"ds_read_b64_tr_b16 a[182:183], v200 offset:22528\n"
"ds_read_b64_tr_b16 a[184:185], v200 offset:24576\n"
"ds_read_b64_tr_b16 a[186:187], v200 offset:26624\n"
"ds_read_b64_tr_b16 a[188:189], v200 offset:28672\n"
"ds_read_b64_tr_b16 a[190:191], v200 offset:30720\n"
"ds_read_b64_tr_b16 a[192:193], v200 offset:32768\n"
"ds_read_b64_tr_b16 a[194:195], v200 offset:34816\n"
"ds_read_b64_tr_b16 a[196:197], v200 offset:36864\n"
"ds_read_b64_tr_b16 a[198:199], v200 offset:38912\n"
"ds_read_b64_tr_b16 a[200:201], v200 offset:40960\n"
"ds_read_b64_tr_b16 a[202:203], v200 offset:43008\n"
"ds_read_b64_tr_b16 a[204:205], v200 offset:45056\n"
"ds_read_b64_tr_b16 a[206:207], v200 offset:47104\n"
"ds_read_b64_tr_b16 a[208:209], v200 offset:49152\n"
"ds_read_b64_tr_b16 a[210:211], v200 offset:51200\n"
"ds_read_b64_tr_b16 a[212:213], v200 offset:53248\n"
"ds_read_b64_tr_b16 a[214:215], v200 offset:55296\n"
"ds_read_b64_tr_b16 a[216:217], v200 offset:57344\n"
"ds_read_b64_tr_b16 a[218:219], v200 offset:59392\n"
"ds_read_b64_tr_b16 a[220:221], v200 offset:61440\n"
"ds_read_b64_tr_b16 a[222:223], v200 offset:63488\n"
"v_mul_f32 v52, v52, v8\n"
"v_mul_f32 v53, v53, v9\n"
"v_mul_f32 v54, v54, v10\n"
"v_mul_f32 v55, v55, v11\n"
"v_cvt_pk_bf16_f32 v49, v50, v51\n"
"v_accvgpr_read_b32 v58, a74\n"
"v_accvgpr_read_b32 v59, a75\n"
"v_mul_f32 v56, v56, v8\n"
"v_mul_f32 v57, v57, v9\n"
"v_permlane16_swap_b32 v184, v24\n"
"v_cvt_pk_bf16_f32 v50, v52, v53\n"
"v_cvt_pk_bf16_f32 v51, v54, v55\n"
"ds_write_b64 v207, v[48:49] offset:8192\n"
"v_accvgpr_read_b32 v60, a76\n"
"v_accvgpr_read_b32 v61, a77\n"
"v_accvgpr_read_b32 v62, a78\n"
"v_accvgpr_read_b32 v63, a79\n"
"v_mul_f32 v58, v58, v10\n"
"v_mul_f32 v59, v59, v11\n"
"v_cvt_pk_bf16_f32 v56, v56, v57\n"
"v_accvgpr_read_b32 v32, a80\n"
"v_accvgpr_read_b32 v33, a81\n"
"v_add_f32 v184, v184, v24\n"
"ds_write_b64 v207, v[50:51] offset:8704\n"
"v_mul_f32 v60, v60, v8\n"
"v_mul_f32 v61, v61, v9\n"
"v_mul_f32 v62, v62, v10\n"
"v_mul_f32 v63, v63, v11\n"
"v_cvt_pk_bf16_f32 v57, v58, v59\n"
"v_accvgpr_read_b32 v34, a82\n"
"v_accvgpr_read_b32 v35, a83\n"
"v_mul_f32 v32, v32, v8\n"
"v_mul_f32 v33, v33, v9\n"
"v_mov_b32 v24, v184\n"
"v_cvt_pk_bf16_f32 v58, v60, v61\n"
"v_cvt_pk_bf16_f32 v59, v62, v63\n"
"ds_write_b64 v207, v[56:57] offset:9216\n"
"v_accvgpr_read_b32 v36, a84\n"
"v_accvgpr_read_b32 v37, a85\n"
"v_accvgpr_read_b32 v38, a86\n"
"v_accvgpr_read_b32 v39, a87\n"
"v_mul_f32 v34, v34, v10\n"
"v_mul_f32 v35, v35, v11\n"
"v_cvt_pk_bf16_f32 v32, v32, v33\n"
"v_accvgpr_read_b32 v40, a88\n"
"v_accvgpr_read_b32 v41, a89\n"
"ds_write_b64 v207, v[58:59] offset:9728\n"
"v_mul_f32 v36, v36, v8\n"
"v_mul_f32 v37, v37, v9\n"
"v_mul_f32 v38, v38, v10\n"
"v_mul_f32 v39, v39, v11\n"
"v_cvt_pk_bf16_f32 v33, v34, v35\n"
"v_accvgpr_read_b32 v42, a90\n"
"v_accvgpr_read_b32 v43, a91\n"
"v_mul_f32 v40, v40, v8\n"
"v_mul_f32 v41, v41, v9\n"
"v_cvt_pk_bf16_f32 v34, v36, v37\n"
"v_cvt_pk_bf16_f32 v35, v38, v39\n"
"ds_write_b64 v207, v[32:33] offset:10240\n"
"v_accvgpr_read_b32 v44, a92\n"
"v_accvgpr_read_b32 v45, a93\n"
"v_accvgpr_read_b32 v46, a94\n"
"v_accvgpr_read_b32 v47, a95\n"
"v_mul_f32 v42, v42, v10\n"
"v_mul_f32 v43, v43, v11\n"
"v_cvt_pk_bf16_f32 v40, v40, v41\n"
"v_permlane32_swap_b32 v184, v24\n"
"ds_write_b64 v207, v[34:35] offset:10752\n"
"v_mul_f32 v44, v44, v8\n"
"v_mul_f32 v45, v45, v9\n"
"v_mul_f32 v46, v46, v10\n"
"v_mul_f32 v47, v47, v11\n"
"v_cvt_pk_bf16_f32 v41, v42, v43\n"
"v_add_f32 v184, v184, v24\n"
"v_cvt_pk_bf16_f32 v42, v44, v45\n"
"v_cvt_pk_bf16_f32 v43, v46, v47\n"
"ds_write_b64 v207, v[40:41] offset:11264\n"
"v_rcp_f32 v24, v184\n"
"ds_write_b64 v207, v[42:43] offset:11776\n"
"ds_write_b32 v208, v24 offset:256\n"
"ds_read_b128 v[16:19], v209 offset:256\n"
"v_accvgpr_read_b32 v48, a96\n"
"v_accvgpr_read_b32 v49, a97\n"
"v_accvgpr_read_b32 v50, a98\n"
"v_accvgpr_read_b32 v51, a99\n"
"s_waitcnt lgkmcnt(15)\n"
"v_pk_mul_f32 v[48:49], v[48:49], v[12:13]\n"
"v_accvgpr_read_b32 v52, a100\n"
"v_accvgpr_read_b32 v53, a101\n"
"v_accvgpr_read_b32 v54, a102\n"
"v_accvgpr_read_b32 v55, a103\n"
"v_pk_mul_f32 v[50:51], v[50:51], v[14:15]\n"
"v_cvt_pk_bf16_f32 v48, v48, v49\n"
"v_accvgpr_read_b32 v56, a104\n"
"v_accvgpr_read_b32 v57, a105\n"
"v_pk_mul_f32 v[52:53], v[52:53], v[12:13]\n"
"v_pk_mul_f32 v[54:55], v[54:55], v[14:15]\n"
"v_cvt_pk_bf16_f32 v49, v50, v51\n"
"v_accvgpr_read_b32 v58, a106\n"
"v_accvgpr_read_b32 v59, a107\n"
"v_pk_mul_f32 v[56:57], v[56:57], v[12:13]\n"
"v_cvt_pk_bf16_f32 v50, v52, v53\n"
"v_cvt_pk_bf16_f32 v51, v54, v55\n"
"ds_write_b64 v207, v[48:49] offset:12288\n"
"v_accvgpr_read_b32 v60, a108\n"
"v_accvgpr_read_b32 v61, a109\n"
"v_accvgpr_read_b32 v62, a110\n"
"v_accvgpr_read_b32 v63, a111\n"
"v_pk_mul_f32 v[58:59], v[58:59], v[14:15]\n"
"v_cvt_pk_bf16_f32 v56, v56, v57\n"
"v_accvgpr_read_b32 v32, a112\n"
"v_accvgpr_read_b32 v33, a113\n"
"ds_write_b64 v207, v[50:51] offset:12800\n"
"v_pk_mul_f32 v[60:61], v[60:61], v[12:13]\n"
"v_pk_mul_f32 v[62:63], v[62:63], v[14:15]\n"
"v_cvt_pk_bf16_f32 v57, v58, v59\n"
"v_accvgpr_read_b32 v34, a114\n"
"v_accvgpr_read_b32 v35, a115\n"
"v_pk_mul_f32 v[32:33], v[32:33], v[12:13]\n"
"v_cvt_pk_bf16_f32 v58, v60, v61\n"
"v_cvt_pk_bf16_f32 v59, v62, v63\n"
"ds_write_b64 v207, v[56:57] offset:13312\n"
"v_accvgpr_read_b32 v36, a116\n"
"v_accvgpr_read_b32 v37, a117\n"
"v_accvgpr_read_b32 v38, a118\n"
"v_accvgpr_read_b32 v39, a119\n"
"v_pk_mul_f32 v[34:35], v[34:35], v[14:15]\n"
"v_cvt_pk_bf16_f32 v32, v32, v33\n"
"v_accvgpr_read_b32 v40, a120\n"
"v_accvgpr_read_b32 v41, a121\n"
"v_max3_f32 v25, v180, v181, v182\n"
"ds_write_b64 v207, v[58:59] offset:13824\n"
"v_pk_mul_f32 v[36:37], v[36:37], v[12:13]\n"
"v_pk_mul_f32 v[38:39], v[38:39], v[14:15]\n"
"v_cvt_pk_bf16_f32 v33, v34, v35\n"
"v_accvgpr_read_b32 v42, a122\n"
"v_accvgpr_read_b32 v43, a123\n"
"v_pk_mul_f32 v[40:41], v[40:41], v[12:13]\n"
"v_max3_f32 v25, v25, v183, v184\n"
"v_cvt_pk_bf16_f32 v34, v36, v37\n"
"v_cvt_pk_bf16_f32 v35, v38, v39\n"
"ds_write_b64 v207, v[32:33] offset:14336\n"
"v_accvgpr_read_b32 v44, a124\n"
"v_accvgpr_read_b32 v45, a125\n"
"v_accvgpr_read_b32 v46, a126\n"
"v_accvgpr_read_b32 v47, a127\n"
"v_pk_mul_f32 v[42:43], v[42:43], v[14:15]\n"
"v_cvt_pk_bf16_f32 v40, v40, v41\n"
"v_accvgpr_read_b32 v48, a128\n"
"v_accvgpr_read_b32 v49, a129\n"
"v_fma_f32 v25, v180, 0, v25\n"
"ds_write_b64 v207, v[34:35] offset:14848\n"
"v_pk_mul_f32 v[44:45], v[44:45], v[12:13]\n"
"v_pk_mul_f32 v[46:47], v[46:47], v[14:15]\n"
"v_cvt_pk_bf16_f32 v41, v42, v43\n"
"v_accvgpr_read_b32 v50, a130\n"
"v_accvgpr_read_b32 v51, a131\n"
"s_waitcnt lgkmcnt(6)\n"
"v_pk_mul_f32 v[48:49], v[48:49], v[16:17]\n"
"v_fma_f32 v25, v181, 0, v25\n"
"v_cvt_pk_bf16_f32 v42, v44, v45\n"
"v_cvt_pk_bf16_f32 v43, v46, v47\n"
"ds_write_b64 v207, v[40:41] offset:15360\n"
"v_accvgpr_read_b32 v52, a132\n"
"v_accvgpr_read_b32 v53, a133\n"
"v_accvgpr_read_b32 v54, a134\n"
"v_accvgpr_read_b32 v55, a135\n"
"v_pk_mul_f32 v[50:51], v[50:51], v[18:19]\n"
"v_cvt_pk_bf16_f32 v48, v48, v49\n"
"v_accvgpr_read_b32 v56, a136\n"
"v_accvgpr_read_b32 v57, a137\n"
"v_fma_f32 v25, v182, 0, v25\n"
"ds_write_b64 v207, v[42:43] offset:15872\n"
"v_pk_mul_f32 v[52:53], v[52:53], v[16:17]\n"
"v_pk_mul_f32 v[54:55], v[54:55], v[18:19]\n"
"v_cvt_pk_bf16_f32 v49, v50, v51\n"
"v_accvgpr_read_b32 v58, a138\n"
"v_accvgpr_read_b32 v59, a139\n"
"v_pk_mul_f32 v[56:57], v[56:57], v[16:17]\n"
"v_fma_f32 v25, v183, 0, v25\n"
"v_cvt_pk_bf16_f32 v50, v52, v53\n"
"v_cvt_pk_bf16_f32 v51, v54, v55\n"
"ds_write_b64 v207, v[48:49] offset:16384\n"
"v_accvgpr_read_b32 v60, a140\n"
"v_accvgpr_read_b32 v61, a141\n"
"v_accvgpr_read_b32 v62, a142\n"
"v_accvgpr_read_b32 v63, a143\n"
"v_pk_mul_f32 v[58:59], v[58:59], v[18:19]\n"
"v_cvt_pk_bf16_f32 v56, v56, v57\n"
"v_accvgpr_read_b32 v32, a144\n"
"v_accvgpr_read_b32 v33, a145\n"
"v_fma_f32 v25, v184, 0, v25\n"
"ds_write_b64 v207, v[50:51] offset:16896\n"
"v_pk_mul_f32 v[60:61], v[60:61], v[16:17]\n"
"v_pk_mul_f32 v[62:63], v[62:63], v[18:19]\n"
"v_cvt_pk_bf16_f32 v57, v58, v59\n"
"v_accvgpr_read_b32 v34, a146\n"
"v_accvgpr_read_b32 v35, a147\n"
"v_pk_mul_f32 v[32:33], v[32:33], v[16:17]\n"
"v_cmp_ngt_f32 vcc, 0x5d800000, v25\n"
"v_cvt_pk_bf16_f32 v58, v60, v61\n"
"v_cvt_pk_bf16_f32 v59, v62, v63\n"
"ds_write_b64 v207, v[56:57] offset:17408\n"
"v_accvgpr_read_b32 v36, a148\n"
"v_accvgpr_read_b32 v37, a149\n"
"v_accvgpr_read_b32 v38, a150\n"
"v_accvgpr_read_b32 v39, a151\n"
"v_pk_mul_f32 v[34:35], v[34:35], v[18:19]\n"
"v_cvt_pk_bf16_f32 v32, v32, v33\n"
"v_accvgpr_read_b32 v40, a152\n"
"v_accvgpr_read_b32 v41, a153\n"
"ds_write_b64 v207, v[58:59] offset:17920\n"
"v_pk_mul_f32 v[36:37], v[36:37], v[16:17]\n"
"v_pk_mul_f32 v[38:39], v[38:39], v[18:19]\n"
"v_cvt_pk_bf16_f32 v33, v34, v35\n"
"v_accvgpr_read_b32 v42, a154\n"
"v_accvgpr_read_b32 v43, a155\n"
"v_pk_mul_f32 v[40:41], v[40:41], v[16:17]\n"
"v_cvt_pk_bf16_f32 v34, v36, v37\n"
"v_cvt_pk_bf16_f32 v35, v38, v39\n"
"ds_write_b64 v207, v[32:33] offset:18432\n"
"v_accvgpr_read_b32 v44, a156\n"
"v_accvgpr_read_b32 v45, a157\n"
"v_accvgpr_read_b32 v46, a158\n"
"v_accvgpr_read_b32 v47, a159\n"
"v_pk_mul_f32 v[42:43], v[42:43], v[18:19]\n"
"v_cvt_pk_bf16_f32 v40, v40, v41\n"
"ds_write_b64 v207, v[34:35] offset:18944\n"
"v_pk_mul_f32 v[44:45], v[44:45], v[16:17]\n"
"v_pk_mul_f32 v[46:47], v[46:47], v[18:19]\n"
"v_cvt_pk_bf16_f32 v41, v42, v43\n"
"v_cvt_pk_bf16_f32 v42, v44, v45\n"
"v_cvt_pk_bf16_f32 v43, v46, v47\n"
"ds_write_b64 v207, v[40:41] offset:19456\n"
"s_cmp_lg_u64 vcc, 0\ns_cselect_b32 s22, 1, 0\ns_andn2_b32 s22, s22, s85\ns_lshl_b32 s23, s19, 2\ns_add_u32 s23, s23, 161792\n"
"v_add_u32 v210, s97, v204\n"
"v_min_u32 v218, 1, v213\n"
"ds_write_b64 v207, v[42:43] offset:19968\n"
"v_mov_b32 v25, s22\n"
"v_mov_b32 v26, s23\n"
"v_add_u32 v204, s96, v204\n"
"v_lshlrev_b32 v203, 4, v213\n"
"v_add_u32 v201, 0x10000, v200\n"
"v_mad_u32_u24 v206, v218, s94, v252\n"
"ds_write_b32 v26, v25 offset:0\n"
"s_waitcnt lgkmcnt(0)\n"
"L_join_%=:\n"
"s_barrier\n"
"v_mov_b32 v28, 0x27800\n"
"ds_read_b128 v[28:31], v28 offset:0\n"
"ds_read_b64_tr_b16 a[0:1], v202 offset:0\n"
"ds_read_b64_tr_b16 a[2:3], v202 offset:512\n"
"ds_read_b64_tr_b16 a[4:5], v202 offset:1024\n"
"ds_read_b64_tr_b16 a[6:7], v202 offset:1536\n"
"ds_read_b64_tr_b16 a[8:9], v202 offset:2048\n"
"ds_read_b64_tr_b16 a[10:11], v202 offset:2560\n"
"ds_read_b64_tr_b16 a[12:13], v202 offset:3072\n"
"ds_read_b64_tr_b16 a[14:15], v202 offset:3584\n"
"ds_read_b64_tr_b16 a[16:17], v202 offset:20480\n"
"ds_read_b64_tr_b16 a[18:19], v202 offset:20992\n"
"ds_read_b64_tr_b16 a[20:21], v202 offset:21504\n"
"ds_read_b64_tr_b16 a[22:23], v202 offset:22016\n"
"ds_read_b64_tr_b16 a[24:25], v202 offset:22528\n"
"ds_read_b64_tr_b16 a[26:27], v202 offset:23040\n"
"ds_read_b64_tr_b16 a[28:29], v202 offset:23552\n"
"ds_read_b64_tr_b16 a[30:31], v202 offset:24064\n"
"s_waitcnt lgkmcnt(15)\n"
"v_or3_b32 v28, v28, v29, v30\n"
"v_or_b32 v28, v28, v31\n"
"s_nop 0\n"
"v_readfirstlane_b32 s20, v28\n"
"s_nop 3\n"
"s_cmp_lg_u32 s20, 0\ns_cbranch_scc1 L_fb_%=\n"
"s_cmp_lg_u32 s19, 0\ns_cbranch_scc1 L_sg1_%=\ns_mov_b64 exec, 1\nbuffer_store_dword v253, v206, s[68:71], 0 offen offset:0 sc1\ns_mov_b64 exec, -1\nL_sg1_%=:\n"
"s_waitcnt lgkmcnt(14)\n"
"v_mfma_f32_16x16x32_bf16 v[0:3], a[160:163], a[0:3], 0\n"
"s_waitcnt lgkmcnt(12)\n"
"ds_read_b64_tr_b16 a[32:33], v202 offset:4096\n"
"ds_read_b64_tr_b16 a[34:35], v202 offset:4608\n"
"v_mfma_f32_16x16x32_bf16 v[0:3], a[164:167], a[4:7], v[0:3]\n"
"s_waitcnt lgkmcnt(12)\n"
"s_add_u32 s76, s26, 0x180\ns_add_u32 m0, s81, 0\n"
"ds_read_b64_tr_b16 a[36:37], v202 offset:5120\n"
"ds_read_b64_tr_b16 a[38:39], v202 offset:5632\n"
"v_mfma_f32_16x16x32_bf16 v[0:3], a[168:171], a[8:11], v[0:3]\n"
"s_waitcnt lgkmcnt(12)\n"
"ds_read_b64_tr_b16 a[40:41], v202 offset:6144\n"
"ds_read_b64_tr_b16 a[42:43], v202 offset:6656\n"
"v_mfma_f32_16x16x32_bf16 v[0:3], a[172:175], a[12:15], v[0:3]\n"
"s_waitcnt lgkmcnt(12)\n"
"ds_read_b64_tr_b16 a[44:45], v202 offset:7168\n"
"ds_read_b64_tr_b16 a[46:47], v202 offset:7680\n"
"v_mfma_f32_16x16x32_bf16 v[0:3], a[176:179], a[16:19], v[0:3]\n"
"s_waitcnt lgkmcnt(12)\n"
"ds_read_b64_tr_b16 a[48:49], v202 offset:24576\n"
"ds_read_b64_tr_b16 a[50:51], v202 offset:25088\n"
"v_mfma_f32_16x16x32_bf16 v[0:3], a[180:183], a[20:23], v[0:3]\n"
"s_waitcnt lgkmcnt(12)\n"
"ds_read_b64_tr_b16 a[52:53], v202 offset:25600\n"
"ds_read_b64_tr_b16 a[54:55], v202 offset:26112\n"
"v_mfma_f32_16x16x32_bf16 v[0:3], a[184:187], a[24:27], v[0:3]\n"
"s_waitcnt lgkmcnt(12)\n"
"ds_read_b64_tr_b16 a[56:57], v202 offset:26624\n"
"ds_read_b64_tr_b16 a[58:59], v202 offset:27136\n"
"buffer_load_dwordx4 v199, s[60:63], s76 offen offset:0 lds\n"
"v_mfma_f32_16x16x32_bf16 v[0:3], a[188:191], a[28:31], v[0:3]\n"
"s_waitcnt lgkmcnt(12)\n"
"s_add_u32 s76, s26, 0x2580\ns_add_u32 m0, s81, 0x400\n"
"ds_read_b64_tr_b16 a[60:61], v202 offset:27648\n"
"ds_read_b64_tr_b16 a[62:63], v202 offset:28160\n"
"v_mfma_f32_16x16x32_bf16 v[4:7], a[160:163], a[32:35], 0\n"
"s_waitcnt lgkmcnt(12)\n"
"ds_read_b64_tr_b16 a[64:65], v202 offset:8192\n"
"ds_read_b64_tr_b16 a[66:67], v202 offset:8704\n"
"v_mfma_f32_16x16x32_bf16 v[4:7], a[164:167], a[36:39], v[4:7]\n"
"s_waitcnt lgkmcnt(12)\n"
"ds_read_b64_tr_b16 a[68:69], v202 offset:9216\n"
"ds_read_b64_tr_b16 a[70:71], v202 offset:9728\n"
"buffer_load_dwordx4 v199, s[60:63], s76 offen offset:0 lds\n"
"v_mfma_f32_16x16x32_bf16 v[4:7], a[168:171], a[40:43], v[4:7]\n"
"s_waitcnt lgkmcnt(12)\n"
"s_add_u32 s76, s26, 0x4980\ns_add_u32 m0, s81, 0x800\n"
"ds_read_b64_tr_b16 a[72:73], v202 offset:10240\n"
"ds_read_b64_tr_b16 a[74:75], v202 offset:10752\n"
"v_mfma_f32_16x16x32_bf16 v[4:7], a[172:175], a[44:47], v[4:7]\n"
"s_waitcnt lgkmcnt(12)\n"
"ds_read_b64_tr_b16 a[76:77], v202 offset:11264\n"
"ds_read_b64_tr_b16 a[78:79], v202 offset:11776\n"
"v_mfma_f32_16x16x32_bf16 v[4:7], a[176:179], a[48:51], v[4:7]\n"
"s_waitcnt lgkmcnt(12)\n"
"ds_read_b64_tr_b16 a[80:81], v202 offset:28672\n"
"ds_read_b64_tr_b16 a[82:83], v202 offset:29184\n"
"buffer_load_dwordx4 v199, s[60:63], s76 offen offset:0 lds\n"
"v_mfma_f32_16x16x32_bf16 v[4:7], a[180:183], a[52:55], v[4:7]\n"
"s_waitcnt lgkmcnt(12)\n"
"s_add_u32 s76, s26, 0x6d80\ns_add_u32 m0, s81, 0xc00\n"
"ds_read_b64_tr_b16 a[84:85], v202 offset:29696\n"
"ds_read_b64_tr_b16 a[86:87], v202 offset:30208\n"
"v_mfma_f32_16x16x32_bf16 v[4:7], a[184:187], a[56:59], v[4:7]\n"
"s_waitcnt lgkmcnt(12)\n"
"ds_read_b64_tr_b16 a[88:89], v202 offset:30720\n"
"ds_read_b64_tr_b16 a[90:91], v202 offset:31232\n"
"v_mfma_f32_16x16x32_bf16 v[4:7], a[188:191], a[60:63], v[4:7]\n"
"s_waitcnt lgkmcnt(12)\n"
"ds_read_b64_tr_b16 a[92:93], v202 offset:31744\n"
"ds_read_b64_tr_b16 a[94:95], v202 offset:32256\n"
"buffer_load_dwordx4 v199, s[60:63], s76 offen offset:0 lds\n"
"v_mfma_f32_16x16x32_bf16 v[8:11], a[160:163], a[64:67], 0\n"
"s_waitcnt lgkmcnt(12)\n"
"s_add_u32 s76, s26, 0x9180\ns_add_u32 m0, s81, 0x1000\n"
"ds_read_b64_tr_b16 a[96:97], v202 offset:12288\n"
"ds_read_b64_tr_b16 a[98:99], v202 offset:12800\n"
"v_mfma_f32_16x16x32_bf16 v[8:11], a[164:167], a[68:71], v[8:11]\n"
"s_waitcnt lgkmcnt(12)\n"
"ds_read_b64_tr_b16 a[100:101], v202 offset:13312\n"
"ds_read_b64_tr_b16 a[102:103], v202 offset:13824\n"
"v_mfma_f32_16x16x32_bf16 v[8:11], a[168:171], a[72:75], v[8:11]\n"
"s_waitcnt lgkmcnt(12)\n"
"ds_read_b64_tr_b16 a[104:105], v202 offset:14336\n"
"ds_read_b64_tr_b16 a[106:107], v202 offset:14848\n"
"buffer_load_dwordx4 v199, s[60:63], s76 offen offset:0 lds\n"
"v_mfma_f32_16x16x32_bf16 v[8:11], a[172:175], a[76:79], v[8:11]\n"
"s_waitcnt lgkmcnt(12)\n"
"s_add_u32 s76, s26, 0xb580\ns_add_u32 m0, s81, 0x1400\n"
"ds_read_b64_tr_b16 a[108:109], v202 offset:15360\n"
"ds_read_b64_tr_b16 a[110:111], v202 offset:15872\n"
"v_mfma_f32_16x16x32_bf16 v[8:11], a[176:179], a[80:83], v[8:11]\n"
"s_waitcnt lgkmcnt(12)\n"
"ds_read_b64_tr_b16 a[112:113], v202 offset:32768\n"
"ds_read_b64_tr_b16 a[114:115], v202 offset:33280\n"
"v_mfma_f32_16x16x32_bf16 v[8:11], a[180:183], a[84:87], v[8:11]\n"
"s_waitcnt lgkmcnt(12)\n"
"ds_read_b64_tr_b16 a[116:117], v202 offset:33792\n"
"ds_read_b64_tr_b16 a[118:119], v202 offset:34304\n"
"buffer_load_dwordx4 v199, s[60:63], s76 offen offset:0 lds\n"
"v_mfma_f32_16x16x32_bf16 v[8:11], a[184:187], a[88:91], v[8:11]\n"
"s_waitcnt lgkmcnt(12)\n"
"s_add_u32 s76, s26, 0xd980\ns_add_u32 m0, s81, 0x1800\n"
"ds_read_b64_tr_b16 a[120:121], v202 offset:34816\n"
"ds_read_b64_tr_b16 a[122:123], v202 offset:35328\n"
"v_mfma_f32_16x16x32_bf16 v[8:11], a[188:191], a[92:95], v[8:11]\n"
"s_waitcnt lgkmcnt(12)\n"
"ds_read_b64_tr_b16 a[124:125], v202 offset:35840\n"
"ds_read_b64_tr_b16 a[126:127], v202 offset:36352\n"
"v_mfma_f32_16x16x32_bf16 v[12:15], a[160:163], a[96:99], 0\n"
"s_waitcnt lgkmcnt(12)\n"
"ds_read_b64_tr_b16 a[128:129], v202 offset:16384\n"
"ds_read_b64_tr_b16 a[130:131], v202 offset:16896\n"
"buffer_load_dwordx4 v199, s[60:63], s76 offen offset:0 lds\n"
"v_mfma_f32_16x16x32_bf16 v[12:15], a[164:167], a[100:103], v[12:15]\n"
"s_waitcnt lgkmcnt(12)\n"
"s_add_u32 s76, s26, 0xfd80\ns_add_u32 m0, s81, 0x1c00\n"
"ds_read_b64_tr_b16 a[132:133], v202 offset:17408\n"
"ds_read_b64_tr_b16 a[134:135], v202 offset:17920\n"
"v_mfma_f32_16x16x32_bf16 v[12:15], a[168:171], a[104:107], v[12:15]\n"
"s_waitcnt lgkmcnt(12)\n"
"ds_read_b64_tr_b16 a[136:137], v202 offset:18432\n"
"ds_read_b64_tr_b16 a[138:139], v202 offset:18944\n"
"v_mfma_f32_16x16x32_bf16 v[12:15], a[172:175], a[108:111], v[12:15]\n"
"s_waitcnt lgkmcnt(12)\n"
"ds_read_b64_tr_b16 a[140:141], v202 offset:19456\n"
"ds_read_b64_tr_b16 a[142:143], v202 offset:19968\n"
"buffer_load_dwordx4 v199, s[60:63], s76 offen offset:0 lds\n"
"v_mfma_f32_16x16x32_bf16 v[12:15], a[176:179], a[112:115], v[12:15]\n"
"s_waitcnt lgkmcnt(12)\n"
"s_add_u32 s76, s26, 0x200\ns_add_u32 m0, s81, 0x8000\n"
"ds_read_b64_tr_b16 a[144:145], v202 offset:36864\n"
"ds_read_b64_tr_b16 a[146:147], v202 offset:37376\n"
"v_mfma_f32_16x16x32_bf16 v[12:15], a[180:183], a[116:119], v[12:15]\n"
"s_waitcnt lgkmcnt(12)\n"
"ds_read_b64_tr_b16 a[148:149], v202 offset:37888\n"
"ds_read_b64_tr_b16 a[150:151], v202 offset:38400\n"
"v_mfma_f32_16x16x32_bf16 v[12:15], a[184:187], a[120:123], v[12:15]\n"
"s_waitcnt lgkmcnt(12)\n"
"ds_read_b64_tr_b16 a[152:153], v202 offset:38912\n"
"ds_read_b64_tr_b16 a[154:155], v202 offset:39424\n"
"buffer_load_dwordx4 v199, s[60:63], s76 offen offset:0 lds\n"
"v_mfma_f32_16x16x32_bf16 v[12:15], a[188:191], a[124:127], v[12:15]\n"
"s_waitcnt lgkmcnt(12)\n"
"s_add_u32 s76, s26, 0x2600\ns_add_u32 m0, s81, 0x8400\n"
"ds_read_b64_tr_b16 a[156:157], v202 offset:39936\n"
"ds_read_b64_tr_b16 a[158:159], v202 offset:40448\n"
"v_mfma_f32_16x16x32_bf16 v[16:19], a[160:163], a[128:131], 0\n"
"s_waitcnt lgkmcnt(12)\n"
"buffer_load_dwordx4 v199, s[60:63], s76 offen offset:0 lds\n"
"v_mfma_f32_16x16x32_bf16 v[16:19], a[164:167], a[132:135], v[16:19]\n"
"s_waitcnt lgkmcnt(10)\n"
"s_add_u32 s76, s26, 0x4a00\ns_add_u32 m0, s81, 0x8800\n"
"v_mfma_f32_16x16x32_bf16 v[16:19], a[168:171], a[136:139], v[16:19]\n"
"s_waitcnt lgkmcnt(8)\n"
"buffer_load_dwordx4 v199, s[60:63], s76 offen offset:0 lds\n"
"v_mfma_f32_16x16x32_bf16 v[16:19], a[172:175], a[140:143], v[16:19]\n"
"s_waitcnt lgkmcnt(6)\n"
"s_add_u32 s76, s26, 0x6e00\ns_add_u32 m0, s81, 0x8c00\n"
"v_mfma_f32_16x16x32_bf16 v[16:19], a[176:179], a[144:147], v[16:19]\n"
"s_waitcnt lgkmcnt(4)\n"
"buffer_load_dwordx4 v199, s[60:63], s76 offen offset:0 lds\n"
"v_mfma_f32_16x16x32_bf16 v[16:19], a[180:183], a[148:151], v[16:19]\n"
"s_waitcnt lgkmcnt(2)\n"
"s_add_u32 s76, s26, 0x9200\ns_add_u32 m0, s81, 0x9000\n"
"v_mfma_f32_16x16x32_bf16 v[16:19], a[184:187], a[152:155], v[16:19]\n"
"s_waitcnt lgkmcnt(0)\n"
"s_waitcnt vmcnt(12)\n"
"buffer_load_dwordx4 v199, s[60:63], s76 offen offset:0 lds\n"
"v_mfma_f32_16x16x32_bf16 v[16:19], a[188:191], a[156:159], v[16:19]\n"
"s_barrier\n"
"v_mfma_f32_16x16x32_bf16 v[20:23], a[192:195], a[0:3], 0\n"
"s_cmp_lg_u32 s19, 0\ns_cbranch_scc1 L_sg2_%=\ns_mov_b64 exec, 1\nbuffer_store_dword v253, v206, s[68:71], 0 offen offset:4 sc1\ns_mov_b64 exec, -1\nL_sg2_%=:\n"
"ds_read_b64_tr_b16 a[160:161], v201 offset:0\n"
"v_mfma_f32_16x16x32_bf16 v[20:23], a[196:199], a[4:7], v[20:23]\n"
"s_add_u32 s76, s26, 0xb600\ns_add_u32 m0, s81, 0x9400\n"
"ds_read_b64_tr_b16 a[162:163], v201 offset:2048\n"
"v_mfma_f32_16x16x32_bf16 v[20:23], a[200:203], a[8:11], v[20:23]\n"
"ds_read_b64_tr_b16 a[164:165], v201 offset:4096\n"
"buffer_load_dwordx4 v199, s[60:63], s76 offen offset:0 lds\n"
"v_mfma_f32_16x16x32_bf16 v[20:23], a[204:207], a[12:15], v[20:23]\n"
"s_add_u32 s76, s26, 0xda00\ns_add_u32 m0, s81, 0x9800\n"
"ds_read_b64_tr_b16 a[166:167], v201 offset:6144\n"
"v_mfma_f32_16x16x32_bf16 v[20:23], a[208:211], a[16:19], v[20:23]\n"
"ds_read_b64_tr_b16 a[168:169], v201 offset:8192\n"
"buffer_load_dwordx4 v199, s[60:63], s76 offen offset:0 lds\n"
"v_mfma_f32_16x16x32_bf16 v[20:23], a[212:215], a[20:23], v[20:23]\n"
"ds_read_b64_tr_b16 a[170:171], v201 offset:10240\n"
"v_mfma_f32_16x16x32_bf16 v[20:23], a[216:219], a[24:27], v[20:23]\n"
"s_add_u32 s76, s26, 0xfe00\ns_add_u32 m0, s81, 0x9c00\n"
"ds_read_b64_tr_b16 a[172:173], v201 offset:12288\n"
"v_mfma_f32_16x16x32_bf16 v[20:23], a[220:223], a[28:31], v[20:23]\n"
"ds_read_b64_tr_b16 a[174:175], v201 offset:14336\n"
"buffer_load_dwordx4 v199, s[60:63], s76 offen offset:0 lds\n"
"v_mfma_f32_16x16x32_bf16 v[24:27], a[192:195], a[32:35], 0\n"
"ds_read_b64_tr_b16 a[176:177], v201 offset:16384\n"
"v_mfma_f32_16x16x32_bf16 v[24:27], a[196:199], a[36:39], v[24:27]\n"
"ds_read_b64_tr_b16 a[178:179], v201 offset:18432\n"
"v_mfma_f32_16x16x32_bf16 v[24:27], a[200:203], a[40:43], v[24:27]\n"
"ds_read_b64_tr_b16 a[180:181], v201 offset:20480\n"
"v_mfma_f32_16x16x32_bf16 v[24:27], a[204:207], a[44:47], v[24:27]\n"
"ds_read_b64_tr_b16 a[182:183], v201 offset:22528\n"
"v_mfma_f32_16x16x32_bf16 v[24:27], a[208:211], a[48:51], v[24:27]\n"
"buffer_load_dword v212, v254, s[68:71], 0 offen offset:0 sc1\n"
"ds_read_b64_tr_b16 a[184:185], v201 offset:24576\n"
"v_mfma_f32_16x16x32_bf16 v[24:27], a[212:215], a[52:55], v[24:27]\n"
"ds_read_b64_tr_b16 a[186:187], v201 offset:26624\n"
"v_mfma_f32_16x16x32_bf16 v[24:27], a[216:219], a[56:59], v[24:27]\n"
"ds_read_b64_tr_b16 a[188:189], v201 offset:28672\n"
"v_mfma_f32_16x16x32_bf16 v[24:27], a[220:223], a[60:63], v[24:27]\n"
"ds_read_b64_tr_b16 a[190:191], v201 offset:30720\n"
"v_mfma_f32_16x16x32_bf16 v[28:31], a[192:195], a[64:67], 0\n"
"v_mfma_f32_16x16x32_bf16 v[28:31], a[196:199], a[68:71], v[28:31]\n"
"v_mfma_f32_16x16x32_bf16 v[28:31], a[200:203], a[72:75], v[28:31]\n"
"v_mfma_f32_16x16x32_bf16 v[28:31], a[204:207], a[76:79], v[28:31]\n"
"v_mfma_f32_16x16x32_bf16 v[28:31], a[208:211], a[80:83], v[28:31]\n"
"v_mfma_f32_16x16x32_bf16 v[28:31], a[212:215], a[84:87], v[28:31]\n"
"v_mfma_f32_16x16x32_bf16 v[28:31], a[216:219], a[88:91], v[28:31]\n"
"v_mfma_f32_16x16x32_bf16 v[28:31], a[220:223], a[92:95], v[28:31]\n"
"s_waitcnt lgkmcnt(0)\n"
"v_mfma_f32_16x16x32_bf16 v[32:35], a[192:195], a[96:99], 0\n"
"s_waitcnt vmcnt(9)\n"
"v_mfma_f32_16x16x32_bf16 v[32:35], a[196:199], a[100:103], v[32:35]\n"
"s_barrier\n"
"v_mfma_f32_16x16x32_bf16 v[32:35], a[200:203], a[104:107], v[32:35]\n"
"s_add_u32 s76, s26, 0x280\ns_add_u32 m0, s81, 0x10000\n"
"v_mfma_f32_16x16x32_bf16 v[32:35], a[204:207], a[108:111], v[32:35]\n"
"buffer_load_dwordx4 v199, s[60:63], s76 offen offset:0 lds\n"
"v_mfma_f32_16x16x32_bf16 v[32:35], a[208:211], a[112:115], v[32:35]\n"
"v_mfma_f32_16x16x32_bf16 v[32:35], a[212:215], a[116:119], v[32:35]\n"
"s_add_u32 s76, s26, 0x2680\ns_add_u32 m0, s81, 0x10400\n"
"v_mfma_f32_16x16x32_bf16 v[32:35], a[216:219], a[120:123], v[32:35]\n"
"buffer_load_dwordx4 v199, s[60:63], s76 offen offset:0 lds\n"
"v_mfma_f32_16x16x32_bf16 v[32:35], a[220:223], a[124:127], v[32:35]\n"
"v_mfma_f32_16x16x32_bf16 v[36:39], a[192:195], a[128:131], 0\n"
"s_add_u32 s76, s26, 0x4a80\ns_add_u32 m0, s81, 0x10800\n"
"v_mfma_f32_16x16x32_bf16 v[36:39], a[196:199], a[132:135], v[36:39]\n"
"buffer_load_dwordx4 v199, s[60:63], s76 offen offset:0 lds\n"
"v_mfma_f32_16x16x32_bf16 v[36:39], a[200:203], a[136:139], v[36:39]\n"
"v_mfma_f32_16x16x32_bf16 v[36:39], a[204:207], a[140:143], v[36:39]\n"
"s_add_u32 s76, s26, 0x6e80\ns_add_u32 m0, s81, 0x10c00\n"
"v_mfma_f32_16x16x32_bf16 v[36:39], a[208:211], a[144:147], v[36:39]\n"
"buffer_load_dwordx4 v199, s[60:63], s76 offen offset:0 lds\n"
"v_mfma_f32_16x16x32_bf16 v[36:39], a[212:215], a[148:151], v[36:39]\n"
"v_mfma_f32_16x16x32_bf16 v[36:39], a[216:219], a[152:155], v[36:39]\n"
"s_add_u32 s76, s26, 0x9280\ns_add_u32 m0, s81, 0x11000\n"
"v_mfma_f32_16x16x32_bf16 v[36:39], a[220:223], a[156:159], v[36:39]\n"
"buffer_load_dwordx4 v199, s[60:63], s76 offen offset:0 lds\n"
"v_mfma_f32_16x16x32_bf16 v[40:43], a[160:163], a[0:3], 0\n"
"ds_read_b64_tr_b16 a[192:193], v200 offset:0\n"
"v_mfma_f32_16x16x32_bf16 v[40:43], a[164:167], a[4:7], v[40:43]\n"
"s_add_u32 s76, s26, 0xb680\ns_add_u32 m0, s81, 0x11400\n"
"ds_read_b64_tr_b16 a[194:195], v200 offset:2048\n"
"v_mfma_f32_16x16x32_bf16 v[40:43], a[168:171], a[8:11], v[40:43]\n"
"ds_read_b64_tr_b16 a[196:197], v200 offset:4096\n"
"buffer_load_dwordx4 v199, s[60:63], s76 offen offset:0 lds\n"
"v_mfma_f32_16x16x32_bf16 v[40:43], a[172:175], a[12:15], v[40:43]\n"
"ds_read_b64_tr_b16 a[198:199], v200 offset:6144\n"
"v_mfma_f32_16x16x32_bf16 v[40:43], a[176:179], a[16:19], v[40:43]\n"
"s_add_u32 s76, s26, 0xda80\ns_add_u32 m0, s81, 0x11800\n"
"ds_read_b64_tr_b16 a[200:201], v200 offset:8192\n"
"v_mfma_f32_16x16x32_bf16 v[40:43], a[180:183], a[20:23], v[40:43]\n"
"ds_read_b64_tr_b16 a[202:203], v200 offset:10240\n"
"buffer_load_dwordx4 v199, s[60:63], s76 offen offset:0 lds\n"
"v_mfma_f32_16x16x32_bf16 v[40:43], a[184:187], a[24:27], v[40:43]\n"
"ds_read_b64_tr_b16 a[204:205], v200 offset:12288\n"
"v_mfma_f32_16x16x32_bf16 v[40:43], a[188:191], a[28:31], v[40:43]\n"
"s_add_u32 s76, s26, 0xfe80\ns_add_u32 m0, s81, 0x11c00\n"
"ds_read_b64_tr_b16 a[206:207], v200 offset:14336\n"
"v_mfma_f32_16x16x32_bf16 v[44:47], a[160:163], a[32:35], 0\n"
"ds_read_b64_tr_b16 a[208:209], v200 offset:16384\n"
"buffer_load_dwordx4 v199, s[60:63], s76 offen offset:0 lds\n"
"v_mfma_f32_16x16x32_bf16 v[44:47], a[164:167], a[36:39], v[44:47]\n"
"ds_read_b64_tr_b16 a[210:211], v200 offset:18432\n"
"v_mfma_f32_16x16x32_bf16 v[44:47], a[168:171], a[40:43], v[44:47]\n"
"ds_read_b64_tr_b16 a[212:213], v200 offset:20480\n"
"v_mfma_f32_16x16x32_bf16 v[44:47], a[172:175], a[44:47], v[44:47]\n"
"ds_read_b64_tr_b16 a[214:215], v200 offset:22528\n"
"v_mfma_f32_16x16x32_bf16 v[44:47], a[176:179], a[48:51], v[44:47]\n"
"ds_read_b64_tr_b16 a[216:217], v200 offset:24576\n"
"v_mfma_f32_16x16x32_bf16 v[44:47], a[180:183], a[52:55], v[44:47]\n"
"ds_read_b64_tr_b16 a[218:219], v200 offset:26624\n"
"v_mfma_f32_16x16x32_bf16 v[44:47], a[184:187], a[56:59], v[44:47]\n"
"ds_read_b64_tr_b16 a[220:221], v200 offset:28672\n"
"v_mfma_f32_16x16x32_bf16 v[44:47], a[188:191], a[60:63], v[44:47]\n"
"ds_read_b64_tr_b16 a[222:223], v200 offset:30720\n"
"v_mfma_f32_16x16x32_bf16 v[48:51], a[160:163], a[64:67], 0\n"
"s_waitcnt vmcnt(8)\n"
"v_mfma_f32_16x16x32_bf16 v[48:51], a[164:167], a[68:71], v[48:51]\n"
"v_cmp_eq_u32 vcc, s80, v212\ns_nop 4\ns_and_b32 s20, vcc_lo, 0x1\ns_cmp_eq_u32 s20, 0x1\ns_cbranch_scc0 L_sp3_%=\nL_bk3_%=:\n"
"buffer_load_dword v248, v252, s[68:71], 0 offen offset:128 sc1\n"
"v_mfma_f32_16x16x32_bf16 v[48:51], a[168:171], a[72:75], v[48:51]\n"
"s_add_u32 s79, s96, 0\n"
"buffer_load_dwordx4 a[224:227], v203, s[48:51], s79 offen offset:0 sc1\n"
"v_mfma_f32_16x16x32_bf16 v[48:51], a[172:175], a[76:79], v[48:51]\n"
"buffer_load_dwordx4 a[228:231], v203, s[48:51], s79 offen offset:1024 sc1\n"
"v_mfma_f32_16x16x32_bf16 v[48:51], a[176:179], a[80:83], v[48:51]\n"
"buffer_load_dwordx4 a[232:235], v203, s[48:51], s79 offen offset:2048 sc1\n"
"v_mfma_f32_16x16x32_bf16 v[48:51], a[180:183], a[84:87], v[48:51]\n"
"buffer_load_dwordx4 a[236:239], v203, s[48:51], s79 offen offset:3072 sc1\n"
"v_mfma_f32_16x16x32_bf16 v[48:51], a[184:187], a[88:91], v[48:51]\n"
"s_add_u32 s79, s96, 0x1000\n"
"buffer_load_dwordx4 a[240:243], v203, s[48:51], s79 offen offset:0 sc1\n"
"v_mfma_f32_16x16x32_bf16 v[48:51], a[188:191], a[92:95], v[48:51]\n"
"s_waitcnt lgkmcnt(0)\n"
"buffer_load_dwordx4 a[244:247], v203, s[48:51], s79 offen offset:1024 sc1\n"
"v_mfma_f32_16x16x32_bf16 v[52:55], a[160:163], a[96:99], 0\n"
"s_waitcnt vmcnt(15)\n"
"v_mfma_f32_16x16x32_bf16 v[52:55], a[164:167], a[100:103], v[52:55]\n"
"s_barrier\n"
"v_mfma_f32_16x16x32_bf16 v[52:55], a[168:171], a[104:107], v[52:55]\n"
"s_add_u32 s76, s26, 0x300\ns_add_u32 m0, s81, 0\n"
"v_mfma_f32_16x16x32_bf16 v[52:55], a[172:175], a[108:111], v[52:55]\n"
"buffer_load_dwordx4 v199, s[60:63], s76 offen offset:0 lds\n"
"v_mfma_f32_16x16x32_bf16 v[52:55], a[176:179], a[112:115], v[52:55]\n"
"v_mfma_f32_16x16x32_bf16 v[52:55], a[180:183], a[116:119], v[52:55]\n"
"s_add_u32 s76, s26, 0x2700\ns_add_u32 m0, s81, 0x400\n"
"v_mfma_f32_16x16x32_bf16 v[52:55], a[184:187], a[120:123], v[52:55]\n"
"buffer_load_dwordx4 v199, s[60:63], s76 offen offset:0 lds\n"
"v_mfma_f32_16x16x32_bf16 v[52:55], a[188:191], a[124:127], v[52:55]\n"
"v_mfma_f32_16x16x32_bf16 v[56:59], a[160:163], a[128:131], 0\n"
"s_add_u32 s76, s26, 0x4b00\ns_add_u32 m0, s81, 0x800\n"
"v_mfma_f32_16x16x32_bf16 v[56:59], a[164:167], a[132:135], v[56:59]\n"
"buffer_load_dwordx4 v199, s[60:63], s76 offen offset:0 lds\n"
"v_mfma_f32_16x16x32_bf16 v[56:59], a[168:171], a[136:139], v[56:59]\n"
"v_mfma_f32_16x16x32_bf16 v[56:59], a[172:175], a[140:143], v[56:59]\n"
"s_add_u32 s76, s26, 0x6f00\ns_add_u32 m0, s81, 0xc00\n"
"v_mfma_f32_16x16x32_bf16 v[56:59], a[176:179], a[144:147], v[56:59]\n"
"buffer_load_dwordx4 v199, s[60:63], s76 offen offset:0 lds\n"
"v_mfma_f32_16x16x32_bf16 v[56:59], a[180:183], a[148:151], v[56:59]\n"
"v_mfma_f32_16x16x32_bf16 v[56:59], a[184:187], a[152:155], v[56:59]\n"
"s_add_u32 s76, s26, 0x9300\ns_add_u32 m0, s81, 0x1000\n"
"v_mfma_f32_16x16x32_bf16 v[56:59], a[188:191], a[156:159], v[56:59]\n"
"buffer_load_dwordx4 v199, s[60:63], s76 offen offset:0 lds\n"
"v_mfma_f32_16x16x32_bf16 v[60:63], a[192:195], a[0:3], 0\n"
"ds_read_b64_tr_b16 a[160:161], v200 offset:32768\n"
"v_mfma_f32_16x16x32_bf16 v[60:63], a[196:199], a[4:7], v[60:63]\n"
"s_add_u32 s76, s26, 0xb700\ns_add_u32 m0, s81, 0x1400\n"
"ds_read_b64_tr_b16 a[162:163], v200 offset:34816\n"
"v_mfma_f32_16x16x32_bf16 v[60:63], a[200:203], a[8:11], v[60:63]\n"
"ds_read_b64_tr_b16 a[164:165], v200 offset:36864\n"
"buffer_load_dwordx4 v199, s[60:63], s76 offen offset:0 lds\n"
"v_mfma_f32_16x16x32_bf16 v[60:63], a[204:207], a[12:15], v[60:63]\n"
"ds_read_b64_tr_b16 a[166:167], v200 offset:38912\n"
"v_mfma_f32_16x16x32_bf16 v[60:63], a[208:211], a[16:19], v[60:63]\n"
"s_add_u32 s76, s26, 0xdb00\ns_add_u32 m0, s81, 0x1800\n"
"ds_read_b64_tr_b16 a[168:169], v200 offset:40960\n"
"v_mfma_f32_16x16x32_bf16 v[60:63], a[212:215], a[20:23], v[60:63]\n"
"ds_read_b64_tr_b16 a[170:171], v200 offset:43008\n"
"buffer_load_dwordx4 v199, s[60:63], s76 offen offset:0 lds\n"
"v_mfma_f32_16x16x32_bf16 v[60:63], a[216:219], a[24:27], v[60:63]\n"
"buffer_load_dword v212, v254, s[68:71], 0 offen offset:0 sc1\n"
"ds_read_b64_tr_b16 a[172:173], v200 offset:45056\n"
"v_mfma_f32_16x16x32_bf16 v[60:63], a[220:223], a[28:31], v[60:63]\n"
"s_add_u32 s76, s26, 0xff00\ns_add_u32 m0, s81, 0x1c00\n"
"ds_read_b64_tr_b16 a[174:175], v200 offset:47104\n"
"v_mfma_f32_16x16x32_bf16 v[64:67], a[192:195], a[32:35], 0\n"
"ds_read_b64_tr_b16 a[176:177], v200 offset:49152\n"
"buffer_load_dwordx4 v199, s[60:63], s76 offen offset:0 lds\n"
"v_mfma_f32_16x16x32_bf16 v[64:67], a[196:199], a[36:39], v[64:67]\n"
"ds_read_b64_tr_b16 a[178:179], v200 offset:51200\n"
"v_mfma_f32_16x16x32_bf16 v[64:67], a[200:203], a[40:43], v[64:67]\n"
"ds_read_b64_tr_b16 a[180:181], v200 offset:53248\n"
"v_mfma_f32_16x16x32_bf16 v[64:67], a[204:207], a[44:47], v[64:67]\n"
"ds_read_b64_tr_b16 a[182:183], v200 offset:55296\n"
"v_mfma_f32_16x16x32_bf16 v[64:67], a[208:211], a[48:51], v[64:67]\n"
"ds_read_b64_tr_b16 a[184:185], v200 offset:57344\n"
"v_mfma_f32_16x16x32_bf16 v[64:67], a[212:215], a[52:55], v[64:67]\n"
"ds_read_b64_tr_b16 a[186:187], v200 offset:59392\n"
"v_mfma_f32_16x16x32_bf16 v[64:67], a[216:219], a[56:59], v[64:67]\n"
"ds_read_b64_tr_b16 a[188:189], v200 offset:61440\n"
"v_mfma_f32_16x16x32_bf16 v[64:67], a[220:223], a[60:63], v[64:67]\n"
"ds_read_b64_tr_b16 a[190:191], v200 offset:63488\n"
"v_mfma_f32_16x16x32_bf16 v[68:71], a[192:195], a[64:67], 0\n"
"v_mfma_f32_16x16x32_bf16 v[68:71], a[196:199], a[68:71], v[68:71]\n"
"v_mfma_f32_16x16x32_bf16 v[68:71], a[200:203], a[72:75], v[68:71]\n"
"v_mfma_f32_16x16x32_bf16 v[68:71], a[204:207], a[76:79], v[68:71]\n"
"v_mfma_f32_16x16x32_bf16 v[68:71], a[208:211], a[80:83], v[68:71]\n"
"v_mfma_f32_16x16x32_bf16 v[68:71], a[212:215], a[84:87], v[68:71]\n"
"v_mfma_f32_16x16x32_bf16 v[68:71], a[216:219], a[88:91], v[68:71]\n"
"v_mfma_f32_16x16x32_bf16 v[68:71], a[220:223], a[92:95], v[68:71]\n"
"s_waitcnt lgkmcnt(0)\n"
"v_mfma_f32_16x16x32_bf16 v[72:75], a[192:195], a[96:99], 0\n"
"s_waitcnt vmcnt(16)\n"
"v_mfma_f32_16x16x32_bf16 v[72:75], a[196:199], a[100:103], v[72:75]\n"
"s_barrier\n"
"v_mfma_f32_16x16x32_bf16 v[72:75], a[200:203], a[104:107], v[72:75]\n"
"s_add_u32 s76, s26, 0x380\ns_add_u32 m0, s81, 0x8000\n"
"v_mfma_f32_16x16x32_bf16 v[72:75], a[204:207], a[108:111], v[72:75]\n"
"buffer_load_dwordx4 v199, s[60:63], s76 offen offset:0 lds\n"
"v_mfma_f32_16x16x32_bf16 v[72:75], a[208:211], a[112:115], v[72:75]\n"
"v_mfma_f32_16x16x32_bf16 v[72:75], a[212:215], a[116:119], v[72:75]\n"
"s_add_u32 s76, s26, 0x2780\ns_add_u32 m0, s81, 0x8400\n"
"v_mfma_f32_16x16x32_bf16 v[72:75], a[216:219], a[120:123], v[72:75]\n"
"buffer_load_dwordx4 v199, s[60:63], s76 offen offset:0 lds\n"
"v_mfma_f32_16x16x32_bf16 v[72:75], a[220:223], a[124:127], v[72:75]\n"
"v_mfma_f32_16x16x32_bf16 v[76:79], a[192:195], a[128:131], 0\n"
"s_add_u32 s76, s26, 0x4b80\ns_add_u32 m0, s81, 0x8800\n"
"v_mfma_f32_16x16x32_bf16 v[76:79], a[196:199], a[132:135], v[76:79]\n"
"buffer_load_dwordx4 v199, s[60:63], s76 offen offset:0 lds\n"
"v_mfma_f32_16x16x32_bf16 v[76:79], a[200:203], a[136:139], v[76:79]\n"
"v_mfma_f32_16x16x32_bf16 v[76:79], a[204:207], a[140:143], v[76:79]\n"
"s_add_u32 s76, s26, 0x6f80\ns_add_u32 m0, s81, 0x8c00\n"
"v_mfma_f32_16x16x32_bf16 v[76:79], a[208:211], a[144:147], v[76:79]\n"
"buffer_load_dwordx4 v199, s[60:63], s76 offen offset:0 lds\n"
"v_mfma_f32_16x16x32_bf16 v[76:79], a[212:215], a[148:151], v[76:79]\n"
"v_mfma_f32_16x16x32_bf16 v[76:79], a[216:219], a[152:155], v[76:79]\n"
"s_add_u32 s76, s26, 0x9380\ns_add_u32 m0, s81, 0x9000\n"
"v_mfma_f32_16x16x32_bf16 v[76:79], a[220:223], a[156:159], v[76:79]\n"
"buffer_load_dwordx4 v199, s[60:63], s76 offen offset:0 lds\n"
"v_mfma_f32_16x16x32_bf16 v[80:83], a[160:163], a[0:3], 0\n"
"ds_read_b64_tr_b16 a[192:193], v201 offset:0\n"
"v_mfma_f32_16x16x32_bf16 v[80:83], a[164:167], a[4:7], v[80:83]\n"
"s_add_u32 s76, s26, 0xb780\ns_add_u32 m0, s81, 0x9400\n"
"ds_read_b64_tr_b16 a[194:195], v201 offset:2048\n"
"v_mfma_f32_16x16x32_bf16 v[80:83], a[168:171], a[8:11], v[80:83]\n"
"ds_read_b64_tr_b16 a[196:197], v201 offset:4096\n"
"buffer_load_dwordx4 v199, s[60:63], s76 offen offset:0 lds\n"
"v_mfma_f32_16x16x32_bf16 v[80:83], a[172:175], a[12:15], v[80:83]\n"
"ds_read_b64_tr_b16 a[198:199], v201 offset:6144\n"
"v_mfma_f32_16x16x32_bf16 v[80:83], a[176:179], a[16:19], v[80:83]\n"
"s_add_u32 s76, s26, 0xdb80\ns_add_u32 m0, s81, 0x9800\n"
"ds_read_b64_tr_b16 a[200:201], v201 offset:8192\n"
"v_mfma_f32_16x16x32_bf16 v[80:83], a[180:183], a[20:23], v[80:83]\n"
"ds_read_b64_tr_b16 a[202:203], v201 offset:10240\n"
"buffer_load_dwordx4 v199, s[60:63], s76 offen offset:0 lds\n"
"v_mfma_f32_16x16x32_bf16 v[80:83], a[184:187], a[24:27], v[80:83]\n"
"ds_read_b64_tr_b16 a[204:205], v201 offset:12288\n"
"v_mfma_f32_16x16x32_bf16 v[80:83], a[188:191], a[28:31], v[80:83]\n"
"s_add_u32 s76, s26, 0xff80\ns_add_u32 m0, s81, 0x9c00\n"
"ds_read_b64_tr_b16 a[206:207], v201 offset:14336\n"
"v_mfma_f32_16x16x32_bf16 v[84:87], a[160:163], a[32:35], 0\n"
"ds_read_b64_tr_b16 a[208:209], v201 offset:16384\n"
"buffer_load_dwordx4 v199, s[60:63], s76 offen offset:0 lds\n"
"v_mfma_f32_16x16x32_bf16 v[84:87], a[164:167], a[36:39], v[84:87]\n"
"ds_read_b64_tr_b16 a[210:211], v201 offset:18432\n"
"v_mfma_f32_16x16x32_bf16 v[84:87], a[168:171], a[40:43], v[84:87]\n"
"ds_read_b64_tr_b16 a[212:213], v201 offset:20480\n"
"v_mfma_f32_16x16x32_bf16 v[84:87], a[172:175], a[44:47], v[84:87]\n"
"ds_read_b64_tr_b16 a[214:215], v201 offset:22528\n"
"v_mfma_f32_16x16x32_bf16 v[84:87], a[176:179], a[48:51], v[84:87]\n"
"s_waitcnt vmcnt(9)\n"
"ds_read_b64_tr_b16 a[216:217], v201 offset:24576\n"
"v_mfma_f32_16x16x32_bf16 v[84:87], a[180:183], a[52:55], v[84:87]\n"
"v_cmp_eq_u32 vcc, s80, v212\ns_nop 4\ns_and_b32 s20, vcc_lo, 0x2\ns_cmp_eq_u32 s20, 0x2\ns_cbranch_scc0 L_sp7_%=\nL_bk7_%=:\n"
"ds_read_b64_tr_b16 a[218:219], v201 offset:26624\n"
"v_mfma_f32_16x16x32_bf16 v[84:87], a[184:187], a[56:59], v[84:87]\n"
"s_add_u32 s79, s97, 0\n"
"buffer_load_dwordx4 a[248:251], v203, s[48:51], s79 offen offset:0 sc1\n"
"ds_read_b64_tr_b16 a[220:221], v201 offset:28672\n"
"v_mfma_f32_16x16x32_bf16 v[84:87], a[188:191], a[60:63], v[84:87]\n"
"buffer_load_dwordx4 a[252:255], v203, s[48:51], s79 offen offset:1024 sc1\n"
"ds_read_b64_tr_b16 a[222:223], v201 offset:30720\n"
"v_mfma_f32_16x16x32_bf16 v[88:91], a[160:163], a[64:67], 0\n"
"buffer_load_dwordx4 v[240:243], v203, s[48:51], s79 offen offset:2048 sc1\n"
"v_mfma_f32_16x16x32_bf16 v[88:91], a[164:167], a[68:71], v[88:91]\n"
"buffer_load_dwordx4 v[244:247], v203, s[48:51], s79 offen offset:3072 sc1\n"
"v_mfma_f32_16x16x32_bf16 v[88:91], a[168:171], a[72:75], v[88:91]\n"
"v_mfma_f32_16x16x32_bf16 v[88:91], a[172:175], a[76:79], v[88:91]\n"
"v_mfma_f32_16x16x32_bf16 v[88:91], a[176:179], a[80:83], v[88:91]\n"
"v_mfma_f32_16x16x32_bf16 v[88:91], a[180:183], a[84:87], v[88:91]\n"
"v_mfma_f32_16x16x32_bf16 v[88:91], a[184:187], a[88:91], v[88:91]\n"
"v_mfma_f32_16x16x32_bf16 v[88:91], a[188:191], a[92:95], v[88:91]\n"
"s_waitcnt lgkmcnt(0)\n"
"v_mfma_f32_16x16x32_bf16 v[92:95], a[160:163], a[96:99], 0\n"
"s_waitcnt vmcnt(12)\n"
"v_mfma_f32_16x16x32_bf16 v[92:95], a[164:167], a[100:103], v[92:95]\n"
"s_barrier\n"
"v_mfma_f32_16x16x32_bf16 v[92:95], a[168:171], a[104:107], v[92:95]\n"
"s_add_u32 s76, s26, 0x400\ns_add_u32 m0, s81, 0x10000\n"
"v_mfma_f32_16x16x32_bf16 v[92:95], a[172:175], a[108:111], v[92:95]\n"
"buffer_load_dwordx4 v199, s[60:63], s76 offen offset:0 lds\n"
"v_mfma_f32_16x16x32_bf16 v[92:95], a[176:179], a[112:115], v[92:95]\n"
"v_mfma_f32_16x16x32_bf16 v[92:95], a[180:183], a[116:119], v[92:95]\n"
"s_add_u32 s76, s26, 0x2800\ns_add_u32 m0, s81, 0x10400\n"
"v_mfma_f32_16x16x32_bf16 v[92:95], a[184:187], a[120:123], v[92:95]\n"
"s_waitcnt vmcnt(13)\n"
"buffer_load_dwordx4 v199, s[60:63], s76 offen offset:0 lds\n"
"v_mfma_f32_16x16x32_bf16 v[92:95], a[188:191], a[124:127], v[92:95]\n"
"v_cmp_ne_u32 vcc, v253, v248\ns_nop 4\ns_and_b32 s20, vcc_lo, 1\ns_cbranch_scc1 L_fx4_%=\nL_bk4_%=:\n"
"v_mfma_f32_16x16x32_bf16 v[96:99], a[160:163], a[128:131], 0\n"
"s_add_u32 s76, s26, 0x4c00\ns_add_u32 m0, s81, 0x10800\n"
"ds_write_b64 v204, a[224:225] offset:0\n"
"v_mfma_f32_16x16x32_bf16 v[96:99], a[164:167], a[132:135], v[96:99]\n"
"buffer_load_dwordx4 v199, s[60:63], s76 offen offset:0 lds\n"
"v_mfma_f32_16x16x32_bf16 v[96:99], a[168:171], a[136:139], v[96:99]\n"
"ds_write_b64 v204, a[226:227] offset:512\n"
"v_mfma_f32_16x16x32_bf16 v[96:99], a[172:175], a[140:143], v[96:99]\n"
"s_add_u32 s76, s26, 0x7000\ns_add_u32 m0, s81, 0x10c00\n"
"v_mfma_f32_16x16x32_bf16 v[96:99], a[176:179], a[144:147], v[96:99]\n"
"ds_write_b64 v204, a[228:229] offset:1024\n"
"buffer_load_dwordx4 v199, s[60:63], s76 offen offset:0 lds\n"
"v_mfma_f32_16x16x32_bf16 v[96:99], a[180:183], a[148:151], v[96:99]\n"
"v_mfma_f32_16x16x32_bf16 v[96:99], a[184:187], a[152:155], v[96:99]\n"
"s_add_u32 s76, s26, 0x9400\ns_add_u32 m0, s81, 0x11000\n"
"ds_write_b64 v204, a[230:231] offset:1536\n"
"v_mfma_f32_16x16x32_bf16 v[96:99], a[188:191], a[156:159], v[96:99]\n"
"buffer_load_dwordx4 v199, s[60:63], s76 offen offset:0 lds\n"
"v_mfma_f32_16x16x32_bf16 v[100:103], a[192:195], a[0:3], 0\n"
"ds_read_b64_tr_b16 a[160:161], v200 offset:0\n"
"v_mfma_f32_16x16x32_bf16 v[100:103], a[196:199], a[4:7], v[100:103]\n"
"s_add_u32 s76, s26, 0xb800\ns_add_u32 m0, s81, 0x11400\n"
"ds_read_b64_tr_b16 a[162:163], v200 offset:2048\n"
"v_mfma_f32_16x16x32_bf16 v[100:103], a[200:203], a[8:11], v[100:103]\n"
"ds_read_b64_tr_b16 a[164:165], v200 offset:4096\n"
"buffer_load_dwordx4 v199, s[60:63], s76 offen offset:0 lds\n"
"v_mfma_f32_16x16x32_bf16 v[100:103], a[204:207], a[12:15], v[100:103]\n"
"ds_read_b64_tr_b16 a[166:167], v200 offset:6144\n"
"v_mfma_f32_16x16x32_bf16 v[100:103], a[208:211], a[16:19], v[100:103]\n"
"s_add_u32 s76, s26, 0xdc00\ns_add_u32 m0, s81, 0x11800\n"
"ds_read_b64_tr_b16 a[168:169], v200 offset:8192\n"
"v_mfma_f32_16x16x32_bf16 v[100:103], a[212:215], a[20:23], v[100:103]\n"
"ds_read_b64_tr_b16 a[170:171], v200 offset:10240\n"
"buffer_load_dwordx4 v199, s[60:63], s76 offen offset:0 lds\n"
"v_mfma_f32_16x16x32_bf16 v[100:103], a[216:219], a[24:27], v[100:103]\n"
"ds_read_b64_tr_b16 a[172:173], v200 offset:12288\n"
"v_mfma_f32_16x16x32_bf16 v[100:103], a[220:223], a[28:31], v[100:103]\n"
"s_add_u32 s76, s26, 0x10000\ns_add_u32 m0, s81, 0x11c00\n"
"ds_read_b64_tr_b16 a[174:175], v200 offset:14336\n"
"v_mfma_f32_16x16x32_bf16 v[104:107], a[192:195], a[32:35], 0\n"
"ds_read_b64_tr_b16 a[176:177], v200 offset:16384\n"
"buffer_load_dwordx4 v199, s[60:63], s76 offen offset:0 lds\n"
"v_mfma_f32_16x16x32_bf16 v[104:107], a[196:199], a[36:39], v[104:107]\n"
"ds_read_b64_tr_b16 a[178:179], v200 offset:18432\n"
"v_mfma_f32_16x16x32_bf16 v[104:107], a[200:203], a[40:43], v[104:107]\n"
"ds_read_b64_tr_b16 a[180:181], v200 offset:20480\n"
"v_mfma_f32_16x16x32_bf16 v[104:107], a[204:207], a[44:47], v[104:107]\n"
"ds_read_b64_tr_b16 a[182:183], v200 offset:22528\n"
"v_mfma_f32_16x16x32_bf16 v[104:107], a[208:211], a[48:51], v[104:107]\n"
"ds_read_b64_tr_b16 a[184:185], v200 offset:24576\n"
"v_mfma_f32_16x16x32_bf16 v[104:107], a[212:215], a[52:55], v[104:107]\n"
"ds_read_b64_tr_b16 a[186:187], v200 offset:26624\n"
"v_mfma_f32_16x16x32_bf16 v[104:107], a[216:219], a[56:59], v[104:107]\n"
"ds_read_b64_tr_b16 a[188:189], v200 offset:28672\n"
"v_mfma_f32_16x16x32_bf16 v[104:107], a[220:223], a[60:63], v[104:107]\n"
"ds_read_b64_tr_b16 a[190:191], v200 offset:30720\n"
"v_mfma_f32_16x16x32_bf16 v[108:111], a[192:195], a[64:67], 0\n"
"ds_write_b64 v204, a[232:233] offset:2048\n"
"v_mfma_f32_16x16x32_bf16 v[108:111], a[196:199], a[68:71], v[108:111]\n"
"v_mfma_f32_16x16x32_bf16 v[108:111], a[200:203], a[72:75], v[108:111]\n"
"ds_write_b64 v204, a[234:235] offset:2560\n"
"v_mfma_f32_16x16x32_bf16 v[108:111], a[204:207], a[76:79], v[108:111]\n"
"v_mfma_f32_16x16x32_bf16 v[108:111], a[208:211], a[80:83], v[108:111]\n"
"ds_write_b64 v204, a[236:237] offset:3072\n"
"v_mfma_f32_16x16x32_bf16 v[108:111], a[212:215], a[84:87], v[108:111]\n"
"v_mfma_f32_16x16x32_bf16 v[108:111], a[216:219], a[88:91], v[108:111]\n"
"ds_write_b64 v204, a[238:239] offset:3584\n"
"v_mfma_f32_16x16x32_bf16 v[108:111], a[220:223], a[92:95], v[108:111]\n"
"s_waitcnt lgkmcnt(0)\n"
"v_mfma_f32_16x16x32_bf16 v[112:115], a[192:195], a[96:99], 0\n"
"s_waitcnt vmcnt(12)\n"
"ds_write_b64 v204, a[240:241] offset:4096\n"
"v_mfma_f32_16x16x32_bf16 v[112:115], a[196:199], a[100:103], v[112:115]\n"
"s_barrier\n"
"v_mfma_f32_16x16x32_bf16 v[112:115], a[200:203], a[104:107], v[112:115]\n"
"s_add_u32 s76, s27, 0\ns_add_u32 m0, s81, 0\n"
"ds_write_b64 v204, a[242:243] offset:4608\n"
"v_mfma_f32_16x16x32_bf16 v[112:115], a[204:207], a[108:111], v[112:115]\n"
"buffer_load_dwordx4 v199, s[60:63], s76 offen offset:0 lds\n"
"v_mfma_f32_16x16x32_bf16 v[112:115], a[208:211], a[112:115], v[112:115]\n"
"ds_write_b64 v204, a[244:245] offset:5120\n"
"v_mfma_f32_16x16x32_bf16 v[112:115], a[212:215], a[116:119], v[112:115]\n"
"s_add_u32 s76, s27, 0x2400\ns_add_u32 m0, s81, 0x400\n"
"v_mfma_f32_16x16x32_bf16 v[112:115], a[216:219], a[120:123], v[112:115]\n"
"ds_write_b64 v204, a[246:247] offset:5632\n"
"buffer_load_dwordx4 v199, s[60:63], s76 offen offset:0 lds\n"
"v_mfma_f32_16x16x32_bf16 v[112:115], a[220:223], a[124:127], v[112:115]\n"
"v_mfma_f32_16x16x32_bf16 v[116:119], a[192:195], a[128:131], 0\n"
"s_add_u32 s76, s27, 0x4800\ns_add_u32 m0, s81, 0x800\n"
"v_mfma_f32_16x16x32_bf16 v[116:119], a[196:199], a[132:135], v[116:119]\n"
"buffer_load_dwordx4 v199, s[60:63], s76 offen offset:0 lds\n"
"v_mfma_f32_16x16x32_bf16 v[116:119], a[200:203], a[136:139], v[116:119]\n"
"v_mfma_f32_16x16x32_bf16 v[116:119], a[204:207], a[140:143], v[116:119]\n"
"s_add_u32 s76, s27, 0x6c00\ns_add_u32 m0, s81, 0xc00\n"
"v_mfma_f32_16x16x32_bf16 v[116:119], a[208:211], a[144:147], v[116:119]\n"
"buffer_load_dwordx4 v199, s[60:63], s76 offen offset:0 lds\n"
"v_mfma_f32_16x16x32_bf16 v[116:119], a[212:215], a[148:151], v[116:119]\n"
"v_mfma_f32_16x16x32_bf16 v[116:119], a[216:219], a[152:155], v[116:119]\n"
"s_add_u32 s76, s27, 0x9000\ns_add_u32 m0, s81, 0x1000\n"
"v_mfma_f32_16x16x32_bf16 v[116:119], a[220:223], a[156:159], v[116:119]\n"
"buffer_load_dwordx4 v199, s[60:63], s76 offen offset:0 lds\n"
"v_mfma_f32_16x16x32_bf16 v[120:123], a[160:163], a[0:3], 0\n"
"ds_read_b64_tr_b16 a[192:193], v200 offset:32768\n"
"v_mfma_f32_16x16x32_bf16 v[120:123], a[164:167], a[4:7], v[120:123]\n"
"s_add_u32 s76, s27, 0xb400\ns_add_u32 m0, s81, 0x1400\n"
"ds_read_b64_tr_b16 a[194:195], v200 offset:34816\n"
"v_mfma_f32_16x16x32_bf16 v[120:123], a[168:171], a[8:11], v[120:123]\n"
"ds_read_b64_tr_b16 a[196:197], v200 offset:36864\n"
"buffer_load_dwordx4 v199, s[60:63], s76 offen offset:0 lds\n"
"v_mfma_f32_16x16x32_bf16 v[120:123], a[172:175], a[12:15], v[120:123]\n"
"ds_read_b64_tr_b16 a[198:199], v200 offset:38912\n"
"v_mfma_f32_16x16x32_bf16 v[120:123], a[176:179], a[16:19], v[120:123]\n"
"s_add_u32 s76, s27, 0xd800\ns_add_u32 m0, s81, 0x1800\n"
"ds_read_b64_tr_b16 a[200:201], v200 offset:40960\n"
"v_mfma_f32_16x16x32_bf16 v[120:123], a[180:183], a[20:23], v[120:123]\n"
"ds_read_b64_tr_b16 a[202:203], v200 offset:43008\n"
"buffer_load_dwordx4 v199, s[60:63], s76 offen offset:0 lds\n"
"v_mfma_f32_16x16x32_bf16 v[120:123], a[184:187], a[24:27], v[120:123]\n"
"ds_read_b64_tr_b16 a[204:205], v200 offset:45056\n"
"v_mfma_f32_16x16x32_bf16 v[120:123], a[188:191], a[28:31], v[120:123]\n"
"s_add_u32 s76, s27, 0xfc00\ns_add_u32 m0, s81, 0x1c00\n"
"ds_read_b64_tr_b16 a[206:207], v200 offset:47104\n"
"v_mfma_f32_16x16x32_bf16 v[124:127], a[160:163], a[32:35], 0\n"
"ds_read_b64_tr_b16 a[208:209], v200 offset:49152\n"
"buffer_load_dwordx4 v199, s[60:63], s76 offen offset:0 lds\n"
"v_mfma_f32_16x16x32_bf16 v[124:127], a[164:167], a[36:39], v[124:127]\n"
"ds_read_b64_tr_b16 a[210:211], v200 offset:51200\n"
"v_mfma_f32_16x16x32_bf16 v[124:127], a[168:171], a[40:43], v[124:127]\n"
"s_waitcnt vmcnt(16)\n"
"ds_read_b64_tr_b16 a[212:213], v200 offset:53248\n"
"v_mfma_f32_16x16x32_bf16 v[124:127], a[172:175], a[44:47], v[124:127]\n"
"ds_read_b64_tr_b16 a[214:215], v200 offset:55296\n"
"v_mfma_f32_16x16x32_bf16 v[124:127], a[176:179], a[48:51], v[124:127]\n"
"ds_read_b64_tr_b16 a[216:217], v200 offset:57344\n"
"v_mfma_f32_16x16x32_bf16 v[124:127], a[180:183], a[52:55], v[124:127]\n"
"ds_read_b64_tr_b16 a[218:219], v200 offset:59392\n"
"v_mfma_f32_16x16x32_bf16 v[124:127], a[184:187], a[56:59], v[124:127]\n"
"ds_read_b64_tr_b16 a[220:221], v200 offset:61440\n"
"v_mfma_f32_16x16x32_bf16 v[124:127], a[188:191], a[60:63], v[124:127]\n"
"ds_read_b64_tr_b16 a[222:223], v200 offset:63488\n"
"v_mfma_f32_16x16x32_bf16 v[128:131], a[160:163], a[64:67], 0\n"
"ds_write_b64 v210, a[248:249] offset:0\n"
"v_mfma_f32_16x16x32_bf16 v[128:131], a[164:167], a[68:71], v[128:131]\n"
"v_mfma_f32_16x16x32_bf16 v[128:131], a[168:171], a[72:75], v[128:131]\n"
"ds_write_b64 v210, a[250:251] offset:512\n"
"v_mfma_f32_16x16x32_bf16 v[128:131], a[172:175], a[76:79], v[128:131]\n"
"v_mfma_f32_16x16x32_bf16 v[128:131], a[176:179], a[80:83], v[128:131]\n"
"ds_write_b64 v210, a[252:253] offset:1024\n"
"v_mfma_f32_16x16x32_bf16 v[128:131], a[180:183], a[84:87], v[128:131]\n"
"v_mfma_f32_16x16x32_bf16 v[128:131], a[184:187], a[88:91], v[128:131]\n"
"ds_write_b64 v210, a[254:255] offset:1536\n"
"v_mfma_f32_16x16x32_bf16 v[128:131], a[188:191], a[92:95], v[128:131]\n"
"s_waitcnt lgkmcnt(0)\n"
"v_mfma_f32_16x16x32_bf16 v[132:135], a[160:163], a[96:99], 0\n"
"s_waitcnt vmcnt(8)\n"
"ds_write_b64 v210, v[240:241] offset:2048\n"
"v_mfma_f32_16x16x32_bf16 v[132:135], a[164:167], a[100:103], v[132:135]\n"
"s_barrier\n"
"v_mfma_f32_16x16x32_bf16 v[132:135], a[168:171], a[104:107], v[132:135]\n"
"s_add_u32 s76, s27, 0x80\ns_add_u32 m0, s81, 0x8000\n"
"ds_write_b64 v210, v[242:243] offset:2560\n"
"v_mfma_f32_16x16x32_bf16 v[132:135], a[172:175], a[108:111], v[132:135]\n"
"buffer_load_dwordx4 v199, s[60:63], s76 offen offset:0 lds\n"
"v_mfma_f32_16x16x32_bf16 v[132:135], a[176:179], a[112:115], v[132:135]\n"
"ds_write_b64 v210, v[244:245] offset:3072\n"
"v_mfma_f32_16x16x32_bf16 v[132:135], a[180:183], a[116:119], v[132:135]\n"
"s_add_u32 s76, s27, 0x2480\ns_add_u32 m0, s81, 0x8400\n"
"v_mfma_f32_16x16x32_bf16 v[132:135], a[184:187], a[120:123], v[132:135]\n"
"ds_write_b64 v210, v[246:247] offset:3584\n"
"buffer_load_dwordx4 v199, s[60:63], s76 offen offset:0 lds\n"
"v_mfma_f32_16x16x32_bf16 v[132:135], a[188:191], a[124:127], v[132:135]\n"
"v_mfma_f32_16x16x32_bf16 v[136:139], a[160:163], a[128:131], 0\n"
"s_add_u32 s76, s27, 0x4880\ns_add_u32 m0, s81, 0x8800\n"
"v_mfma_f32_16x16x32_bf16 v[136:139], a[164:167], a[132:135], v[136:139]\n"
"buffer_load_dwordx4 v199, s[60:63], s76 offen offset:0 lds\n"
"v_mfma_f32_16x16x32_bf16 v[136:139], a[168:171], a[136:139], v[136:139]\n"
"v_mfma_f32_16x16x32_bf16 v[136:139], a[172:175], a[140:143], v[136:139]\n"
"s_add_u32 s76, s27, 0x6c80\ns_add_u32 m0, s81, 0x8c00\n"
"v_mfma_f32_16x16x32_bf16 v[136:139], a[176:179], a[144:147], v[136:139]\n"
"buffer_load_dwordx4 v199, s[60:63], s76 offen offset:0 lds\n"
"v_mfma_f32_16x16x32_bf16 v[136:139], a[180:183], a[148:151], v[136:139]\n"
"v_mfma_f32_16x16x32_bf16 v[136:139], a[184:187], a[152:155], v[136:139]\n"
"s_add_u32 s76, s27, 0x9080\ns_add_u32 m0, s81, 0x9000\n"
"v_mfma_f32_16x16x32_bf16 v[136:139], a[188:191], a[156:159], v[136:139]\n"
"buffer_load_dwordx4 v199, s[60:63], s76 offen offset:0 lds\n"
"v_mfma_f32_16x16x32_bf16 v[140:143], a[192:195], a[0:3], 0\n"
"ds_read_b64_tr_b16 a[160:161], v201 offset:0\n"
"v_mfma_f32_16x16x32_bf16 v[140:143], a[196:199], a[4:7], v[140:143]\n"
"s_add_u32 s76, s27, 0xb480\ns_add_u32 m0, s81, 0x9400\n"
"ds_read_b64_tr_b16 a[162:163], v201 offset:2048\n"
"v_mfma_f32_16x16x32_bf16 v[140:143], a[200:203], a[8:11], v[140:143]\n"
"ds_read_b64_tr_b16 a[164:165], v201 offset:4096\n"
"buffer_load_dwordx4 v199, s[60:63], s76 offen offset:0 lds\n"
"v_mfma_f32_16x16x32_bf16 v[140:143], a[204:207], a[12:15], v[140:143]\n"
"ds_read_b64_tr_b16 a[166:167], v201 offset:6144\n"
"v_mfma_f32_16x16x32_bf16 v[140:143], a[208:211], a[16:19], v[140:143]\n"
"s_add_u32 s76, s27, 0xd880\ns_add_u32 m0, s81, 0x9800\n"
"ds_read_b64_tr_b16 a[168:169], v201 offset:8192\n"
"v_mfma_f32_16x16x32_bf16 v[140:143], a[212:215], a[20:23], v[140:143]\n"
"ds_read_b64_tr_b16 a[170:171], v201 offset:10240\n"
"buffer_load_dwordx4 v199, s[60:63], s76 offen offset:0 lds\n"
"v_mfma_f32_16x16x32_bf16 v[140:143], a[216:219], a[24:27], v[140:143]\n"
"ds_read_b64_tr_b16 a[172:173], v201 offset:12288\n"
"v_mfma_f32_16x16x32_bf16 v[140:143], a[220:223], a[28:31], v[140:143]\n"
"s_add_u32 s76, s27, 0xfc80\ns_add_u32 m0, s81, 0x9c00\n"
"ds_read_b64_tr_b16 a[174:175], v201 offset:14336\n"
"v_mfma_f32_16x16x32_bf16 v[144:147], a[192:195], a[32:35], 0\n"
"ds_read_b64_tr_b16 a[176:177], v201 offset:16384\n"
"buffer_load_dwordx4 v199, s[60:63], s76 offen offset:0 lds\n"
"v_mfma_f32_16x16x32_bf16 v[144:147], a[196:199], a[36:39], v[144:147]\n"
"ds_read_b64_tr_b16 a[178:179], v201 offset:18432\n"
"v_mfma_f32_16x16x32_bf16 v[144:147], a[200:203], a[40:43], v[144:147]\n"
"ds_read_b64_tr_b16 a[180:181], v201 offset:20480\n"
"v_mfma_f32_16x16x32_bf16 v[144:147], a[204:207], a[44:47], v[144:147]\n"
"ds_read_b64_tr_b16 a[182:183], v201 offset:22528\n"
"v_mfma_f32_16x16x32_bf16 v[144:147], a[208:211], a[48:51], v[144:147]\n"
"ds_read_b64_tr_b16 a[184:185], v201 offset:24576\n"
"v_mfma_f32_16x16x32_bf16 v[144:147], a[212:215], a[52:55], v[144:147]\n"
"ds_read_b64_tr_b16 a[186:187], v201 offset:26624\n"
"v_mfma_f32_16x16x32_bf16 v[144:147], a[216:219], a[56:59], v[144:147]\n"
"ds_read_b64_tr_b16 a[188:189], v201 offset:28672\n"
"v_mfma_f32_16x16x32_bf16 v[144:147], a[220:223], a[60:63], v[144:147]\n"
"ds_read_b64_tr_b16 a[190:191], v201 offset:30720\n"
"v_mfma_f32_16x16x32_bf16 v[148:151], a[192:195], a[64:67], 0\n"
"v_mfma_f32_16x16x32_bf16 v[148:151], a[196:199], a[68:71], v[148:151]\n"
"v_mfma_f32_16x16x32_bf16 v[148:151], a[200:203], a[72:75], v[148:151]\n"
"v_mfma_f32_16x16x32_bf16 v[148:151], a[204:207], a[76:79], v[148:151]\n"
"v_mfma_f32_16x16x32_bf16 v[148:151], a[208:211], a[80:83], v[148:151]\n"
"v_mfma_f32_16x16x32_bf16 v[148:151], a[212:215], a[84:87], v[148:151]\n"
"v_mfma_f32_16x16x32_bf16 v[148:151], a[216:219], a[88:91], v[148:151]\n"
"v_mfma_f32_16x16x32_bf16 v[148:151], a[220:223], a[92:95], v[148:151]\n"
"s_waitcnt lgkmcnt(0)\n"
"v_mfma_f32_16x16x32_bf16 v[152:155], a[192:195], a[96:99], 0\n"
"s_waitcnt vmcnt(8)\n"
"v_mfma_f32_16x16x32_bf16 v[152:155], a[196:199], a[100:103], v[152:155]\n"
"s_barrier\n"
"v_mfma_f32_16x16x32_bf16 v[152:155], a[200:203], a[104:107], v[152:155]\n"
"s_add_u32 s76, s27, 0x100\ns_add_u32 m0, s81, 0x10000\n"
"ds_read_b64_tr_b16 a[224:225], v202 offset:0\n"
"v_mfma_f32_16x16x32_bf16 v[152:155], a[204:207], a[108:111], v[152:155]\n"
"ds_read_b64_tr_b16 a[226:227], v202 offset:512\n"
"buffer_load_dwordx4 v199, s[60:63], s76 offen offset:0 lds\n"
"v_mfma_f32_16x16x32_bf16 v[152:155], a[208:211], a[112:115], v[152:155]\n"
"ds_read_b64_tr_b16 a[228:229], v202 offset:1024\n"
"v_mfma_f32_16x16x32_bf16 v[152:155], a[212:215], a[116:119], v[152:155]\n"
"s_add_u32 s76, s27, 0x2500\ns_add_u32 m0, s81, 0x10400\n"
"ds_read_b64_tr_b16 a[230:231], v202 offset:1536\n"
"v_mfma_f32_16x16x32_bf16 v[152:155], a[216:219], a[120:123], v[152:155]\n"
"ds_read_b64_tr_b16 a[232:233], v202 offset:2048\n"
"buffer_load_dwordx4 v199, s[60:63], s76 offen offset:0 lds\n"
"v_mfma_f32_16x16x32_bf16 v[152:155], a[220:223], a[124:127], v[152:155]\n"
"ds_read_b64_tr_b16 a[234:235], v202 offset:2560\n"
"v_mfma_f32_16x16x32_bf16 v[156:159], a[192:195], a[128:131], 0\n"
"s_add_u32 s76, s27, 0x4900\ns_add_u32 m0, s81, 0x10800\n"
"ds_read_b64_tr_b16 a[236:237], v202 offset:3072\n"
"v_mfma_f32_16x16x32_bf16 v[156:159], a[196:199], a[132:135], v[156:159]\n"
"ds_read_b64_tr_b16 a[238:239], v202 offset:3584\n"
"buffer_load_dwordx4 v199, s[60:63], s76 offen offset:0 lds\n"
"v_mfma_f32_16x16x32_bf16 v[156:159], a[200:203], a[136:139], v[156:159]\n"
"ds_read_b64_tr_b16 a[240:241], v202 offset:20480\n"
"v_mfma_f32_16x16x32_bf16 v[156:159], a[204:207], a[140:143], v[156:159]\n"
"s_add_u32 s76, s27, 0x6d00\ns_add_u32 m0, s81, 0x10c00\n"
"ds_read_b64_tr_b16 a[242:243], v202 offset:20992\n"
"v_mfma_f32_16x16x32_bf16 v[156:159], a[208:211], a[144:147], v[156:159]\n"
"ds_read_b64_tr_b16 a[244:245], v202 offset:21504\n"
"buffer_load_dwordx4 v199, s[60:63], s76 offen offset:0 lds\n"
"v_mfma_f32_16x16x32_bf16 v[156:159], a[212:215], a[148:151], v[156:159]\n"
"ds_read_b64_tr_b16 a[246:247], v202 offset:22016\n"
"v_mfma_f32_16x16x32_bf16 v[156:159], a[216:219], a[152:155], v[156:159]\n"
"s_add_u32 s76, s27, 0x9100\ns_add_u32 m0, s81, 0x11000\n"
"ds_read_b64_tr_b16 a[248:249], v202 offset:22528\n"
"v_mfma_f32_16x16x32_bf16 v[156:159], a[220:223], a[156:159], v[156:159]\n"
"ds_read_b64_tr_b16 a[250:251], v202 offset:23040\n"
"buffer_load_dwordx4 v199, s[60:63], s76 offen offset:0 lds\n"
"v_mfma_f32_16x16x32_bf16 v[160:163], a[160:163], a[0:3], 0\n"
"ds_read_b64_tr_b16 a[192:193], v200 offset:0\n"
"ds_read_b64_tr_b16 a[194:195], v200 offset:2048\n"
"v_mfma_f32_16x16x32_bf16 v[160:163], a[164:167], a[4:7], v[160:163]\n"
"s_add_u32 s76, s27, 0xb500\ns_add_u32 m0, s81, 0x11400\n"
"ds_read_b64_tr_b16 a[196:197], v200 offset:4096\n"
"ds_read_b64_tr_b16 a[198:199], v200 offset:6144\n"
"v_mfma_f32_16x16x32_bf16 v[160:163], a[168:171], a[8:11], v[160:163]\n"
"ds_read_b64_tr_b16 a[200:201], v200 offset:8192\n"
"ds_read_b64_tr_b16 a[202:203], v200 offset:10240\n"
"v_mfma_f32_16x16x32_bf16 v[160:163], a[172:175], a[12:15], v[160:163]\n"
"ds_read_b64_tr_b16 a[204:205], v200 offset:12288\n"
"ds_read_b64_tr_b16 a[206:207], v200 offset:14336\n"
"v_mfma_f32_16x16x32_bf16 v[160:163], a[176:179], a[16:19], v[160:163]\n"
"ds_read_b64_tr_b16 a[208:209], v200 offset:16384\n"
"ds_read_b64_tr_b16 a[210:211], v200 offset:18432\n"
"v_mfma_f32_16x16x32_bf16 v[160:163], a[180:183], a[20:23], v[160:163]\n"
"ds_read_b64_tr_b16 a[212:213], v200 offset:20480\n"
"ds_read_b64_tr_b16 a[214:215], v200 offset:22528\n"
"v_mfma_f32_16x16x32_bf16 v[160:163], a[184:187], a[24:27], v[160:163]\n"
"buffer_load_dwordx4 v199, s[60:63], s76 offen offset:0 lds\n"
"ds_read_b64_tr_b16 a[216:217], v200 offset:24576\n"
"ds_read_b64_tr_b16 a[218:219], v200 offset:26624\n"
"v_mfma_f32_16x16x32_bf16 v[160:163], a[188:191], a[28:31], v[160:163]\n"
"s_add_u32 s76, s27, 0xd900\ns_add_u32 m0, s81, 0x11800\n"
"ds_read_b64_tr_b16 a[220:221], v200 offset:28672\n"
"ds_read_b64_tr_b16 a[222:223], v200 offset:30720\n"
"v_mfma_f32_16x16x32_bf16 v[164:167], a[160:163], a[32:35], 0\n"
"ds_read_b64_tr_b16 a[252:253], v202 offset:23552\n"
"ds_read_b64_tr_b16 a[254:255], v202 offset:24064\n"
"v_mfma_f32_16x16x32_bf16 v[164:167], a[164:167], a[36:39], v[164:167]\n"
"buffer_load_dwordx4 v199, s[60:63], s76 offen offset:0 lds\n"
"ds_read_b64_tr_b16 a[0:1], v202 offset:4096\n"
"ds_read_b64_tr_b16 a[2:3], v202 offset:4608\n"
"v_mfma_f32_16x16x32_bf16 v[164:167], a[168:171], a[40:43], v[164:167]\n"
"s_add_u32 s76, s27, 0xfd00\ns_add_u32 m0, s81, 0x11c00\n"
"ds_read_b64_tr_b16 a[4:5], v202 offset:5120\n"
"ds_read_b64_tr_b16 a[6:7], v202 offset:5632\n"
"v_mfma_f32_16x16x32_bf16 v[164:167], a[172:175], a[44:47], v[164:167]\n"
"ds_read_b64_tr_b16 a[8:9], v202 offset:6144\n"
"ds_read_b64_tr_b16 a[10:11], v202 offset:6656\n"
"v_mfma_f32_16x16x32_bf16 v[164:167], a[176:179], a[48:51], v[164:167]\n"
"buffer_load_dwordx4 v199, s[60:63], s76 offen offset:0 lds\n"
"ds_read_b64_tr_b16 a[12:13], v202 offset:7168\n"
"ds_read_b64_tr_b16 a[14:15], v202 offset:7680\n"
"v_mfma_f32_16x16x32_bf16 v[164:167], a[180:183], a[52:55], v[164:167]\n"
"ds_read_b64_tr_b16 a[16:17], v202 offset:24576\n"
"ds_read_b64_tr_b16 a[18:19], v202 offset:25088\n"
"v_mfma_f32_16x16x32_bf16 v[164:167], a[184:187], a[56:59], v[164:167]\n"
"ds_read_b64_tr_b16 a[20:21], v202 offset:25600\n"
"ds_read_b64_tr_b16 a[22:23], v202 offset:26112\n"
"v_mfma_f32_16x16x32_bf16 v[164:167], a[188:191], a[60:63], v[164:167]\n"
"ds_read_b64_tr_b16 a[24:25], v202 offset:26624\n"
"ds_read_b64_tr_b16 a[26:27], v202 offset:27136\n"
"v_mfma_f32_16x16x32_bf16 v[168:171], a[160:163], a[64:67], 0\n"
"ds_read_b64_tr_b16 a[28:29], v202 offset:27648\n"
"ds_read_b64_tr_b16 a[30:31], v202 offset:28160\n"
"v_mfma_f32_16x16x32_bf16 v[168:171], a[164:167], a[68:71], v[168:171]\n"
"v_mfma_f32_16x16x32_bf16 v[168:171], a[168:171], a[72:75], v[168:171]\n"
"v_mfma_f32_16x16x32_bf16 v[168:171], a[172:175], a[76:79], v[168:171]\n"
"v_mfma_f32_16x16x32_bf16 v[168:171], a[176:179], a[80:83], v[168:171]\n"
"v_mfma_f32_16x16x32_bf16 v[168:171], a[180:183], a[84:87], v[168:171]\n"
"v_mfma_f32_16x16x32_bf16 v[168:171], a[184:187], a[88:91], v[168:171]\n"
"v_mfma_f32_16x16x32_bf16 v[168:171], a[188:191], a[92:95], v[168:171]\n"
"s_waitcnt lgkmcnt(0)\n"
"v_mfma_f32_16x16x32_bf16 v[172:175], a[160:163], a[96:99], 0\n"
"s_waitcnt vmcnt(8)\n"
"v_mfma_f32_16x16x32_bf16 v[172:175], a[164:167], a[100:103], v[172:175]\n"
"s_barrier\n"
"v_mfma_f32_16x16x32_bf16 v[172:175], a[168:171], a[104:107], v[172:175]\n"
"s_add_u32 s76, s27, 0x180\ns_add_u32 m0, s81, 0\n"
"ds_read_b64_tr_b16 a[32:33], v202 offset:8192\n"
"ds_read_b64_tr_b16 a[34:35], v202 offset:8704\n"
"v_mfma_f32_16x16x32_bf16 v[172:175], a[172:175], a[108:111], v[172:175]\n"
"ds_read_b64_tr_b16 a[36:37], v202 offset:9216\n"
"ds_read_b64_tr_b16 a[38:39], v202 offset:9728\n"
"v_mfma_f32_16x16x32_bf16 v[172:175], a[176:179], a[112:115], v[172:175]\n"
"ds_read_b64_tr_b16 a[40:41], v202 offset:10240\n"
"ds_read_b64_tr_b16 a[42:43], v202 offset:10752\n"
"v_mfma_f32_16x16x32_bf16 v[172:175], a[180:183], a[116:119], v[172:175]\n"
"ds_read_b64_tr_b16 a[44:45], v202 offset:11264\n"
"ds_read_b64_tr_b16 a[46:47], v202 offset:11776\n"
"v_mfma_f32_16x16x32_bf16 v[172:175], a[184:187], a[120:123], v[172:175]\n"
"ds_read_b64_tr_b16 a[48:49], v202 offset:28672\n"
"ds_read_b64_tr_b16 a[50:51], v202 offset:29184\n"
"v_mfma_f32_16x16x32_bf16 v[172:175], a[188:191], a[124:127], v[172:175]\n"
"buffer_load_dwordx4 v199, s[60:63], s76 offen offset:0 lds\n"
"ds_read_b64_tr_b16 a[52:53], v202 offset:29696\n"
"ds_read_b64_tr_b16 a[54:55], v202 offset:30208\n"
"v_mfma_f32_16x16x32_bf16 v[176:179], a[160:163], a[128:131], 0\n"
"s_add_u32 s76, s27, 0x2580\ns_add_u32 m0, s81, 0x400\n"
"ds_read_b64_tr_b16 a[56:57], v202 offset:30720\n"
"ds_read_b64_tr_b16 a[58:59], v202 offset:31232\n"
"v_mfma_f32_16x16x32_bf16 v[176:179], a[164:167], a[132:135], v[176:179]\n"
"ds_read_b64_tr_b16 a[60:61], v202 offset:31744\n"
"ds_read_b64_tr_b16 a[62:63], v202 offset:32256\n"
"v_mfma_f32_16x16x32_bf16 v[176:179], a[168:171], a[136:139], v[176:179]\n"
"buffer_load_dwordx4 v199, s[60:63], s76 offen offset:0 lds\n"
"ds_read_b64_tr_b16 a[64:65], v202 offset:12288\n"
"ds_read_b64_tr_b16 a[66:67], v202 offset:12800\n"
"v_mfma_f32_16x16x32_bf16 v[176:179], a[172:175], a[140:143], v[176:179]\n"
"s_add_u32 s76, s27, 0x4980\ns_add_u32 m0, s81, 0x800\n"
"ds_read_b64_tr_b16 a[68:69], v202 offset:13312\n"
"ds_read_b64_tr_b16 a[70:71], v202 offset:13824\n"
"v_mfma_f32_16x16x32_bf16 v[176:179], a[176:179], a[144:147], v[176:179]\n"
"ds_read_b64_tr_b16 a[72:73], v202 offset:14336\n"
"ds_read_b64_tr_b16 a[74:75], v202 offset:14848\n"
"v_mfma_f32_16x16x32_bf16 v[176:179], a[180:183], a[148:151], v[176:179]\n"
"buffer_load_dwordx4 v199, s[60:63], s76 offen offset:0 lds\n"
"ds_read_b64_tr_b16 a[76:77], v202 offset:15360\n"
"ds_read_b64_tr_b16 a[78:79], v202 offset:15872\n"
"v_mfma_f32_16x16x32_bf16 v[176:179], a[184:187], a[152:155], v[176:179]\n"
"s_add_u32 s76, s27, 0x6d80\ns_add_u32 m0, s81, 0xc00\n"
"ds_read_b64_tr_b16 a[80:81], v202 offset:32768\n"
"ds_read_b64_tr_b16 a[82:83], v202 offset:33280\n"
"v_mfma_f32_16x16x32_bf16 v[176:179], a[188:191], a[156:159], v[176:179]\n"
"ds_read_b64_tr_b16 a[84:85], v202 offset:33792\n"
"ds_read_b64_tr_b16 a[86:87], v202 offset:34304\n"
"v_mfma_f32_16x16x32_bf16 v[0:3], a[192:195], a[224:227], v[0:3]\n"
"buffer_load_dwordx4 v199, s[60:63], s76 offen offset:0 lds\n"
"ds_read_b64_tr_b16 a[160:161], v200 offset:32768\n"
"ds_read_b64_tr_b16 a[162:163], v200 offset:34816\n"
"v_mfma_f32_16x16x32_bf16 v[0:3], a[196:199], a[228:231], v[0:3]\n"
"s_add_u32 s76, s27, 0x9180\ns_add_u32 m0, s81, 0x1000\n"
"ds_read_b64_tr_b16 a[164:165], v200 offset:36864\n"
"ds_read_b64_tr_b16 a[166:167], v200 offset:38912\n"
"v_mfma_f32_16x16x32_bf16 v[0:3], a[200:203], a[232:235], v[0:3]\n"
"ds_read_b64_tr_b16 a[168:169], v200 offset:40960\n"
"ds_read_b64_tr_b16 a[170:171], v200 offset:43008\n"
"v_mfma_f32_16x16x32_bf16 v[0:3], a[204:207], a[236:239], v[0:3]\n"
"buffer_load_dwordx4 v199, s[60:63], s76 offen offset:0 lds\n"
"ds_read_b64_tr_b16 a[172:173], v200 offset:45056\n"
"ds_read_b64_tr_b16 a[174:175], v200 offset:47104\n"
"v_mfma_f32_16x16x32_bf16 v[0:3], a[208:211], a[240:243], v[0:3]\n"
"s_add_u32 s76, s27, 0xb580\ns_add_u32 m0, s81, 0x1400\n"
"ds_read_b64_tr_b16 a[176:177], v200 offset:49152\n"
"ds_read_b64_tr_b16 a[178:179], v200 offset:51200\n"
"v_mfma_f32_16x16x32_bf16 v[0:3], a[212:215], a[244:247], v[0:3]\n"
"ds_read_b64_tr_b16 a[180:181], v200 offset:53248\n"
"ds_read_b64_tr_b16 a[182:183], v200 offset:55296\n"
"v_mfma_f32_16x16x32_bf16 v[0:3], a[216:219], a[248:251], v[0:3]\n"
"buffer_load_dwordx4 v199, s[60:63], s76 offen offset:0 lds\n"
"ds_read_b64_tr_b16 a[184:185], v200 offset:57344\n"
"ds_read_b64_tr_b16 a[186:187], v200 offset:59392\n"
"v_mfma_f32_16x16x32_bf16 v[0:3], a[220:223], a[252:255], v[0:3]\n"
"s_add_u32 s76, s27, 0xd980\ns_add_u32 m0, s81, 0x1800\n"
"ds_read_b64_tr_b16 a[188:189], v200 offset:61440\n"
"ds_read_b64_tr_b16 a[190:191], v200 offset:63488\n"
"v_mfma_f32_16x16x32_bf16 v[4:7], a[192:195], a[0:3], v[4:7]\n"
"ds_read_b64_tr_b16 a[88:89], v202 offset:34816\n"
"ds_read_b64_tr_b16 a[90:91], v202 offset:35328\n"
"v_mfma_f32_16x16x32_bf16 v[4:7], a[196:199], a[4:7], v[4:7]\n"
"buffer_load_dwordx4 v199, s[60:63], s76 offen offset:0 lds\n"
"ds_read_b64_tr_b16 a[92:93], v202 offset:35840\n"
"ds_read_b64_tr_b16 a[94:95], v202 offset:36352\n"
"v_mfma_f32_16x16x32_bf16 v[4:7], a[200:203], a[8:11], v[4:7]\n"
"s_add_u32 s76, s27, 0xfd80\ns_add_u32 m0, s81, 0x1c00\n"
"ds_read_b64_tr_b16 a[96:97], v202 offset:16384\n"
"ds_read_b64_tr_b16 a[98:99], v202 offset:16896\n"
"v_mfma_f32_16x16x32_bf16 v[4:7], a[204:207], a[12:15], v[4:7]\n"
"s_and_b32 s23, s86, 0xfff1\n"
"ds_read_b64_tr_b16 a[100:101], v202 offset:17408\n"
"ds_read_b64_tr_b16 a[102:103], v202 offset:17920\n"
"v_mfma_f32_16x16x32_bf16 v[4:7], a[208:211], a[16:19], v[4:7]\n"
"s_mul_i32 s23, s23, 0x17000\n"
"buffer_load_dwordx4 v199, s[60:63], s76 offen offset:0 lds\n"
"ds_read_b64_tr_b16 a[104:105], v202 offset:18432\n"
"ds_read_b64_tr_b16 a[106:107], v202 offset:18944\n"
"v_mfma_f32_16x16x32_bf16 v[4:7], a[212:215], a[20:23], v[4:7]\n"
"s_add_u32 s23, s23, 0xa00000\n"
"ds_read_b64_tr_b16 a[108:109], v202 offset:19456\n"
"ds_read_b64_tr_b16 a[110:111], v202 offset:19968\n"
"v_mfma_f32_16x16x32_bf16 v[4:7], a[216:219], a[24:27], v[4:7]\n"
"s_add_u32 s52, s72, s23\ns_addc_u32 s53, s73, 0\n"
"ds_read_b64_tr_b16 a[112:113], v202 offset:36864\n"
"ds_read_b64_tr_b16 a[114:115], v202 offset:37376\n"
"v_mfma_f32_16x16x32_bf16 v[4:7], a[220:223], a[28:31], v[4:7]\n"
"s_waitcnt lgkmcnt(12)\n"
"s_and_b32 s53, s53, 0xffff\n"
"ds_read_b64_tr_b16 a[116:117], v202 offset:37888\n"
"ds_read_b64_tr_b16 a[118:119], v202 offset:38400\n"
"v_cvt_pk_bf16_f32 v0, v0, v1\n"
"v_cvt_pk_bf16_f32 v1, v2, v3\n"
"v_mfma_f32_16x16x32_bf16 v[8:11], a[192:195], a[32:35], v[8:11]\n"
"s_mov_b32 s54, 0x159000\n"
"ds_read_b64_tr_b16 a[120:121], v202 offset:38912\n"
"ds_read_b64_tr_b16 a[122:123], v202 offset:39424\n"
"v_mfma_f32_16x16x32_bf16 v[8:11], a[196:199], a[36:39], v[8:11]\n"
"s_mov_b32 s55, 0x20000\n"
"v_mfma_f32_16x16x32_bf16 v[8:11], a[200:203], a[40:43], v[8:11]\n"
"s_mov_b32 s56, s82\n"
"v_mfma_f32_16x16x32_bf16 v[8:11], a[204:207], a[44:47], v[8:11]\n"
"s_and_b32 s57, s83, 0xffff\n"
"v_mfma_f32_16x16x32_bf16 v[8:11], a[208:211], a[48:51], v[8:11]\n"
"s_mov_b32 s58, 0x2d0000\n"
"v_mfma_f32_16x16x32_bf16 v[8:11], a[212:215], a[52:55], v[8:11]\n"
"s_mov_b32 s59, 0x20000\n"
"v_mfma_f32_16x16x32_bf16 v[8:11], a[216:219], a[56:59], v[8:11]\n"
"s_add_u32 s21, s87, 1\n"
"v_mfma_f32_16x16x32_bf16 v[8:11], a[220:223], a[60:63], v[8:11]\n"
"s_waitcnt lgkmcnt(0)\n"
"v_cvt_pk_bf16_f32 v2, v4, v5\n"
"v_cvt_pk_bf16_f32 v3, v6, v7\n"
"v_mfma_f32_16x16x32_bf16 v[12:15], a[192:195], a[64:67], v[12:15]\n"
"s_waitcnt vmcnt(8)\n"
"s_cmp_eq_u32 s87, 0\ns_cselect_b32 s66, 0, 94208\ns_add_u32 s79, s37, 0\n"
"v_mfma_f32_16x16x32_bf16 v[12:15], a[196:199], a[68:71], v[12:15]\n"
"s_barrier\n"
"buffer_store_dwordx4 v[0:3], v203, s[64:67], s79 offen offset:0 sc1\n"
"v_mfma_f32_16x16x32_bf16 v[12:15], a[200:203], a[72:75], v[12:15]\n"
"s_add_u32 s76, s27, 0x200\ns_add_u32 m0, s81, 0x8000\n"
"ds_read_b64_tr_b16 a[124:125], v202 offset:39936\n"
"ds_read_b64_tr_b16 a[126:127], v202 offset:40448\n"
"v_mfma_f32_16x16x32_bf16 v[12:15], a[204:207], a[76:79], v[12:15]\n"
"s_and_b32 s21, s21, 7\n"
"buffer_load_dwordx4 v199, s[60:63], s76 offen offset:0 lds\n"
"v_mfma_f32_16x16x32_bf16 v[12:15], a[208:211], a[80:83], v[12:15]\n"
"s_mul_i32 s92, s21, 0x2e000\n"
"v_mfma_f32_16x16x32_bf16 v[12:15], a[212:215], a[84:87], v[12:15]\n"
"s_add_u32 s76, s27, 0x2600\ns_add_u32 m0, s81, 0x8400\n"
"v_mfma_f32_16x16x32_bf16 v[12:15], a[216:219], a[88:91], v[12:15]\n"
"s_add_u32 s21, s87, 2\n"
"buffer_load_dwordx4 v199, s[60:63], s76 offen offset:0 lds\n"
"v_mfma_f32_16x16x32_bf16 v[12:15], a[220:223], a[92:95], v[12:15]\n"
"s_and_b32 s21, s21, 7\n"
"v_cvt_pk_bf16_f32 v8, v8, v9\n"
"v_cvt_pk_bf16_f32 v9, v10, v11\n"
"v_mfma_f32_16x16x32_bf16 v[16:19], a[192:195], a[96:99], v[16:19]\n"
"s_add_u32 s76, s27, 0x4a00\ns_add_u32 m0, s81, 0x8800\n"
"v_mfma_f32_16x16x32_bf16 v[16:19], a[196:199], a[100:103], v[16:19]\n"
"s_mul_i32 s93, s21, 0x2e000\n"
"buffer_load_dwordx4 v199, s[60:63], s76 offen offset:0 lds\n"
"v_mfma_f32_16x16x32_bf16 v[16:19], a[200:203], a[104:107], v[16:19]\n"
"s_add_u32 s21, s87, 3\n"
"v_mfma_f32_16x16x32_bf16 v[16:19], a[204:207], a[108:111], v[16:19]\n"
"s_add_u32 s76, s27, 0x6e00\ns_add_u32 m0, s81, 0x8c00\n"
"v_mfma_f32_16x16x32_bf16 v[16:19], a[208:211], a[112:115], v[16:19]\n"
"s_and_b32 s21, s21, 7\n"
"buffer_load_dwordx4 v199, s[60:63], s76 offen offset:0 lds\n"
"v_mfma_f32_16x16x32_bf16 v[16:19], a[212:215], a[116:119], v[16:19]\n"
"s_mul_i32 s94, s21, 0x2e000\n"
"v_mfma_f32_16x16x32_bf16 v[16:19], a[216:219], a[120:123], v[16:19]\n"
"s_waitcnt lgkmcnt(0)\n"
"s_add_u32 s76, s27, 0x9200\ns_add_u32 m0, s81, 0x9000\n"
"v_mfma_f32_16x16x32_bf16 v[16:19], a[220:223], a[124:127], v[16:19]\n"
"s_add_u32 s21, s87, 4\n"
"v_cvt_pk_bf16_f32 v10, v12, v13\n"
"v_cvt_pk_bf16_f32 v11, v14, v15\n"
"v_mfma_f32_16x16x32_bf16 v[20:23], a[160:163], a[224:227], v[20:23]\n"
"s_cmp_eq_u32 s87, 1\ns_cselect_b32 s66, 0, 94208\ns_add_u32 s79, s37, 4096\n"
"ds_read_b64_tr_b16 a[192:193], v201 offset:0\n"
"buffer_load_dwordx4 v199, s[60:63], s76 offen offset:0 lds\n"
"v_mfma_f32_16x16x32_bf16 v[20:23], a[164:167], a[228:231], v[20:23]\n"
"s_add_u32 s76, s27, 0xb600\ns_add_u32 m0, s81, 0x9400\n"
"buffer_store_dwordx4 v[8:11], v203, s[64:67], s79 offen offset:0 sc1\n"
"ds_read_b64_tr_b16 a[194:195], v201 offset:2048\n"
"v_mfma_f32_16x16x32_bf16 v[20:23], a[168:171], a[232:235], v[20:23]\n"
"s_and_b32 s21, s21, 7\n"
"ds_read_b64_tr_b16 a[196:197], v201 offset:4096\n"
"buffer_load_dwordx4 v199, s[60:63], s76 offen offset:0 lds\n"
"v_mfma_f32_16x16x32_bf16 v[20:23], a[172:175], a[236:239], v[20:23]\n"
"s_mul_i32 s95, s21, 0x2e000\n"
"ds_read_b64_tr_b16 a[198:199], v201 offset:6144\n"
"v_mfma_f32_16x16x32_bf16 v[20:23], a[176:179], a[240:243], v[20:23]\n"
"s_add_u32 s76, s27, 0xda00\ns_add_u32 m0, s81, 0x9800\n"
"ds_read_b64_tr_b16 a[200:201], v201 offset:8192\n"
"v_mfma_f32_16x16x32_bf16 v[20:23], a[180:183], a[244:247], v[20:23]\n"
"s_add_u32 s21, s87, 5\n"
"ds_read_b64_tr_b16 a[202:203], v201 offset:10240\n"
"buffer_load_dwordx4 v199, s[60:63], s76 offen offset:0 lds\n"
"v_mfma_f32_16x16x32_bf16 v[20:23], a[184:187], a[248:251], v[20:23]\n"
"s_and_b32 s21, s21, 7\n"
"ds_read_b64_tr_b16 a[204:205], v201 offset:12288\n"
"v_mfma_f32_16x16x32_bf16 v[20:23], a[188:191], a[252:255], v[20:23]\n"
"s_add_u32 s76, s27, 0xfe00\ns_add_u32 m0, s81, 0x9c00\n"
"ds_read_b64_tr_b16 a[206:207], v201 offset:14336\n"
"v_cvt_pk_bf16_f32 v16, v16, v17\n"
"v_cvt_pk_bf16_f32 v17, v18, v19\n"
"v_mfma_f32_16x16x32_bf16 v[24:27], a[160:163], a[0:3], v[24:27]\n"
"s_mul_i32 s96, s21, 0x2e000\n"
"ds_read_b64_tr_b16 a[208:209], v201 offset:16384\n"
"buffer_load_dwordx4 v199, s[60:63], s76 offen offset:0 lds\n"
"v_mfma_f32_16x16x32_bf16 v[24:27], a[164:167], a[4:7], v[24:27]\n"
"s_add_u32 s21, s87, 6\n"
"ds_read_b64_tr_b16 a[210:211], v201 offset:18432\n"
"v_mfma_f32_16x16x32_bf16 v[24:27], a[168:171], a[8:11], v[24:27]\n"
"s_and_b32 s21, s21, 7\n"
"ds_read_b64_tr_b16 a[212:213], v201 offset:20480\n"
"v_mfma_f32_16x16x32_bf16 v[24:27], a[172:175], a[12:15], v[24:27]\n"
"s_mul_i32 s97, s21, 0x2e000\n"
"ds_read_b64_tr_b16 a[214:215], v201 offset:22528\n"
"v_mfma_f32_16x16x32_bf16 v[24:27], a[176:179], a[16:19], v[24:27]\n"
"s_add_u32 s21, s87, 7\n"
"ds_read_b64_tr_b16 a[216:217], v201 offset:24576\n"
"v_mfma_f32_16x16x32_bf16 v[24:27], a[180:183], a[20:23], v[24:27]\n"
"s_and_b32 s21, s21, 7\n"
"ds_read_b64_tr_b16 a[218:219], v201 offset:26624\n"
"v_mfma_f32_16x16x32_bf16 v[24:27], a[184:187], a[24:27], v[24:27]\n"
"s_mul_i32 s98, s21, 0x2e000\n"
"ds_read_b64_tr_b16 a[220:221], v201 offset:28672\n"
"v_mfma_f32_16x16x32_bf16 v[24:27], a[188:191], a[28:31], v[24:27]\n"
"s_lshl_b32 s21, s43, 1\n"
"ds_read_b64_tr_b16 a[222:223], v201 offset:30720\n"
"v_cvt_pk_bf16_f32 v20, v20, v21\n"
"v_cvt_pk_bf16_f32 v21, v22, v23\n"
"v_mfma_f32_16x16x32_bf16 v[28:31], a[160:163], a[32:35], v[28:31]\n"
"s_add_u32 s21, s21, s40\n"
"v_mfma_f32_16x16x32_bf16 v[28:31], a[164:167], a[36:39], v[28:31]\n"
"s_mul_i32 s25, s21, 0x2d000\n"
"v_mfma_f32_16x16x32_bf16 v[28:31], a[168:171], a[40:43], v[28:31]\n"
"s_mul_i32 s21, s41, 0x480\n"
"v_mfma_f32_16x16x32_bf16 v[28:31], a[172:175], a[44:47], v[28:31]\n"
"s_add_u32 s25, s25, s21\n"
"v_mfma_f32_16x16x32_bf16 v[28:31], a[176:179], a[48:51], v[28:31]\n"
"s_lshl_b32 s21, s19, 5\n"
"v_mfma_f32_16x16x32_bf16 v[28:31], a[180:183], a[52:55], v[28:31]\n"
"s_add_u32 s25, s25, s21\n"
"v_mfma_f32_16x16x32_bf16 v[28:31], a[184:187], a[56:59], v[28:31]\n"
"s_mov_b32 s22, 0x21012000\n"
"v_mfma_f32_16x16x32_bf16 v[28:31], a[188:191], a[60:63], v[28:31]\n"
"s_waitcnt lgkmcnt(0)\n"
"v_cvt_pk_bf16_f32 v22, v24, v25\n"
"v_cvt_pk_bf16_f32 v23, v26, v27\n"
"v_mfma_f32_16x16x32_bf16 v[32:35], a[160:163], a[64:67], v[32:35]\n"
"s_waitcnt vmcnt(10)\n"
"s_cmp_eq_u32 s87, 2\ns_cselect_b32 s66, 0, 94208\ns_add_u32 s79, s37, 8192\n"
"v_mfma_f32_16x16x32_bf16 v[32:35], a[164:167], a[68:71], v[32:35]\n"
"s_barrier\n"
"buffer_store_dwordx4 v[20:23], v203, s[64:67], s79 offen offset:0 sc1\n"
"v_mfma_f32_16x16x32_bf16 v[32:35], a[168:171], a[72:75], v[32:35]\n"
"s_add_u32 s76, s27, 0x280\ns_add_u32 m0, s81, 0x10000\n"
"v_mfma_f32_16x16x32_bf16 v[32:35], a[172:175], a[76:79], v[32:35]\n"
"s_mov_b32 s23, 0x3220240\n"
"buffer_load_dwordx4 v199, s[60:63], s76 offen offset:0 lds\n"
"v_mfma_f32_16x16x32_bf16 v[32:35], a[176:179], a[80:83], v[32:35]\n"
"s_lshl_b32 s21, s87, 3\n"
"v_mfma_f32_16x16x32_bf16 v[32:35], a[180:183], a[84:87], v[32:35]\n"
"s_add_u32 s76, s27, 0x2680\ns_add_u32 m0, s81, 0x10400\n"
"v_mfma_f32_16x16x32_bf16 v[32:35], a[184:187], a[88:91], v[32:35]\n"
"s_lshr_b64 s[22:23], s[22:23], s21\n"
"buffer_load_dwordx4 v199, s[60:63], s76 offen offset:0 lds\n"
"v_mfma_f32_16x16x32_bf16 v[32:35], a[188:191], a[92:95], v[32:35]\n"
"s_bfe_u32 s21, s22, 0x40004\n"
"v_cvt_pk_bf16_f32 v28, v28, v29\n"
"v_cvt_pk_bf16_f32 v29, v30, v31\n"
"v_mfma_f32_16x16x32_bf16 v[36:39], a[160:163], a[96:99], v[36:39]\n"
"s_add_u32 s76, s27, 0x4a80\ns_add_u32 m0, s81, 0x10800\n"
"v_mfma_f32_16x16x32_bf16 v[36:39], a[164:167], a[100:103], v[36:39]\n"
"s_and_b32 s24, s22, 15\n"
"buffer_load_dwordx4 v199, s[60:63], s76 offen offset:0 lds\n"
"v_mfma_f32_16x16x32_bf16 v[36:39], a[168:171], a[104:107], v[36:39]\n"
"s_mul_i32 s22, s24, 20\n"
"v_mfma_f32_16x16x32_bf16 v[36:39], a[172:175], a[108:111], v[36:39]\n"
"s_add_u32 s76, s27, 0x6e80\ns_add_u32 m0, s81, 0x10c00\n"
"v_mfma_f32_16x16x32_bf16 v[36:39], a[176:179], a[112:115], v[36:39]\n"
"s_lshl_b32 s23, s21, 2\n"
"buffer_load_dwordx4 v199, s[60:63], s76 offen offset:0 lds\n"
"v_mfma_f32_16x16x32_bf16 v[36:39], a[180:183], a[116:119], v[36:39]\n"
"s_add_u32 s89, s22, s23\n"
"v_mfma_f32_16x16x32_bf16 v[36:39], a[184:187], a[120:123], v[36:39]\n"
"s_add_u32 s76, s27, 0x9280\ns_add_u32 m0, s81, 0x11000\n"
"v_mfma_f32_16x16x32_bf16 v[36:39], a[188:191], a[124:127], v[36:39]\n"
"s_mul_i32 s22, s21, 0x9000\n"
"v_cvt_pk_bf16_f32 v30, v32, v33\n"
"v_cvt_pk_bf16_f32 v31, v34, v35\n"
"v_mfma_f32_16x16x32_bf16 v[40:43], a[192:195], a[224:227], v[40:43]\n"
"s_cmp_eq_u32 s87, 3\ns_cselect_b32 s66, 0, 94208\ns_add_u32 s79, s37, 12288\n"
"ds_read_b64_tr_b16 a[160:161], v200 offset:0\n"
"buffer_load_dwordx4 v199, s[60:63], s76 offen offset:0 lds\n"
"v_mfma_f32_16x16x32_bf16 v[40:43], a[196:199], a[228:231], v[40:43]\n"
"s_add_u32 s76, s27, 0xb680\ns_add_u32 m0, s81, 0x11400\n"
"buffer_store_dwordx4 v[28:31], v203, s[64:67], s79 offen offset:0 sc1\n"
"ds_read_b64_tr_b16 a[162:163], v200 offset:2048\n"
"v_mfma_f32_16x16x32_bf16 v[40:43], a[200:203], a[232:235], v[40:43]\n"
"s_lshl_b32 s23, s24, 7\n"
"ds_read_b64_tr_b16 a[164:165], v200 offset:4096\n"
"buffer_load_dwordx4 v199, s[60:63], s76 offen offset:0 lds\n"
"v_mfma_f32_16x16x32_bf16 v[40:43], a[204:207], a[236:239], v[40:43]\n"
"s_add_u32 s22, s22, s23\n"
"ds_read_b64_tr_b16 a[166:167], v200 offset:6144\n"
"v_mfma_f32_16x16x32_bf16 v[40:43], a[208:211], a[240:243], v[40:43]\n"
"s_add_u32 s76, s27, 0xda80\ns_add_u32 m0, s81, 0x11800\n"
"ds_read_b64_tr_b16 a[168:169], v200 offset:8192\n"
"v_mfma_f32_16x16x32_bf16 v[40:43], a[212:215], a[244:247], v[40:43]\n"
"s_add_u32 s31, s22, s25\n"
"ds_read_b64_tr_b16 a[170:171], v200 offset:10240\n"
"buffer_load_dwordx4 v199, s[60:63], s76 offen offset:0 lds\n"
"v_mfma_f32_16x16x32_bf16 v[40:43], a[216:219], a[248:251], v[40:43]\n"
"s_cmp_eq_u32 s21, 4\ns_cselect_b32 s22, 1, 288\n"
"ds_read_b64_tr_b16 a[172:173], v200 offset:12288\n"
"v_mfma_f32_16x16x32_bf16 v[40:43], a[220:223], a[252:255], v[40:43]\n"
"s_add_u32 s76, s27, 0xfe80\ns_add_u32 m0, s81, 0x11c00\n"
"ds_read_b64_tr_b16 a[174:175], v200 offset:14336\n"
"v_cvt_pk_bf16_f32 v18, v36, v37\n"
"v_cvt_pk_bf16_f32 v19, v38, v39\n"
"v_mfma_f32_16x16x32_bf16 v[44:47], a[192:195], a[0:3], v[44:47]\n"
"s_cmp_eq_u32 s87, 4\ns_cselect_b32 s66, 0, 94208\ns_add_u32 s79, s37, 16384\n"
"ds_read_b64_tr_b16 a[176:177], v200 offset:16384\n"
"buffer_load_dwordx4 v199, s[60:63], s76 offen offset:0 lds\n"
"v_mfma_f32_16x16x32_bf16 v[44:47], a[196:199], a[4:7], v[44:47]\n"
"s_lshl_b32 s22, s22, 7\n"
"buffer_store_dwordx4 v[16:19], v203, s[64:67], s79 offen offset:0 sc1\n"
"ds_read_b64_tr_b16 a[178:179], v200 offset:18432\n"
"v_mfma_f32_16x16x32_bf16 v[44:47], a[200:203], a[8:11], v[44:47]\n"
"s_add_u32 s34, s31, s22\n"
"ds_read_b64_tr_b16 a[180:181], v200 offset:20480\n"
"v_mfma_f32_16x16x32_bf16 v[44:47], a[204:207], a[12:15], v[44:47]\n"
"s_add_u32 s28, s87, 0\n"
"ds_read_b64_tr_b16 a[182:183], v200 offset:22528\n"
"v_mfma_f32_16x16x32_bf16 v[44:47], a[208:211], a[16:19], v[44:47]\n"
"s_lshl_b32 s28, s28, 12\n"
"ds_read_b64_tr_b16 a[184:185], v200 offset:24576\n"
"v_mfma_f32_16x16x32_bf16 v[44:47], a[212:215], a[20:23], v[44:47]\n"
"s_add_u32 s28, s28, s37\n"
"ds_read_b64_tr_b16 a[186:187], v200 offset:26624\n"
"v_mfma_f32_16x16x32_bf16 v[44:47], a[216:219], a[24:27], v[44:47]\n"
"s_mov_b32 s22, 0x24044223\n"
"ds_read_b64_tr_b16 a[188:189], v200 offset:28672\n"
"v_mfma_f32_16x16x32_bf16 v[44:47], a[220:223], a[28:31], v[44:47]\n"
"s_mov_b32 s23, 0x6442505\n"
"ds_read_b64_tr_b16 a[190:191], v200 offset:30720\n"
"v_cvt_pk_bf16_f32 v40, v40, v41\n"
"v_cvt_pk_bf16_f32 v41, v42, v43\n"
"v_mfma_f32_16x16x32_bf16 v[48:51], a[192:195], a[32:35], v[48:51]\n"
"s_lshl_b32 s21, s87, 3\n"
"v_mfma_f32_16x16x32_bf16 v[48:51], a[196:199], a[36:39], v[48:51]\n"
"s_lshr_b64 s[22:23], s[22:23], s21\n"
"v_mfma_f32_16x16x32_bf16 v[48:51], a[200:203], a[40:43], v[48:51]\n"
"s_bfe_u32 s21, s22, 0x40004\n"
"v_mfma_f32_16x16x32_bf16 v[48:51], a[204:207], a[44:47], v[48:51]\n"
"s_and_b32 s24, s22, 15\n"
"v_mfma_f32_16x16x32_bf16 v[48:51], a[208:211], a[48:51], v[48:51]\n"
"s_mul_i32 s22, s24, 20\n"
"v_mfma_f32_16x16x32_bf16 v[48:51], a[212:215], a[52:55], v[48:51]\n"
"s_lshl_b32 s23, s21, 2\n"
"v_mfma_f32_16x16x32_bf16 v[48:51], a[216:219], a[56:59], v[48:51]\n"
"s_add_u32 s90, s22, s23\n"
"v_mfma_f32_16x16x32_bf16 v[48:51], a[220:223], a[60:63], v[48:51]\n"
"s_waitcnt lgkmcnt(0)\n"
"v_cvt_pk_bf16_f32 v42, v44, v45\n"
"v_cvt_pk_bf16_f32 v43, v46, v47\n"
"v_mfma_f32_16x16x32_bf16 v[52:55], a[192:195], a[64:67], v[52:55]\n"
"s_waitcnt vmcnt(11)\n"
"s_cmp_eq_u32 s87, 5\ns_cselect_b32 s66, 0, 94208\ns_add_u32 s79, s37, 20480\n"
"v_mfma_f32_16x16x32_bf16 v[52:55], a[196:199], a[68:71], v[52:55]\n"
"s_barrier\n"
"buffer_store_dwordx4 v[40:43], v203, s[64:67], s79 offen offset:0 sc1\n"
"v_mfma_f32_16x16x32_bf16 v[52:55], a[200:203], a[72:75], v[52:55]\n"
"s_add_u32 s76, s27, 0x300\ns_add_u32 m0, s81, 0\n"
"v_mfma_f32_16x16x32_bf16 v[52:55], a[204:207], a[76:79], v[52:55]\n"
"s_mul_i32 s22, s21, 0x9000\n"
"buffer_load_dwordx4 v199, s[60:63], s76 offen offset:0 lds\n"
"v_mfma_f32_16x16x32_bf16 v[52:55], a[208:211], a[80:83], v[52:55]\n"
"s_lshl_b32 s23, s24, 7\n"
"v_mfma_f32_16x16x32_bf16 v[52:55], a[212:215], a[84:87], v[52:55]\n"
"s_add_u32 s76, s27, 0x2700\ns_add_u32 m0, s81, 0x400\n"
"v_mfma_f32_16x16x32_bf16 v[52:55], a[216:219], a[88:91], v[52:55]\n"
"s_add_u32 s22, s22, s23\n"
"buffer_load_dwordx4 v199, s[60:63], s76 offen offset:0 lds\n"
"v_mfma_f32_16x16x32_bf16 v[52:55], a[220:223], a[92:95], v[52:55]\n"
"s_add_u32 s32, s22, s25\n"
"v_cvt_pk_bf16_f32 v48, v48, v49\n"
"v_cvt_pk_bf16_f32 v49, v50, v51\n"
"v_mfma_f32_16x16x32_bf16 v[56:59], a[192:195], a[96:99], v[56:59]\n"
"s_add_u32 s76, s27, 0x4b00\ns_add_u32 m0, s81, 0x800\n"
"v_mfma_f32_16x16x32_bf16 v[56:59], a[196:199], a[100:103], v[56:59]\n"
"s_cmp_eq_u32 s21, 4\ns_cselect_b32 s22, 1, 288\n"
"buffer_load_dwordx4 v199, s[60:63], s76 offen offset:0 lds\n"
"v_mfma_f32_16x16x32_bf16 v[56:59], a[200:203], a[104:107], v[56:59]\n"
"s_lshl_b32 s22, s22, 7\n"
"v_mfma_f32_16x16x32_bf16 v[56:59], a[204:207], a[108:111], v[56:59]\n"
"s_add_u32 s76, s27, 0x6f00\ns_add_u32 m0, s81, 0xc00\n"
"v_mfma_f32_16x16x32_bf16 v[56:59], a[208:211], a[112:115], v[56:59]\n"
"s_add_u32 s35, s32, s22\n"
"buffer_load_dwordx4 v199, s[60:63], s76 offen offset:0 lds\n"
"v_mfma_f32_16x16x32_bf16 v[56:59], a[212:215], a[116:119], v[56:59]\n"
"s_add_u32 s29, s87, 8\n"
"v_mfma_f32_16x16x32_bf16 v[56:59], a[216:219], a[120:123], v[56:59]\n"
"s_add_u32 s76, s27, 0x9300\ns_add_u32 m0, s81, 0x1000\n"
"v_mfma_f32_16x16x32_bf16 v[56:59], a[220:223], a[124:127], v[56:59]\n"
"s_lshl_b32 s29, s29, 12\n"
"v_cvt_pk_bf16_f32 v50, v52, v53\n"
"v_cvt_pk_bf16_f32 v51, v54, v55\n"
"v_mfma_f32_16x16x32_bf16 v[60:63], a[160:163], a[224:227], v[60:63]\n"
"s_cmp_eq_u32 s87, 6\ns_cselect_b32 s66, 0, 94208\ns_add_u32 s79, s37, 24576\n"
"ds_read_b64_tr_b16 a[192:193], v200 offset:32768\n"
"buffer_load_dwordx4 v199, s[60:63], s76 offen offset:0 lds\n"
"v_mfma_f32_16x16x32_bf16 v[60:63], a[164:167], a[228:231], v[60:63]\n"
"s_add_u32 s76, s27, 0xb700\ns_add_u32 m0, s81, 0x1400\n"
"buffer_store_dwordx4 v[48:51], v203, s[64:67], s79 offen offset:0 sc1\n"
"ds_read_b64_tr_b16 a[194:195], v200 offset:34816\n"
"v_mfma_f32_16x16x32_bf16 v[60:63], a[168:171], a[232:235], v[60:63]\n"
"s_add_u32 s29, s29, s37\n"
"ds_read_b64_tr_b16 a[196:197], v200 offset:36864\n"
"buffer_load_dwordx4 v199, s[60:63], s76 offen offset:0 lds\n"
"v_mfma_f32_16x16x32_bf16 v[60:63], a[172:175], a[236:239], v[60:63]\n"
"s_mov_b32 s22, 0x46270726\n"
"ds_read_b64_tr_b16 a[198:199], v200 offset:38912\n"
"v_mfma_f32_16x16x32_bf16 v[60:63], a[176:179], a[240:243], v[60:63]\n"
"s_add_u32 s76, s27, 0xdb00\ns_add_u32 m0, s81, 0x1800\n"
"ds_read_b64_tr_b16 a[200:201], v200 offset:40960\n"
"v_mfma_f32_16x16x32_bf16 v[60:63], a[180:183], a[244:247], v[60:63]\n"
"s_mov_b32 s23, 0x48482808\n"
"ds_read_b64_tr_b16 a[202:203], v200 offset:43008\n"
"buffer_load_dwordx4 v199, s[60:63], s76 offen offset:0 lds\n"
"v_mfma_f32_16x16x32_bf16 v[60:63], a[184:187], a[248:251], v[60:63]\n"
"s_lshl_b32 s21, s87, 3\n"
"ds_read_b64_tr_b16 a[204:205], v200 offset:45056\n"
"v_mfma_f32_16x16x32_bf16 v[60:63], a[188:191], a[252:255], v[60:63]\n"
"s_add_u32 s76, s27, 0xff00\ns_add_u32 m0, s81, 0x1c00\n"
"ds_read_b64_tr_b16 a[206:207], v200 offset:47104\n"
"v_cvt_pk_bf16_f32 v56, v56, v57\n"
"v_cvt_pk_bf16_f32 v57, v58, v59\n"
"v_mfma_f32_16x16x32_bf16 v[64:67], a[160:163], a[0:3], v[64:67]\n"
"s_lshr_b64 s[22:23], s[22:23], s21\n"
"ds_read_b64_tr_b16 a[208:209], v200 offset:49152\n"
"buffer_load_dwordx4 v199, s[60:63], s76 offen offset:0 lds\n"
"v_mfma_f32_16x16x32_bf16 v[64:67], a[164:167], a[4:7], v[64:67]\n"
"s_bfe_u32 s21, s22, 0x40004\n"
"ds_read_b64_tr_b16 a[210:211], v200 offset:51200\n"
"v_mfma_f32_16x16x32_bf16 v[64:67], a[168:171], a[8:11], v[64:67]\n"
"s_and_b32 s24, s22, 15\n"
"ds_read_b64_tr_b16 a[212:213], v200 offset:53248\n"
"v_mfma_f32_16x16x32_bf16 v[64:67], a[172:175], a[12:15], v[64:67]\n"
"s_mul_i32 s22, s24, 20\n"
"ds_read_b64_tr_b16 a[214:215], v200 offset:55296\n"
"v_mfma_f32_16x16x32_bf16 v[64:67], a[176:179], a[16:19], v[64:67]\n"
"s_lshl_b32 s23, s21, 2\n"
"ds_read_b64_tr_b16 a[216:217], v200 offset:57344\n"
"v_mfma_f32_16x16x32_bf16 v[64:67], a[180:183], a[20:23], v[64:67]\n"
"s_add_u32 s91, s22, s23\n"
"ds_read_b64_tr_b16 a[218:219], v200 offset:59392\n"
"v_mfma_f32_16x16x32_bf16 v[64:67], a[184:187], a[24:27], v[64:67]\n"
"s_mul_i32 s22, s21, 0x9000\n"
"ds_read_b64_tr_b16 a[220:221], v200 offset:61440\n"
"v_mfma_f32_16x16x32_bf16 v[64:67], a[188:191], a[28:31], v[64:67]\n"
"s_lshl_b32 s23, s24, 7\n"
"ds_read_b64_tr_b16 a[222:223], v200 offset:63488\n"
"v_cvt_pk_bf16_f32 v60, v60, v61\n"
"v_cvt_pk_bf16_f32 v61, v62, v63\n"
"v_mfma_f32_16x16x32_bf16 v[68:71], a[160:163], a[32:35], v[68:71]\n"
"s_add_u32 s22, s22, s23\n"
"v_mfma_f32_16x16x32_bf16 v[68:71], a[164:167], a[36:39], v[68:71]\n"
"s_add_u32 s33, s22, s25\n"
"v_mfma_f32_16x16x32_bf16 v[68:71], a[168:171], a[40:43], v[68:71]\n"
"s_cmp_eq_u32 s21, 4\ns_cselect_b32 s22, 1, 288\n"
"v_mfma_f32_16x16x32_bf16 v[68:71], a[172:175], a[44:47], v[68:71]\n"
"s_lshl_b32 s22, s22, 7\n"
"v_mfma_f32_16x16x32_bf16 v[68:71], a[176:179], a[48:51], v[68:71]\n"
"s_add_u32 s36, s33, s22\n"
"v_mfma_f32_16x16x32_bf16 v[68:71], a[180:183], a[52:55], v[68:71]\n"
"s_add_u32 s30, s87, 16\n"
"v_mfma_f32_16x16x32_bf16 v[68:71], a[184:187], a[56:59], v[68:71]\n"
"s_lshl_b32 s30, s30, 12\n"
"v_mfma_f32_16x16x32_bf16 v[68:71], a[188:191], a[60:63], v[68:71]\n"
"s_waitcnt lgkmcnt(0)\n"
"v_cvt_pk_bf16_f32 v62, v64, v65\n"
"v_cvt_pk_bf16_f32 v63, v66, v67\n"
"v_mfma_f32_16x16x32_bf16 v[72:75], a[160:163], a[64:67], v[72:75]\n"
"s_waitcnt vmcnt(11)\n"
"s_cmp_eq_u32 s87, 7\ns_cselect_b32 s66, 0, 94208\ns_add_u32 s79, s37, 28672\n"
"v_mfma_f32_16x16x32_bf16 v[72:75], a[164:167], a[68:71], v[72:75]\n"
"s_barrier\n"
"buffer_store_dwordx4 v[60:63], v203, s[64:67], s79 offen offset:0 sc1\n"
"v_mfma_f32_16x16x32_bf16 v[72:75], a[168:171], a[72:75], v[72:75]\n"
"s_add_u32 s76, s27, 0x380\ns_add_u32 m0, s81, 0x8000\n"
"v_mfma_f32_16x16x32_bf16 v[72:75], a[172:175], a[76:79], v[72:75]\n"
"s_add_u32 s30, s30, s37\n"
"buffer_load_dwordx4 v199, s[60:63], s76 offen offset:0 lds\n"
"v_mfma_f32_16x16x32_bf16 v[72:75], a[176:179], a[80:83], v[72:75]\n"
"s_cmp_lt_u32 s87, 6\ns_cselect_b32 s36, s36, 0x7ff00000\n"
"v_mfma_f32_16x16x32_bf16 v[72:75], a[180:183], a[84:87], v[72:75]\n"
"s_add_u32 s76, s27, 0x2780\ns_add_u32 m0, s81, 0x8400\n"
"v_mfma_f32_16x16x32_bf16 v[72:75], a[184:187], a[88:91], v[72:75]\n"
"s_cmp_lt_u32 s87, 7\ns_cselect_b32 s33, s33, 0x7ff00000\ns_cselect_b32 s30, s30, 0x40000000\n"
"buffer_load_dwordx4 v199, s[60:63], s76 offen offset:0 lds\n"
"v_mfma_f32_16x16x32_bf16 v[72:75], a[188:191], a[92:95], v[72:75]\n"
"v_cvt_pk_bf16_f32 v68, v68, v69\n"
"v_cvt_pk_bf16_f32 v69, v70, v71\n"
"v_mfma_f32_16x16x32_bf16 v[76:79], a[160:163], a[96:99], v[76:79]\n"
"s_add_u32 s76, s27, 0x4b80\ns_add_u32 m0, s81, 0x8800\n"
"v_mfma_f32_16x16x32_bf16 v[76:79], a[164:167], a[100:103], v[76:79]\n"
"buffer_load_dwordx4 v199, s[60:63], s76 offen offset:0 lds\n"
"v_mfma_f32_16x16x32_bf16 v[76:79], a[168:171], a[104:107], v[76:79]\n"
"v_mfma_f32_16x16x32_bf16 v[76:79], a[172:175], a[108:111], v[76:79]\n"
"s_add_u32 s76, s27, 0x6f80\ns_add_u32 m0, s81, 0x8c00\n"
"v_mfma_f32_16x16x32_bf16 v[76:79], a[176:179], a[112:115], v[76:79]\n"
"buffer_load_dwordx4 v199, s[60:63], s76 offen offset:0 lds\n"
"v_mfma_f32_16x16x32_bf16 v[76:79], a[180:183], a[116:119], v[76:79]\n"
"v_mfma_f32_16x16x32_bf16 v[76:79], a[184:187], a[120:123], v[76:79]\n"
"s_add_u32 s76, s27, 0x9380\ns_add_u32 m0, s81, 0x9000\n"
"v_mfma_f32_16x16x32_bf16 v[76:79], a[188:191], a[124:127], v[76:79]\n"
"v_cvt_pk_bf16_f32 v70, v72, v73\n"
"v_cvt_pk_bf16_f32 v71, v74, v75\n"
"v_mfma_f32_16x16x32_bf16 v[80:83], a[192:195], a[224:227], v[80:83]\n"
"s_cmp_eq_u32 s87, 0\ns_cselect_b32 s66, 0, 94208\ns_add_u32 s79, s37, 32768\n"
"ds_read_b64_tr_b16 a[160:161], v201 offset:0\n"
"buffer_load_dwordx4 v199, s[60:63], s76 offen offset:0 lds\n"
"v_mfma_f32_16x16x32_bf16 v[80:83], a[196:199], a[228:231], v[80:83]\n"
"s_add_u32 s76, s27, 0xb780\ns_add_u32 m0, s81, 0x9400\n"
"buffer_store_dwordx4 v[68:71], v203, s[64:67], s79 offen offset:0 sc1\n"
"ds_read_b64_tr_b16 a[162:163], v201 offset:2048\n"
"v_mfma_f32_16x16x32_bf16 v[80:83], a[200:203], a[232:235], v[80:83]\n"
"ds_read_b64_tr_b16 a[164:165], v201 offset:4096\n"
"buffer_load_dwordx4 v199, s[60:63], s76 offen offset:0 lds\n"
"v_mfma_f32_16x16x32_bf16 v[80:83], a[204:207], a[236:239], v[80:83]\n"
"ds_read_b64_tr_b16 a[166:167], v201 offset:6144\n"
"v_mfma_f32_16x16x32_bf16 v[80:83], a[208:211], a[240:243], v[80:83]\n"
"s_add_u32 s76, s27, 0xdb80\ns_add_u32 m0, s81, 0x9800\n"
"ds_read_b64_tr_b16 a[168:169], v201 offset:8192\n"
"v_mfma_f32_16x16x32_bf16 v[80:83], a[212:215], a[244:247], v[80:83]\n"
"ds_read_b64_tr_b16 a[170:171], v201 offset:10240\n"
"buffer_load_dwordx4 v199, s[60:63], s76 offen offset:0 lds\n"
"v_mfma_f32_16x16x32_bf16 v[80:83], a[216:219], a[248:251], v[80:83]\n"
"ds_read_b64_tr_b16 a[172:173], v201 offset:12288\n"
"v_mfma_f32_16x16x32_bf16 v[80:83], a[220:223], a[252:255], v[80:83]\n"
"s_add_u32 s76, s27, 0xff80\ns_add_u32 m0, s81, 0x9c00\n"
"ds_read_b64_tr_b16 a[174:175], v201 offset:14336\n"
"v_cvt_pk_bf16_f32 v58, v76, v77\n"
"v_cvt_pk_bf16_f32 v59, v78, v79\n"
"v_mfma_f32_16x16x32_bf16 v[84:87], a[192:195], a[0:3], v[84:87]\n"
"s_cmp_eq_u32 s87, 1\ns_cselect_b32 s66, 0, 94208\ns_add_u32 s79, s37, 36864\n"
"ds_read_b64_tr_b16 a[176:177], v201 offset:16384\n"
"buffer_load_dwordx4 v199, s[60:63], s76 offen offset:0 lds\n"
"v_mfma_f32_16x16x32_bf16 v[84:87], a[196:199], a[4:7], v[84:87]\n"
"buffer_store_dwordx4 v[56:59], v203, s[64:67], s79 offen offset:0 sc1\n"
"ds_read_b64_tr_b16 a[178:179], v201 offset:18432\n"
"v_mfma_f32_16x16x32_bf16 v[84:87], a[200:203], a[8:11], v[84:87]\n"
"ds_read_b64_tr_b16 a[180:181], v201 offset:20480\n"
"v_mfma_f32_16x16x32_bf16 v[84:87], a[204:207], a[12:15], v[84:87]\n"
"ds_read_b64_tr_b16 a[182:183], v201 offset:22528\n"
"v_mfma_f32_16x16x32_bf16 v[84:87], a[208:211], a[16:19], v[84:87]\n"
"ds_read_b64_tr_b16 a[184:185], v201 offset:24576\n"
"v_mfma_f32_16x16x32_bf16 v[84:87], a[212:215], a[20:23], v[84:87]\n"
"ds_read_b64_tr_b16 a[186:187], v201 offset:26624\n"
"v_mfma_f32_16x16x32_bf16 v[84:87], a[216:219], a[24:27], v[84:87]\n"
"ds_read_b64_tr_b16 a[188:189], v201 offset:28672\n"
"v_mfma_f32_16x16x32_bf16 v[84:87], a[220:223], a[28:31], v[84:87]\n"
"ds_read_b64_tr_b16 a[190:191], v201 offset:30720\n"
"v_cvt_pk_bf16_f32 v80, v80, v81\n"
"v_cvt_pk_bf16_f32 v81, v82, v83\n"
"v_mfma_f32_16x16x32_bf16 v[88:91], a[192:195], a[32:35], v[88:91]\n"
"v_mfma_f32_16x16x32_bf16 v[88:91], a[196:199], a[36:39], v[88:91]\n"
"v_mfma_f32_16x16x32_bf16 v[88:91], a[200:203], a[40:43], v[88:91]\n"
"v_mfma_f32_16x16x32_bf16 v[88:91], a[204:207], a[44:47], v[88:91]\n"
"v_mfma_f32_16x16x32_bf16 v[88:91], a[208:211], a[48:51], v[88:91]\n"
"v_mfma_f32_16x16x32_bf16 v[88:91], a[212:215], a[52:55], v[88:91]\n"
"v_mfma_f32_16x16x32_bf16 v[88:91], a[216:219], a[56:59], v[88:91]\n"
"v_mfma_f32_16x16x32_bf16 v[88:91], a[220:223], a[60:63], v[88:91]\n"
"s_waitcnt lgkmcnt(0)\n"
"v_cvt_pk_bf16_f32 v82, v84, v85\n"
"v_cvt_pk_bf16_f32 v83, v86, v87\n"
"v_mfma_f32_16x16x32_bf16 v[92:95], a[192:195], a[64:67], v[92:95]\n"
"s_waitcnt vmcnt(10)\n"
"s_cmp_eq_u32 s87, 2\ns_cselect_b32 s66, 0, 94208\ns_add_u32 s79, s37, 40960\n"
"v_mfma_f32_16x16x32_bf16 v[92:95], a[196:199], a[68:71], v[92:95]\n"
"s_barrier\n"
"buffer_store_dwordx4 v[80:83], v203, s[64:67], s79 offen offset:0 sc1\n"
"v_mfma_f32_16x16x32_bf16 v[92:95], a[200:203], a[72:75], v[92:95]\n"
"s_cmp_lg_u32 s19, 0\ns_cbranch_scc1 L_sg8_%=\ns_mov_b64 exec, 0x1fe\nbuffer_store_dword v253, v206, s[68:71], 0 offen offset:0 sc1\ns_mov_b64 exec, -1\nL_sg8_%=:\n"
"v_mfma_f32_16x16x32_bf16 v[92:95], a[204:207], a[76:79], v[92:95]\n"
"s_add_u32 s76, s27, 0x400\ns_add_u32 m0, s81, 0x10000\n"
"v_mfma_f32_16x16x32_bf16 v[92:95], a[208:211], a[80:83], v[92:95]\n"
"buffer_load_dwordx4 v199, s[60:63], s76 offen offset:0 lds\n"
"v_mfma_f32_16x16x32_bf16 v[92:95], a[212:215], a[84:87], v[92:95]\n"
"s_add_u32 s76, s27, 0x2800\ns_add_u32 m0, s81, 0x10400\n"
"v_mfma_f32_16x16x32_bf16 v[92:95], a[216:219], a[88:91], v[92:95]\n"
"buffer_load_dwordx4 v199, s[60:63], s76 offen offset:0 lds\n"
"v_mfma_f32_16x16x32_bf16 v[92:95], a[220:223], a[92:95], v[92:95]\n"
"v_cvt_pk_bf16_f32 v88, v88, v89\n"
"v_cvt_pk_bf16_f32 v89, v90, v91\n"
"v_mfma_f32_16x16x32_bf16 v[96:99], a[192:195], a[96:99], v[96:99]\n"
"s_add_u32 s76, s27, 0x4c00\ns_add_u32 m0, s81, 0x10800\n"
"v_mfma_f32_16x16x32_bf16 v[96:99], a[196:199], a[100:103], v[96:99]\n"
"buffer_load_dwordx4 v199, s[60:63], s76 offen offset:0 lds\n"
"v_mfma_f32_16x16x32_bf16 v[96:99], a[200:203], a[104:107], v[96:99]\n"
"v_mfma_f32_16x16x32_bf16 v[96:99], a[204:207], a[108:111], v[96:99]\n"
"s_add_u32 s76, s27, 0x7000\ns_add_u32 m0, s81, 0x10c00\n"
"v_mfma_f32_16x16x32_bf16 v[96:99], a[208:211], a[112:115], v[96:99]\n"
"buffer_load_dwordx4 v199, s[60:63], s76 offen offset:0 lds\n"
"v_mfma_f32_16x16x32_bf16 v[96:99], a[212:215], a[116:119], v[96:99]\n"
"v_mfma_f32_16x16x32_bf16 v[96:99], a[216:219], a[120:123], v[96:99]\n"
"s_add_u32 s76, s27, 0x9400\ns_add_u32 m0, s81, 0x11000\n"
"v_mfma_f32_16x16x32_bf16 v[96:99], a[220:223], a[124:127], v[96:99]\n"
"v_cvt_pk_bf16_f32 v90, v92, v93\n"
"v_cvt_pk_bf16_f32 v91, v94, v95\n"
"v_mfma_f32_16x16x32_bf16 v[100:103], a[160:163], a[224:227], v[100:103]\n"
"s_cmp_eq_u32 s87, 3\ns_cselect_b32 s66, 0, 94208\ns_add_u32 s79, s37, 45056\n"
"ds_read_b64_tr_b16 a[192:193], v200 offset:0\n"
"buffer_load_dwordx4 v199, s[60:63], s76 offen offset:0 lds\n"
"v_mfma_f32_16x16x32_bf16 v[100:103], a[164:167], a[228:231], v[100:103]\n"
"s_add_u32 s76, s27, 0xb800\ns_add_u32 m0, s81, 0x11400\n"
"buffer_store_dwordx4 v[88:91], v203, s[64:67], s79 offen offset:0 sc1\n"
"ds_read_b64_tr_b16 a[194:195], v200 offset:2048\n"
"v_mfma_f32_16x16x32_bf16 v[100:103], a[168:171], a[232:235], v[100:103]\n"
"ds_read_b64_tr_b16 a[196:197], v200 offset:4096\n"
"buffer_load_dwordx4 v199, s[60:63], s76 offen offset:0 lds\n"
"v_mfma_f32_16x16x32_bf16 v[100:103], a[172:175], a[236:239], v[100:103]\n"
"ds_read_b64_tr_b16 a[198:199], v200 offset:6144\n"
"v_mfma_f32_16x16x32_bf16 v[100:103], a[176:179], a[240:243], v[100:103]\n"
"s_add_u32 s76, s27, 0xdc00\ns_add_u32 m0, s81, 0x11800\n"
"ds_read_b64_tr_b16 a[200:201], v200 offset:8192\n"
"v_mfma_f32_16x16x32_bf16 v[100:103], a[180:183], a[244:247], v[100:103]\n"
"ds_read_b64_tr_b16 a[202:203], v200 offset:10240\n"
"buffer_load_dwordx4 v199, s[60:63], s76 offen offset:0 lds\n"
"v_mfma_f32_16x16x32_bf16 v[100:103], a[184:187], a[248:251], v[100:103]\n"
"ds_read_b64_tr_b16 a[204:205], v200 offset:12288\n"
"v_mfma_f32_16x16x32_bf16 v[100:103], a[188:191], a[252:255], v[100:103]\n"
"s_add_u32 s76, s27, 0x10000\ns_add_u32 m0, s81, 0x11c00\n"
"ds_read_b64_tr_b16 a[206:207], v200 offset:14336\n"
"v_cvt_pk_bf16_f32 v96, v96, v97\n"
"v_cvt_pk_bf16_f32 v97, v98, v99\n"
"v_mfma_f32_16x16x32_bf16 v[104:107], a[160:163], a[0:3], v[104:107]\n"
"ds_read_b64_tr_b16 a[208:209], v200 offset:16384\n"
"buffer_load_dwordx4 v199, s[60:63], s76 offen offset:0 lds\n"
"v_mfma_f32_16x16x32_bf16 v[104:107], a[164:167], a[4:7], v[104:107]\n"
"ds_read_b64_tr_b16 a[210:211], v200 offset:18432\n"
"v_mfma_f32_16x16x32_bf16 v[104:107], a[168:171], a[8:11], v[104:107]\n"
"ds_read_b64_tr_b16 a[212:213], v200 offset:20480\n"
"v_mfma_f32_16x16x32_bf16 v[104:107], a[172:175], a[12:15], v[104:107]\n"
"ds_read_b64_tr_b16 a[214:215], v200 offset:22528\n"
"v_mfma_f32_16x16x32_bf16 v[104:107], a[176:179], a[16:19], v[104:107]\n"
"ds_read_b64_tr_b16 a[216:217], v200 offset:24576\n"
"v_mfma_f32_16x16x32_bf16 v[104:107], a[180:183], a[20:23], v[104:107]\n"
"ds_read_b64_tr_b16 a[218:219], v200 offset:26624\n"
"v_mfma_f32_16x16x32_bf16 v[104:107], a[184:187], a[24:27], v[104:107]\n"
"ds_read_b64_tr_b16 a[220:221], v200 offset:28672\n"
"v_mfma_f32_16x16x32_bf16 v[104:107], a[188:191], a[28:31], v[104:107]\n"
"ds_read_b64_tr_b16 a[222:223], v200 offset:30720\n"
"v_cvt_pk_bf16_f32 v100, v100, v101\n"
"v_cvt_pk_bf16_f32 v101, v102, v103\n"
"v_mfma_f32_16x16x32_bf16 v[108:111], a[160:163], a[32:35], v[108:111]\n"
"v_mfma_f32_16x16x32_bf16 v[108:111], a[164:167], a[36:39], v[108:111]\n"
"v_mfma_f32_16x16x32_bf16 v[108:111], a[168:171], a[40:43], v[108:111]\n"
"v_mfma_f32_16x16x32_bf16 v[108:111], a[172:175], a[44:47], v[108:111]\n"
"v_mfma_f32_16x16x32_bf16 v[108:111], a[176:179], a[48:51], v[108:111]\n"
"v_mfma_f32_16x16x32_bf16 v[108:111], a[180:183], a[52:55], v[108:111]\n"
"v_mfma_f32_16x16x32_bf16 v[108:111], a[184:187], a[56:59], v[108:111]\n"
"v_mfma_f32_16x16x32_bf16 v[108:111], a[188:191], a[60:63], v[108:111]\n"
"s_waitcnt lgkmcnt(0)\n"
"v_cvt_pk_bf16_f32 v102, v104, v105\n"
"v_cvt_pk_bf16_f32 v103, v106, v107\n"
"v_mfma_f32_16x16x32_bf16 v[112:115], a[160:163], a[64:67], v[112:115]\n"
"s_waitcnt vmcnt(11)\n"
"s_cmp_eq_u32 s87, 4\ns_cselect_b32 s66, 0, 94208\ns_add_u32 s79, s37, 49152\n"
"v_mfma_f32_16x16x32_bf16 v[112:115], a[164:167], a[68:71], v[112:115]\n"
"s_barrier\n"
"buffer_store_dwordx4 v[100:103], v203, s[64:67], s79 offen offset:0 sc1\n"
"v_mfma_f32_16x16x32_bf16 v[112:115], a[168:171], a[72:75], v[112:115]\n"
"v_mfma_f32_16x16x32_bf16 v[112:115], a[172:175], a[76:79], v[112:115]\n"
"v_mfma_f32_16x16x32_bf16 v[112:115], a[176:179], a[80:83], v[112:115]\n"
"v_mfma_f32_16x16x32_bf16 v[112:115], a[180:183], a[84:87], v[112:115]\n"
"v_mfma_f32_16x16x32_bf16 v[112:115], a[184:187], a[88:91], v[112:115]\n"
"buffer_load_dword v212, v254, s[68:71], 0 offen offset:0 sc1\n"
"v_mfma_f32_16x16x32_bf16 v[112:115], a[188:191], a[92:95], v[112:115]\n"
"v_cvt_pk_bf16_f32 v108, v108, v109\n"
"v_cvt_pk_bf16_f32 v109, v110, v111\n"
"v_mfma_f32_16x16x32_bf16 v[116:119], a[160:163], a[96:99], v[116:119]\n"
"v_mfma_f32_16x16x32_bf16 v[116:119], a[164:167], a[100:103], v[116:119]\n"
"v_mfma_f32_16x16x32_bf16 v[116:119], a[168:171], a[104:107], v[116:119]\n"
"v_mfma_f32_16x16x32_bf16 v[116:119], a[172:175], a[108:111], v[116:119]\n"
"v_mfma_f32_16x16x32_bf16 v[116:119], a[176:179], a[112:115], v[116:119]\n"
"v_mfma_f32_16x16x32_bf16 v[116:119], a[180:183], a[116:119], v[116:119]\n"
"v_mfma_f32_16x16x32_bf16 v[116:119], a[184:187], a[120:123], v[116:119]\n"
"v_mfma_f32_16x16x32_bf16 v[116:119], a[188:191], a[124:127], v[116:119]\n"
"v_cvt_pk_bf16_f32 v110, v112, v113\n"
"v_cvt_pk_bf16_f32 v111, v114, v115\n"
"v_mfma_f32_16x16x32_bf16 v[120:123], a[192:195], a[224:227], v[120:123]\n"
"s_cmp_eq_u32 s87, 5\ns_cselect_b32 s66, 0, 94208\ns_add_u32 s79, s37, 53248\n"
"ds_read_b64_tr_b16 a[160:161], v200 offset:32768\n"
"v_mfma_f32_16x16x32_bf16 v[120:123], a[196:199], a[228:231], v[120:123]\n"
"buffer_store_dwordx4 v[108:111], v203, s[64:67], s79 offen offset:0 sc1\n"
"ds_read_b64_tr_b16 a[162:163], v200 offset:34816\n"
"v_mfma_f32_16x16x32_bf16 v[120:123], a[200:203], a[232:235], v[120:123]\n"
"ds_read_b64_tr_b16 a[164:165], v200 offset:36864\n"
"v_mfma_f32_16x16x32_bf16 v[120:123], a[204:207], a[236:239], v[120:123]\n"
"ds_read_b64_tr_b16 a[166:167], v200 offset:38912\n"
"v_mfma_f32_16x16x32_bf16 v[120:123], a[208:211], a[240:243], v[120:123]\n"
"ds_read_b64_tr_b16 a[168:169], v200 offset:40960\n"
"v_mfma_f32_16x16x32_bf16 v[120:123], a[212:215], a[244:247], v[120:123]\n"
"ds_read_b64_tr_b16 a[170:171], v200 offset:43008\n"
"v_mfma_f32_16x16x32_bf16 v[120:123], a[216:219], a[248:251], v[120:123]\n"
"ds_read_b64_tr_b16 a[172:173], v200 offset:45056\n"
"v_mfma_f32_16x16x32_bf16 v[120:123], a[220:223], a[252:255], v[120:123]\n"
"ds_read_b64_tr_b16 a[174:175], v200 offset:47104\n"
"v_cvt_pk_bf16_f32 v98, v116, v117\n"
"v_cvt_pk_bf16_f32 v99, v118, v119\n"
"v_mfma_f32_16x16x32_bf16 v[124:127], a[192:195], a[0:3], v[124:127]\n"
"s_cmp_eq_u32 s87, 6\ns_cselect_b32 s66, 0, 94208\ns_add_u32 s79, s37, 57344\n"
"ds_read_b64_tr_b16 a[176:177], v200 offset:49152\n"
"v_mfma_f32_16x16x32_bf16 v[124:127], a[196:199], a[4:7], v[124:127]\n"
"buffer_store_dwordx4 v[96:99], v203, s[64:67], s79 offen offset:0 sc1\n"
"ds_read_b64_tr_b16 a[178:179], v200 offset:51200\n"
"v_mfma_f32_16x16x32_bf16 v[124:127], a[200:203], a[8:11], v[124:127]\n"
"ds_read_b64_tr_b16 a[180:181], v200 offset:53248\n"
"v_mfma_f32_16x16x32_bf16 v[124:127], a[204:207], a[12:15], v[124:127]\n"
"ds_read_b64_tr_b16 a[182:183], v200 offset:55296\n"
"v_mfma_f32_16x16x32_bf16 v[124:127], a[208:211], a[16:19], v[124:127]\n"
"ds_read_b64_tr_b16 a[184:185], v200 offset:57344\n"
"v_mfma_f32_16x16x32_bf16 v[124:127], a[212:215], a[20:23], v[124:127]\n"
"ds_read_b64_tr_b16 a[186:187], v200 offset:59392\n"
"v_mfma_f32_16x16x32_bf16 v[124:127], a[216:219], a[24:27], v[124:127]\n"
"ds_read_b64_tr_b16 a[188:189], v200 offset:61440\n"
"v_mfma_f32_16x16x32_bf16 v[124:127], a[220:223], a[28:31], v[124:127]\n"
"ds_read_b64_tr_b16 a[190:191], v200 offset:63488\n"
"v_cvt_pk_bf16_f32 v120, v120, v121\n"
"v_cvt_pk_bf16_f32 v121, v122, v123\n"
"v_mfma_f32_16x16x32_bf16 v[128:131], a[192:195], a[32:35], v[128:131]\n"
"v_mfma_f32_16x16x32_bf16 v[128:131], a[196:199], a[36:39], v[128:131]\n"
"v_mfma_f32_16x16x32_bf16 v[128:131], a[200:203], a[40:43], v[128:131]\n"
"v_mfma_f32_16x16x32_bf16 v[128:131], a[204:207], a[44:47], v[128:131]\n"
"v_mfma_f32_16x16x32_bf16 v[128:131], a[208:211], a[48:51], v[128:131]\n"
"v_mfma_f32_16x16x32_bf16 v[128:131], a[212:215], a[52:55], v[128:131]\n"
"v_mfma_f32_16x16x32_bf16 v[128:131], a[216:219], a[56:59], v[128:131]\n"
"v_mfma_f32_16x16x32_bf16 v[128:131], a[220:223], a[60:63], v[128:131]\n"
"s_waitcnt lgkmcnt(0)\n"
"v_cvt_pk_bf16_f32 v122, v124, v125\n"
"v_cvt_pk_bf16_f32 v123, v126, v127\n"
"v_mfma_f32_16x16x32_bf16 v[132:135], a[192:195], a[64:67], v[132:135]\n"
"s_waitcnt vmcnt(4)\n"
"s_cmp_eq_u32 s87, 7\ns_cselect_b32 s66, 0, 94208\ns_add_u32 s79, s37, 61440\n"
"v_mfma_f32_16x16x32_bf16 v[132:135], a[196:199], a[68:71], v[132:135]\n"
"s_barrier\n"
"buffer_store_dwordx4 v[120:123], v203, s[64:67], s79 offen offset:0 sc1\n"
"v_mfma_f32_16x16x32_bf16 v[132:135], a[200:203], a[72:75], v[132:135]\n"
"v_mfma_f32_16x16x32_bf16 v[132:135], a[204:207], a[76:79], v[132:135]\n"
"v_mfma_f32_16x16x32_bf16 v[132:135], a[208:211], a[80:83], v[132:135]\n"
"v_mfma_f32_16x16x32_bf16 v[132:135], a[212:215], a[84:87], v[132:135]\n"
"v_mfma_f32_16x16x32_bf16 v[132:135], a[216:219], a[88:91], v[132:135]\n"
"s_waitcnt vmcnt(3)\n"
"v_mfma_f32_16x16x32_bf16 v[132:135], a[220:223], a[92:95], v[132:135]\n"
"v_cmp_eq_u32 vcc, s80, v212\ns_nop 4\ns_and_b32 s20, vcc_lo, 0xff00\ns_cmp_eq_u32 s20, 0xff00\ns_cbranch_scc0 L_sp10_%=\nL_bk10_%=:\n"
"v_cvt_pk_bf16_f32 v128, v128, v129\n"
"v_cvt_pk_bf16_f32 v129, v130, v131\n"
"v_mfma_f32_16x16x32_bf16 v[136:139], a[192:195], a[96:99], v[136:139]\n"
"buffer_load_dword v248, v252, s[68:71], 0 offen offset:128 sc1\n"
"v_add_u32 v196, s28, v203\n"
"v_mfma_f32_16x16x32_bf16 v[136:139], a[196:199], a[100:103], v[136:139]\n"
"buffer_load_dwordx4 v[220:223], v196, s[52:55], s92 offen offset:0 sc1\n"
"v_mfma_f32_16x16x32_bf16 v[136:139], a[200:203], a[104:107], v[136:139]\n"
"buffer_load_dwordx4 v[224:227], v196, s[52:55], s93 offen offset:0 sc1\n"
"v_mfma_f32_16x16x32_bf16 v[136:139], a[204:207], a[108:111], v[136:139]\n"
"buffer_load_dwordx4 v[228:231], v196, s[52:55], s94 offen offset:0 sc1\n"
"v_mfma_f32_16x16x32_bf16 v[136:139], a[208:211], a[112:115], v[136:139]\n"
"buffer_load_dwordx4 v[232:235], v196, s[52:55], s95 offen offset:0 sc1\n"
"v_mfma_f32_16x16x32_bf16 v[136:139], a[212:215], a[116:119], v[136:139]\n"
"buffer_load_dwordx4 v[236:239], v196, s[52:55], s96 offen offset:0 sc1\n"
"v_mfma_f32_16x16x32_bf16 v[136:139], a[216:219], a[120:123], v[136:139]\n"
"buffer_load_dwordx4 v[240:243], v196, s[52:55], s97 offen offset:0 sc1\n"
"v_mfma_f32_16x16x32_bf16 v[136:139], a[220:223], a[124:127], v[136:139]\n"
"buffer_load_dwordx4 v[244:247], v196, s[52:55], s98 offen offset:0 sc1\n"
"v_cvt_pk_bf16_f32 v130, v132, v133\n"
"v_cvt_pk_bf16_f32 v131, v134, v135\n"
"v_mfma_f32_16x16x32_bf16 v[140:143], a[160:163], a[224:227], v[140:143]\n"
"s_cmp_eq_u32 s87, 0\ns_cselect_b32 s66, 0, 94208\ns_add_u32 s79, s37, 65536\n"
"ds_read_b64_tr_b16 a[192:193], v201 offset:0\n"
"v_mfma_f32_16x16x32_bf16 v[140:143], a[164:167], a[228:231], v[140:143]\n"
"buffer_store_dwordx4 v[128:131], v203, s[64:67], s79 offen offset:0 sc1\n"
"ds_read_b64_tr_b16 a[194:195], v201 offset:2048\n"
"v_mfma_f32_16x16x32_bf16 v[140:143], a[168:171], a[232:235], v[140:143]\n"
"ds_read_b64_tr_b16 a[196:197], v201 offset:4096\n"
"v_mfma_f32_16x16x32_bf16 v[140:143], a[172:175], a[236:239], v[140:143]\n"
"ds_read_b64_tr_b16 a[198:199], v201 offset:6144\n"
"v_mfma_f32_16x16x32_bf16 v[140:143], a[176:179], a[240:243], v[140:143]\n"
"ds_read_b64_tr_b16 a[200:201], v201 offset:8192\n"
"v_mfma_f32_16x16x32_bf16 v[140:143], a[180:183], a[244:247], v[140:143]\n"
"ds_read_b64_tr_b16 a[202:203], v201 offset:10240\n"
"v_mfma_f32_16x16x32_bf16 v[140:143], a[184:187], a[248:251], v[140:143]\n"
"ds_read_b64_tr_b16 a[204:205], v201 offset:12288\n"
"v_mfma_f32_16x16x32_bf16 v[140:143], a[188:191], a[252:255], v[140:143]\n"
"ds_read_b64_tr_b16 a[206:207], v201 offset:14336\n"
"v_cvt_pk_bf16_f32 v136, v136, v137\n"
"v_cvt_pk_bf16_f32 v137, v138, v139\n"
"v_mfma_f32_16x16x32_bf16 v[144:147], a[160:163], a[0:3], v[144:147]\n"
"ds_read_b64_tr_b16 a[208:209], v201 offset:16384\n"
"v_mfma_f32_16x16x32_bf16 v[144:147], a[164:167], a[4:7], v[144:147]\n"
"ds_read_b64_tr_b16 a[210:211], v201 offset:18432\n"
"v_mfma_f32_16x16x32_bf16 v[144:147], a[168:171], a[8:11], v[144:147]\n"
"ds_read_b64_tr_b16 a[212:213], v201 offset:20480\n"
"v_mfma_f32_16x16x32_bf16 v[144:147], a[172:175], a[12:15], v[144:147]\n"
"ds_read_b64_tr_b16 a[214:215], v201 offset:22528\n"
"v_mfma_f32_16x16x32_bf16 v[144:147], a[176:179], a[16:19], v[144:147]\n"
"ds_read_b64_tr_b16 a[216:217], v201 offset:24576\n"
"v_mfma_f32_16x16x32_bf16 v[144:147], a[180:183], a[20:23], v[144:147]\n"
"ds_read_b64_tr_b16 a[218:219], v201 offset:26624\n"
"v_mfma_f32_16x16x32_bf16 v[144:147], a[184:187], a[24:27], v[144:147]\n"
"ds_read_b64_tr_b16 a[220:221], v201 offset:28672\n"
"v_mfma_f32_16x16x32_bf16 v[144:147], a[188:191], a[28:31], v[144:147]\n"
"ds_read_b64_tr_b16 a[222:223], v201 offset:30720\n"
"v_cvt_pk_bf16_f32 v140, v140, v141\n"
"v_cvt_pk_bf16_f32 v141, v142, v143\n"
"v_mfma_f32_16x16x32_bf16 v[148:151], a[160:163], a[32:35], v[148:151]\n"
"v_mfma_f32_16x16x32_bf16 v[148:151], a[164:167], a[36:39], v[148:151]\n"
"v_mfma_f32_16x16x32_bf16 v[148:151], a[168:171], a[40:43], v[148:151]\n"
"v_mfma_f32_16x16x32_bf16 v[148:151], a[172:175], a[44:47], v[148:151]\n"
"v_mfma_f32_16x16x32_bf16 v[148:151], a[176:179], a[48:51], v[148:151]\n"
"v_mfma_f32_16x16x32_bf16 v[148:151], a[180:183], a[52:55], v[148:151]\n"
"v_mfma_f32_16x16x32_bf16 v[148:151], a[184:187], a[56:59], v[148:151]\n"
"v_mfma_f32_16x16x32_bf16 v[148:151], a[188:191], a[60:63], v[148:151]\n"
"s_waitcnt lgkmcnt(0)\n"
"v_cvt_pk_bf16_f32 v142, v144, v145\n"
"v_cvt_pk_bf16_f32 v143, v146, v147\n"
"v_mfma_f32_16x16x32_bf16 v[152:155], a[160:163], a[64:67], v[152:155]\n"
"s_waitcnt vmcnt(9)\n"
"s_cmp_eq_u32 s87, 1\ns_cselect_b32 s66, 0, 94208\ns_add_u32 s79, s37, 69632\n"
"v_mfma_f32_16x16x32_bf16 v[152:155], a[164:167], a[68:71], v[152:155]\n"
"s_barrier\n"
"buffer_store_dwordx4 v[140:143], v203, s[64:67], s79 offen offset:0 sc1\n"
"v_mfma_f32_16x16x32_bf16 v[152:155], a[168:171], a[72:75], v[152:155]\n"
"s_cmp_lg_u32 s19, 0\ns_cbranch_scc1 L_sg9_%=\ns_mov_b64 exec, 0x1fe\nbuffer_store_dword v253, v206, s[68:71], 0 offen offset:32 sc1\ns_mov_b64 exec, -1\nL_sg9_%=:\n"
"v_mfma_f32_16x16x32_bf16 v[152:155], a[172:175], a[76:79], v[152:155]\n"
"v_mfma_f32_16x16x32_bf16 v[152:155], a[176:179], a[80:83], v[152:155]\n"
"v_mfma_f32_16x16x32_bf16 v[152:155], a[180:183], a[84:87], v[152:155]\n"
"v_mfma_f32_16x16x32_bf16 v[152:155], a[184:187], a[88:91], v[152:155]\n"
"v_mfma_f32_16x16x32_bf16 v[152:155], a[188:191], a[92:95], v[152:155]\n"
"v_cvt_pk_bf16_f32 v148, v148, v149\n"
"v_cvt_pk_bf16_f32 v149, v150, v151\n"
"v_mfma_f32_16x16x32_bf16 v[156:159], a[160:163], a[96:99], v[156:159]\n"
"v_mfma_f32_16x16x32_bf16 v[156:159], a[164:167], a[100:103], v[156:159]\n"
"v_mfma_f32_16x16x32_bf16 v[156:159], a[168:171], a[104:107], v[156:159]\n"
"v_mfma_f32_16x16x32_bf16 v[156:159], a[172:175], a[108:111], v[156:159]\n"
"v_mfma_f32_16x16x32_bf16 v[156:159], a[176:179], a[112:115], v[156:159]\n"
"v_mfma_f32_16x16x32_bf16 v[156:159], a[180:183], a[116:119], v[156:159]\n"
"v_mfma_f32_16x16x32_bf16 v[156:159], a[184:187], a[120:123], v[156:159]\n"
"v_mfma_f32_16x16x32_bf16 v[156:159], a[188:191], a[124:127], v[156:159]\n"
"v_cvt_pk_bf16_f32 v150, v152, v153\n"
"v_cvt_pk_bf16_f32 v151, v154, v155\n"
"v_mfma_f32_16x16x32_bf16 v[160:163], a[192:195], a[224:227], v[160:163]\n"
"s_cmp_eq_u32 s87, 2\ns_cselect_b32 s66, 0, 94208\ns_add_u32 s79, s37, 73728\n"
"v_mfma_f32_16x16x32_bf16 v[160:163], a[196:199], a[228:231], v[160:163]\n"
"buffer_store_dwordx4 v[148:151], v203, s[64:67], s79 offen offset:0 sc1\n"
"v_mfma_f32_16x16x32_bf16 v[160:163], a[200:203], a[232:235], v[160:163]\n"
"v_mfma_f32_16x16x32_bf16 v[160:163], a[204:207], a[236:239], v[160:163]\n"
"v_mfma_f32_16x16x32_bf16 v[160:163], a[208:211], a[240:243], v[160:163]\n"
"v_mfma_f32_16x16x32_bf16 v[160:163], a[212:215], a[244:247], v[160:163]\n"
"v_mfma_f32_16x16x32_bf16 v[160:163], a[216:219], a[248:251], v[160:163]\n"
"v_mfma_f32_16x16x32_bf16 v[160:163], a[220:223], a[252:255], v[160:163]\n"
"v_cvt_pk_bf16_f32 v138, v156, v157\n"
"v_cvt_pk_bf16_f32 v139, v158, v159\n"
"v_mfma_f32_16x16x32_bf16 v[164:167], a[192:195], a[0:3], v[164:167]\n"
"s_cmp_eq_u32 s87, 3\ns_cselect_b32 s66, 0, 94208\ns_add_u32 s79, s37, 77824\n"
"v_mfma_f32_16x16x32_bf16 v[164:167], a[196:199], a[4:7], v[164:167]\n"
"buffer_store_dwordx4 v[136:139], v203, s[64:67], s79 offen offset:0 sc1\n"
"v_mfma_f32_16x16x32_bf16 v[164:167], a[200:203], a[8:11], v[164:167]\n"
"v_mfma_f32_16x16x32_bf16 v[164:167], a[204:207], a[12:15], v[164:167]\n"
"v_mfma_f32_16x16x32_bf16 v[164:167], a[208:211], a[16:19], v[164:167]\n"
"v_mfma_f32_16x16x32_bf16 v[164:167], a[212:215], a[20:23], v[164:167]\n"
"v_mfma_f32_16x16x32_bf16 v[164:167], a[216:219], a[24:27], v[164:167]\n"
"v_mfma_f32_16x16x32_bf16 v[164:167], a[220:223], a[28:31], v[164:167]\n"
"v_cvt_pk_bf16_f32 v160, v160, v161\n"
"v_cvt_pk_bf16_f32 v161, v162, v163\n"
"v_mfma_f32_16x16x32_bf16 v[168:171], a[192:195], a[32:35], v[168:171]\n"
"v_mfma_f32_16x16x32_bf16 v[168:171], a[196:199], a[36:39], v[168:171]\n"
"v_mfma_f32_16x16x32_bf16 v[168:171], a[200:203], a[40:43], v[168:171]\n"
"v_mfma_f32_16x16x32_bf16 v[168:171], a[204:207], a[44:47], v[168:171]\n"
"v_mfma_f32_16x16x32_bf16 v[168:171], a[208:211], a[48:51], v[168:171]\n"
"v_mfma_f32_16x16x32_bf16 v[168:171], a[212:215], a[52:55], v[168:171]\n"
"v_mfma_f32_16x16x32_bf16 v[168:171], a[216:219], a[56:59], v[168:171]\n"
"v_mfma_f32_16x16x32_bf16 v[168:171], a[220:223], a[60:63], v[168:171]\n"
"v_cvt_pk_bf16_f32 v162, v164, v165\n"
"v_cvt_pk_bf16_f32 v163, v166, v167\n"
"v_mfma_f32_16x16x32_bf16 v[172:175], a[192:195], a[64:67], v[172:175]\n"
"s_cmp_eq_u32 s87, 4\ns_cselect_b32 s66, 0, 94208\ns_add_u32 s79, s37, 81920\n"
"v_mfma_f32_16x16x32_bf16 v[172:175], a[196:199], a[68:71], v[172:175]\n"
"buffer_store_dwordx4 v[160:163], v203, s[64:67], s79 offen offset:0 sc1\n"
"v_mfma_f32_16x16x32_bf16 v[172:175], a[200:203], a[72:75], v[172:175]\n"
"v_mfma_f32_16x16x32_bf16 v[172:175], a[204:207], a[76:79], v[172:175]\n"
"v_mfma_f32_16x16x32_bf16 v[172:175], a[208:211], a[80:83], v[172:175]\n"
"v_mfma_f32_16x16x32_bf16 v[172:175], a[212:215], a[84:87], v[172:175]\n"
"v_mfma_f32_16x16x32_bf16 v[172:175], a[216:219], a[88:91], v[172:175]\n"
"v_mfma_f32_16x16x32_bf16 v[172:175], a[220:223], a[92:95], v[172:175]\n"
"v_cvt_pk_bf16_f32 v168, v168, v169\n"
"v_cvt_pk_bf16_f32 v169, v170, v171\n"
"v_mfma_f32_16x16x32_bf16 v[176:179], a[192:195], a[96:99], v[176:179]\n"
"v_mfma_f32_16x16x32_bf16 v[176:179], a[196:199], a[100:103], v[176:179]\n"
"v_mfma_f32_16x16x32_bf16 v[176:179], a[200:203], a[104:107], v[176:179]\n"
"v_mfma_f32_16x16x32_bf16 v[176:179], a[204:207], a[108:111], v[176:179]\n"
"v_mfma_f32_16x16x32_bf16 v[176:179], a[208:211], a[112:115], v[176:179]\n"
"v_mfma_f32_16x16x32_bf16 v[176:179], a[212:215], a[116:119], v[176:179]\n"
"v_mfma_f32_16x16x32_bf16 v[176:179], a[216:219], a[120:123], v[176:179]\n"
"v_mfma_f32_16x16x32_bf16 v[176:179], a[220:223], a[124:127], v[176:179]\n"
"v_cvt_pk_bf16_f32 v170, v172, v173\n"
"v_cvt_pk_bf16_f32 v171, v174, v175\n"
"s_cmp_eq_u32 s87, 5\ns_cselect_b32 s66, 0, 94208\ns_add_u32 s79, s37, 86016\n"
"buffer_store_dwordx4 v[168:171], v203, s[64:67], s79 offen offset:0 sc1\n"
"s_nop 3\n"
"v_cvt_pk_bf16_f32 v176, v176, v177\n"
"v_cvt_pk_bf16_f32 v177, v178, v179\n"
"s_cmp_eq_u32 s87, 6\ns_cselect_b32 s66, 0, 94208\ns_add_u32 s79, s37, 90112\n"
"buffer_store_dwordx4 v[176:179], v203, s[64:67], s79 offen offset:0 sc1\n"
"buffer_load_dword v212, v254, s[68:71], 0 offen offset:0 sc1\n"
"s_waitcnt vmcnt(0)\n"
"v_cmp_ne_u32 vcc, v253, v248\ns_nop 4\ns_and_b32 s20, vcc_lo, 0x1fe\ns_cbranch_scc1 L_fx11_%=\nL_bk11_%=:\n"
"v_cmp_eq_u32 vcc, s80, v212\ns_nop 4\ns_and_b32 s20, vcc_lo, 0xff0000\ns_cmp_eq_u32 s20, 0xff0000\ns_cbranch_scc0 L_sp14_%=\nL_bk14_%=:\n"
"v_add_u32 v197, s29, v203\n"
"buffer_load_dwordx4 v[4:7], v197, s[52:55], s92 offen offset:0 sc1\n"
"buffer_load_dwordx4 v[12:15], v197, s[52:55], s93 offen offset:0 sc1\n"
"buffer_load_dwordx4 v[24:27], v197, s[52:55], s94 offen offset:0 sc1\n"
"buffer_load_dwordx4 v[32:35], v197, s[52:55], s95 offen offset:0 sc1\n"
"buffer_load_dwordx4 v[44:47], v197, s[52:55], s96 offen offset:0 sc1\n"
"buffer_load_dwordx4 v[52:55], v197, s[52:55], s97 offen offset:0 sc1\n"
"buffer_load_dwordx4 v[64:67], v197, s[52:55], s98 offen offset:0 sc1\n"
"s_mov_b32 s24, 0xffff0000\n"
"s_set_gpr_idx_on s89, 0x1\ns_nop 1\nv_mov_b32 v180, v0\nv_mov_b32 v181, v1\nv_mov_b32 v182, v2\nv_mov_b32 v183, v3\ns_set_gpr_idx_off\n"
"s_set_gpr_idx_on s90, 0x1\ns_nop 1\nv_mov_b32 v184, v0\nv_mov_b32 v185, v1\nv_mov_b32 v186, v2\nv_mov_b32 v187, v3\ns_set_gpr_idx_off\n"
"s_set_gpr_idx_on s91, 0x1\ns_nop 1\nv_mov_b32 v188, v0\nv_mov_b32 v189, v1\nv_mov_b32 v190, v2\nv_mov_b32 v191, v3\ns_set_gpr_idx_off\n"
"s_waitcnt vmcnt(7)\n"
"s_barrier\n"
"s_cmp_lg_u32 s19, 0\ns_cbranch_scc1 L_sg15_%=\ns_mov_b64 exec, 0x1fe\nbuffer_store_dword v253, v206, s[68:71], 0 offen offset:64 sc1\ns_mov_b64 exec, -1\nL_sg15_%=:\n"
"v_lshlrev_b32 v152, 16, v180\n"
"v_and_b32 v153, s24, v180\n"
"v_lshlrev_b32 v154, 16, v181\n"
"v_and_b32 v155, s24, v181\n"
"v_lshlrev_b32 v144, 16, v182\n"
"v_and_b32 v145, s24, v182\n"
"v_lshlrev_b32 v146, 16, v183\n"
"v_and_b32 v147, s24, v183\n"
"v_lshlrev_b32 v192, 16, v220\n"
"v_and_b32 v193, s24, v220\n"
"v_pk_add_f32 v[152:153], v[152:153], v[192:193]\n"
"v_lshlrev_b32 v194, 16, v221\n"
"v_and_b32 v195, s24, v221\n"
"v_pk_add_f32 v[154:155], v[154:155], v[194:195]\n"
"v_lshlrev_b32 v192, 16, v222\n"
"v_and_b32 v193, s24, v222\n"
"v_pk_add_f32 v[144:145], v[144:145], v[192:193]\n"
"v_lshlrev_b32 v194, 16, v223\n"
"v_and_b32 v195, s24, v223\n"
"v_pk_add_f32 v[146:147], v[146:147], v[194:195]\n"
"v_lshlrev_b32 v192, 16, v224\n"
"v_and_b32 v193, s24, v224\n"
"v_pk_add_f32 v[152:153], v[152:153], v[192:193]\n"
"v_lshlrev_b32 v194, 16, v225\n"
"v_and_b32 v195, s24, v225\n"
"v_pk_add_f32 v[154:155], v[154:155], v[194:195]\n"
"v_lshlrev_b32 v192, 16, v226\n"
"v_and_b32 v193, s24, v226\n"
"v_pk_add_f32 v[144:145], v[144:145], v[192:193]\n"
"v_lshlrev_b32 v194, 16, v227\n"
"v_and_b32 v195, s24, v227\n"
"v_pk_add_f32 v[146:147], v[146:147], v[194:195]\n"
"v_lshlrev_b32 v192, 16, v228\n"
"v_and_b32 v193, s24, v228\n"
"v_pk_add_f32 v[152:153], v[152:153], v[192:193]\n"
"v_lshlrev_b32 v194, 16, v229\n"
"v_and_b32 v195, s24, v229\n"
"v_pk_add_f32 v[154:155], v[154:155], v[194:195]\n"
"v_lshlrev_b32 v192, 16, v230\n"
"v_and_b32 v193, s24, v230\n"
"v_pk_add_f32 v[144:145], v[144:145], v[192:193]\n"
"v_lshlrev_b32 v194, 16, v231\n"
"v_and_b32 v195, s24, v231\n"
"v_pk_add_f32 v[146:147], v[146:147], v[194:195]\n"
"v_lshlrev_b32 v192, 16, v232\n"
"v_and_b32 v193, s24, v232\n"
"v_pk_add_f32 v[152:153], v[152:153], v[192:193]\n"
"v_lshlrev_b32 v194, 16, v233\n"
"v_and_b32 v195, s24, v233\n"
"v_pk_add_f32 v[154:155], v[154:155], v[194:195]\n"
"v_lshlrev_b32 v192, 16, v234\n"
"v_and_b32 v193, s24, v234\n"
"v_pk_add_f32 v[144:145], v[144:145], v[192:193]\n"
"v_lshlrev_b32 v194, 16, v235\n"
"v_and_b32 v195, s24, v235\n"
"v_pk_add_f32 v[146:147], v[146:147], v[194:195]\n"
"v_lshlrev_b32 v192, 16, v236\n"
"v_and_b32 v193, s24, v236\n"
"v_pk_add_f32 v[152:153], v[152:153], v[192:193]\n"
"v_lshlrev_b32 v194, 16, v237\n"
"v_and_b32 v195, s24, v237\n"
"v_pk_add_f32 v[154:155], v[154:155], v[194:195]\n"
"v_lshlrev_b32 v192, 16, v238\n"
"v_and_b32 v193, s24, v238\n"
"v_pk_add_f32 v[144:145], v[144:145], v[192:193]\n"
"v_lshlrev_b32 v194, 16, v239\n"
"v_and_b32 v195, s24, v239\n"
"v_pk_add_f32 v[146:147], v[146:147], v[194:195]\n"
"v_lshlrev_b32 v192, 16, v240\n"
"v_and_b32 v193, s24, v240\n"
"v_pk_add_f32 v[152:153], v[152:153], v[192:193]\n"
"v_lshlrev_b32 v194, 16, v241\n"
"v_and_b32 v195, s24, v241\n"
"v_pk_add_f32 v[154:155], v[154:155], v[194:195]\n"
"v_lshlrev_b32 v192, 16, v242\n"
"v_and_b32 v193, s24, v242\n"
"v_pk_add_f32 v[144:145], v[144:145], v[192:193]\n"
"v_lshlrev_b32 v194, 16, v243\n"
"v_and_b32 v195, s24, v243\n"
"v_pk_add_f32 v[146:147], v[146:147], v[194:195]\n"
"v_lshlrev_b32 v192, 16, v244\n"
"v_and_b32 v193, s24, v244\n"
"v_pk_add_f32 v[152:153], v[152:153], v[192:193]\n"
"v_lshlrev_b32 v194, 16, v245\n"
"v_and_b32 v195, s24, v245\n"
"v_pk_add_f32 v[154:155], v[154:155], v[194:195]\n"
"v_lshlrev_b32 v192, 16, v246\n"
"v_and_b32 v193, s24, v246\n"
"v_pk_add_f32 v[144:145], v[144:145], v[192:193]\n"
"v_lshlrev_b32 v194, 16, v247\n"
"v_and_b32 v195, s24, v247\n"
"v_pk_add_f32 v[146:147], v[146:147], v[194:195]\n"
"v_cvt_pk_bf16_f32 v180, v152, v153\n"
"v_cvt_pk_bf16_f32 v181, v154, v155\n"
"v_cvt_pk_bf16_f32 v182, v144, v145\n"
"v_cvt_pk_bf16_f32 v183, v146, v147\n"
"buffer_load_dword v212, v254, s[68:71], 0 offen offset:0 sc1\n"
"s_waitcnt vmcnt(1)\n"
"v_lshlrev_b32 v152, 16, v184\n"
"v_and_b32 v153, s24, v184\n"
"v_lshlrev_b32 v154, 16, v185\n"
"v_and_b32 v155, s24, v185\n"
"v_lshlrev_b32 v144, 16, v186\n"
"v_and_b32 v145, s24, v186\n"
"v_lshlrev_b32 v146, 16, v187\n"
"v_and_b32 v147, s24, v187\n"
"v_lshlrev_b32 v192, 16, v4\n"
"v_and_b32 v193, s24, v4\n"
"v_pk_add_f32 v[152:153], v[152:153], v[192:193]\n"
"v_lshlrev_b32 v194, 16, v5\n"
"v_and_b32 v195, s24, v5\n"
"v_pk_add_f32 v[154:155], v[154:155], v[194:195]\n"
"v_lshlrev_b32 v192, 16, v6\n"
"v_and_b32 v193, s24, v6\n"
"v_pk_add_f32 v[144:145], v[144:145], v[192:193]\n"
"v_lshlrev_b32 v194, 16, v7\n"
"v_and_b32 v195, s24, v7\n"
"v_pk_add_f32 v[146:147], v[146:147], v[194:195]\n"
"v_lshlrev_b32 v192, 16, v12\n"
"v_and_b32 v193, s24, v12\n"
"v_pk_add_f32 v[152:153], v[152:153], v[192:193]\n"
"v_lshlrev_b32 v194, 16, v13\n"
"v_and_b32 v195, s24, v13\n"
"v_pk_add_f32 v[154:155], v[154:155], v[194:195]\n"
"v_lshlrev_b32 v192, 16, v14\n"
"v_and_b32 v193, s24, v14\n"
"v_pk_add_f32 v[144:145], v[144:145], v[192:193]\n"
"v_lshlrev_b32 v194, 16, v15\n"
"v_and_b32 v195, s24, v15\n"
"v_pk_add_f32 v[146:147], v[146:147], v[194:195]\n"
"v_lshlrev_b32 v192, 16, v24\n"
"v_and_b32 v193, s24, v24\n"
"v_pk_add_f32 v[152:153], v[152:153], v[192:193]\n"
"v_lshlrev_b32 v194, 16, v25\n"
"v_and_b32 v195, s24, v25\n"
"v_pk_add_f32 v[154:155], v[154:155], v[194:195]\n"
"v_lshlrev_b32 v192, 16, v26\n"
"v_and_b32 v193, s24, v26\n"
"v_pk_add_f32 v[144:145], v[144:145], v[192:193]\n"
"v_lshlrev_b32 v194, 16, v27\n"
"v_and_b32 v195, s24, v27\n"
"v_pk_add_f32 v[146:147], v[146:147], v[194:195]\n"
"v_lshlrev_b32 v192, 16, v32\n"
"v_and_b32 v193, s24, v32\n"
"v_pk_add_f32 v[152:153], v[152:153], v[192:193]\n"
"v_lshlrev_b32 v194, 16, v33\n"
"v_and_b32 v195, s24, v33\n"
"v_pk_add_f32 v[154:155], v[154:155], v[194:195]\n"
"v_lshlrev_b32 v192, 16, v34\n"
"v_and_b32 v193, s24, v34\n"
"v_pk_add_f32 v[144:145], v[144:145], v[192:193]\n"
"v_lshlrev_b32 v194, 16, v35\n"
"v_and_b32 v195, s24, v35\n"
"v_pk_add_f32 v[146:147], v[146:147], v[194:195]\n"
"v_lshlrev_b32 v192, 16, v44\n"
"v_and_b32 v193, s24, v44\n"
"v_pk_add_f32 v[152:153], v[152:153], v[192:193]\n"
"v_lshlrev_b32 v194, 16, v45\n"
"v_and_b32 v195, s24, v45\n"
"v_pk_add_f32 v[154:155], v[154:155], v[194:195]\n"
"v_lshlrev_b32 v192, 16, v46\n"
"v_and_b32 v193, s24, v46\n"
"v_pk_add_f32 v[144:145], v[144:145], v[192:193]\n"
"v_lshlrev_b32 v194, 16, v47\n"
"v_and_b32 v195, s24, v47\n"
"v_pk_add_f32 v[146:147], v[146:147], v[194:195]\n"
"v_lshlrev_b32 v192, 16, v52\n"
"v_and_b32 v193, s24, v52\n"
"v_pk_add_f32 v[152:153], v[152:153], v[192:193]\n"
"v_lshlrev_b32 v194, 16, v53\n"
"v_and_b32 v195, s24, v53\n"
"v_pk_add_f32 v[154:155], v[154:155], v[194:195]\n"
"v_lshlrev_b32 v192, 16, v54\n"
"v_and_b32 v193, s24, v54\n"
"v_pk_add_f32 v[144:145], v[144:145], v[192:193]\n"
"v_lshlrev_b32 v194, 16, v55\n"
"v_and_b32 v195, s24, v55\n"
"v_pk_add_f32 v[146:147], v[146:147], v[194:195]\n"
"v_lshlrev_b32 v192, 16, v64\n"
"v_and_b32 v193, s24, v64\n"
"v_pk_add_f32 v[152:153], v[152:153], v[192:193]\n"
"v_lshlrev_b32 v194, 16, v65\n"
"v_and_b32 v195, s24, v65\n"
"v_pk_add_f32 v[154:155], v[154:155], v[194:195]\n"
"v_lshlrev_b32 v192, 16, v66\n"
"v_and_b32 v193, s24, v66\n"
"v_pk_add_f32 v[144:145], v[144:145], v[192:193]\n"
"v_lshlrev_b32 v194, 16, v67\n"
"v_and_b32 v195, s24, v67\n"
"v_pk_add_f32 v[146:147], v[146:147], v[194:195]\n"
"v_cvt_pk_bf16_f32 v184, v152, v153\n"
"v_cvt_pk_bf16_f32 v185, v154, v155\n"
"v_cvt_pk_bf16_f32 v186, v144, v145\n"
"v_cvt_pk_bf16_f32 v187, v146, v147\n"
"s_waitcnt vmcnt(0)\n"
"v_cmp_eq_u32 vcc, s80, v212\ns_nop 4\ns_and_b32 s20, vcc_lo, 0xff000000\ns_cmp_eq_u32 s20, 0xff000000\ns_cbranch_scc0 L_sp16_%=\nL_bk16_%=:\n"
"v_add_u32 v198, s30, v203\n"
"buffer_load_dwordx4 v[72:75], v198, s[52:55], s92 offen offset:0 sc1\n"
"buffer_load_dwordx4 v[84:87], v198, s[52:55], s93 offen offset:0 sc1\n"
"buffer_load_dwordx4 v[92:95], v198, s[52:55], s94 offen offset:0 sc1\n"
"buffer_load_dwordx4 v[104:107], v198, s[52:55], s95 offen offset:0 sc1\n"
"buffer_load_dwordx4 v[112:115], v198, s[52:55], s96 offen offset:0 sc1\n"
"buffer_load_dwordx4 v[124:127], v198, s[52:55], s97 offen offset:0 sc1\n"
"buffer_load_dwordx4 v[132:135], v198, s[52:55], s98 offen offset:0 sc1\n"
"v_add_u32 v220, s31, v205\n"
"v_add_u32 v221, s34, v205\n"
"buffer_store_dwordx2 v[180:181], v220, s[56:59], 0 offen offset:0 \n"
"buffer_store_dwordx2 v[182:183], v221, s[56:59], 0 offen offset:0 \n"
"v_add_u32 v222, s32, v205\n"
"v_add_u32 v223, s35, v205\n"
"buffer_store_dwordx2 v[184:185], v222, s[56:59], 0 offen offset:0 \n"
"buffer_store_dwordx2 v[186:187], v223, s[56:59], 0 offen offset:0 \n"
"s_waitcnt vmcnt(4)\n"
"v_lshlrev_b32 v152, 16, v188\n"
"v_and_b32 v153, s24, v188\n"
"v_lshlrev_b32 v154, 16, v189\n"
"v_and_b32 v155, s24, v189\n"
"v_lshlrev_b32 v144, 16, v190\n"
"v_and_b32 v145, s24, v190\n"
"v_lshlrev_b32 v146, 16, v191\n"
"v_and_b32 v147, s24, v191\n"
"v_lshlrev_b32 v192, 16, v72\n"
"v_and_b32 v193, s24, v72\n"
"v_pk_add_f32 v[152:153], v[152:153], v[192:193]\n"
"v_lshlrev_b32 v194, 16, v73\n"
"v_and_b32 v195, s24, v73\n"
"v_pk_add_f32 v[154:155], v[154:155], v[194:195]\n"
"v_lshlrev_b32 v192, 16, v74\n"
"v_and_b32 v193, s24, v74\n"
"v_pk_add_f32 v[144:145], v[144:145], v[192:193]\n"
"v_lshlrev_b32 v194, 16, v75\n"
"v_and_b32 v195, s24, v75\n"
"v_pk_add_f32 v[146:147], v[146:147], v[194:195]\n"
"v_lshlrev_b32 v192, 16, v84\n"
"v_and_b32 v193, s24, v84\n"
"v_pk_add_f32 v[152:153], v[152:153], v[192:193]\n"
"v_lshlrev_b32 v194, 16, v85\n"
"v_and_b32 v195, s24, v85\n"
"v_pk_add_f32 v[154:155], v[154:155], v[194:195]\n"
"v_lshlrev_b32 v192, 16, v86\n"
"v_and_b32 v193, s24, v86\n"
"v_pk_add_f32 v[144:145], v[144:145], v[192:193]\n"
"v_lshlrev_b32 v194, 16, v87\n"
"v_and_b32 v195, s24, v87\n"
"v_pk_add_f32 v[146:147], v[146:147], v[194:195]\n"
"v_lshlrev_b32 v192, 16, v92\n"
"v_and_b32 v193, s24, v92\n"
"v_pk_add_f32 v[152:153], v[152:153], v[192:193]\n"
"v_lshlrev_b32 v194, 16, v93\n"
"v_and_b32 v195, s24, v93\n"
"v_pk_add_f32 v[154:155], v[154:155], v[194:195]\n"
"v_lshlrev_b32 v192, 16, v94\n"
"v_and_b32 v193, s24, v94\n"
"v_pk_add_f32 v[144:145], v[144:145], v[192:193]\n"
"v_lshlrev_b32 v194, 16, v95\n"
"v_and_b32 v195, s24, v95\n"
"v_pk_add_f32 v[146:147], v[146:147], v[194:195]\n"
"v_lshlrev_b32 v192, 16, v104\n"
"v_and_b32 v193, s24, v104\n"
"v_pk_add_f32 v[152:153], v[152:153], v[192:193]\n"
"v_lshlrev_b32 v194, 16, v105\n"
"v_and_b32 v195, s24, v105\n"
"v_pk_add_f32 v[154:155], v[154:155], v[194:195]\n"
"v_lshlrev_b32 v192, 16, v106\n"
"v_and_b32 v193, s24, v106\n"
"v_pk_add_f32 v[144:145], v[144:145], v[192:193]\n"
"v_lshlrev_b32 v194, 16, v107\n"
"v_and_b32 v195, s24, v107\n"
"v_pk_add_f32 v[146:147], v[146:147], v[194:195]\n"
"v_lshlrev_b32 v192, 16, v112\n"
"v_and_b32 v193, s24, v112\n"
"v_pk_add_f32 v[152:153], v[152:153], v[192:193]\n"
"v_lshlrev_b32 v194, 16, v113\n"
"v_and_b32 v195, s24, v113\n"
"v_pk_add_f32 v[154:155], v[154:155], v[194:195]\n"
"v_lshlrev_b32 v192, 16, v114\n"
"v_and_b32 v193, s24, v114\n"
"v_pk_add_f32 v[144:145], v[144:145], v[192:193]\n"
"v_lshlrev_b32 v194, 16, v115\n"
"v_and_b32 v195, s24, v115\n"
"v_pk_add_f32 v[146:147], v[146:147], v[194:195]\n"
"v_lshlrev_b32 v192, 16, v124\n"
"v_and_b32 v193, s24, v124\n"
"v_pk_add_f32 v[152:153], v[152:153], v[192:193]\n"
"v_lshlrev_b32 v194, 16, v125\n"
"v_and_b32 v195, s24, v125\n"
"v_pk_add_f32 v[154:155], v[154:155], v[194:195]\n"
"v_lshlrev_b32 v192, 16, v126\n"
"v_and_b32 v193, s24, v126\n"
"v_pk_add_f32 v[144:145], v[144:145], v[192:193]\n"
"v_lshlrev_b32 v194, 16, v127\n"
"v_and_b32 v195, s24, v127\n"
"v_pk_add_f32 v[146:147], v[146:147], v[194:195]\n"
"v_lshlrev_b32 v192, 16, v132\n"
"v_and_b32 v193, s24, v132\n"
"v_pk_add_f32 v[152:153], v[152:153], v[192:193]\n"
"v_lshlrev_b32 v194, 16, v133\n"
"v_and_b32 v195, s24, v133\n"
"v_pk_add_f32 v[154:155], v[154:155], v[194:195]\n"
"v_lshlrev_b32 v192, 16, v134\n"
"v_and_b32 v193, s24, v134\n"
"v_pk_add_f32 v[144:145], v[144:145], v[192:193]\n"
"v_lshlrev_b32 v194, 16, v135\n"
"v_and_b32 v195, s24, v135\n"
"v_pk_add_f32 v[146:147], v[146:147], v[194:195]\n"
"v_cvt_pk_bf16_f32 v188, v152, v153\n"
"v_cvt_pk_bf16_f32 v189, v154, v155\n"
"v_cvt_pk_bf16_f32 v190, v144, v145\n"
"v_cvt_pk_bf16_f32 v191, v146, v147\n"
"v_add_u32 v224, s33, v205\n"
"v_add_u32 v225, s36, v205\n"
"buffer_store_dwordx2 v[188:189], v224, s[56:59], 0 offen offset:0 \n"
"buffer_store_dwordx2 v[190:191], v225, s[56:59], 0 offen offset:0 \n"
"s_branch L_end_%=\n"
"L_snd_%=:\n"
"v_mfma_f32_16x16x32_bf16 v[120:123], a[192:195], v[48:51], v[172:175]\n"
"s_mov_b32 s79, 0\n"
"v_cvt_pk_bf16_f32 v152, v128, v129\n"
"v_cvt_pk_bf16_f32 v153, v130, v131\n"
"v_mfma_f32_16x16x32_bf16 v[120:123], a[196:199], v[52:55], v[120:123]\n"
"v_cvt_pk_bf16_f32 v154, v132, v133\n"
"v_cvt_pk_bf16_f32 v155, v134, v135\n"
"v_mfma_f32_16x16x32_bf16 v[120:123], a[200:203], v[56:59], v[120:123]\n"
"v_cvt_pk_bf16_f32 v156, v136, v137\n"
"v_cvt_pk_bf16_f32 v157, v138, v139\n"
"v_mfma_f32_16x16x32_bf16 v[120:123], a[204:207], v[60:63], v[120:123]\n"
"v_exp_f32 v142, v142\n"
"v_mfma_f32_16x16x32_bf16 v[124:127], a[220:223], v[60:63], v[172:175]\n"
"v_exp_f32 v143, v143\n"
"v_mfma_f32_16x16x32_bf16 v[124:127], a[216:219], v[56:59], v[124:127]\n"
"v_add_f32 v182, v182, v142\n"
"v_add_f32 v182, v182, v143\n"
"v_mfma_f32_16x16x32_bf16 v[124:127], a[212:215], v[52:55], v[124:127]\n"
"v_cvt_pk_bf16_f32 v158, v140, v141\n"
"v_cvt_pk_bf16_f32 v159, v142, v143\n"
"v_mfma_f32_16x16x32_bf16 v[124:127], a[208:211], v[48:51], v[124:127]\n"
"v_exp_f32 v112, v112\n"
"v_mfma_f32_16x16x32_bf16 a[32:35], v[144:147], v[220:223], a[32:35]\n"
"v_exp_f32 v113, v113\n"
"v_mfma_f32_16x16x32_bf16 a[32:35], v[148:151], v[224:227], a[32:35]\n"
"v_exp_f32 v114, v114\n"
"v_mfma_f32_16x16x32_bf16 a[36:39], v[148:151], v[232:235], a[36:39]\n"
"v_exp_f32 v115, v115\n"
"v_mfma_f32_16x16x32_bf16 a[36:39], v[144:147], v[228:231], a[36:39]\n"
"v_exp_f32 v116, v116\n"
"v_mfma_f32_16x16x32_bf16 a[40:43], v[144:147], v[236:239], a[40:43]\n"
"v_exp_f32 v117, v117\n"
"v_mfma_f32_16x16x32_bf16 a[40:43], v[148:151], v[240:243], a[40:43]\n"
"v_exp_f32 v118, v118\n"
"v_mfma_f32_16x16x32_bf16 a[44:47], v[148:151], v[248:251], a[44:47]\n"
"v_exp_f32 v119, v119\n"
"v_mfma_f32_16x16x32_bf16 a[44:47], v[144:147], v[244:247], a[44:47]\n"
"v_exp_f32 v120, v120\n"
"v_mfma_f32_16x16x32_bf16 a[48:51], v[144:147], v[80:83], a[48:51]\n"
"v_exp_f32 v121, v121\n"
"v_mfma_f32_16x16x32_bf16 a[48:51], v[148:151], v[84:87], a[48:51]\n"
"v_exp_f32 v122, v122\n"
"v_mfma_f32_16x16x32_bf16 a[52:55], v[148:151], v[92:95], a[52:55]\n"
"v_exp_f32 v123, v123\n"
"v_mfma_f32_16x16x32_bf16 a[52:55], v[144:147], v[88:91], a[52:55]\n"
"v_exp_f32 v124, v124\n"
"v_mfma_f32_16x16x32_bf16 a[56:59], v[144:147], v[96:99], a[56:59]\n"
"v_exp_f32 v125, v125\n"
"v_mfma_f32_16x16x32_bf16 a[56:59], v[148:151], v[100:103], a[56:59]\n"
"v_exp_f32 v126, v126\n"
"v_mfma_f32_16x16x32_bf16 a[60:63], v[148:151], v[108:111], a[60:63]\n"
"v_exp_f32 v127, v127\n"
"v_mfma_f32_16x16x32_bf16 a[60:63], v[144:147], v[104:107], a[60:63]\n"
"v_cvt_pk_bf16_f32 v144, v112, v113\n"
"v_cvt_pk_bf16_f32 v145, v114, v115\n"
"v_mfma_f32_16x16x32_bf16 v[128:131], a[160:163], v[64:67], v[176:179]\n"
"v_cvt_pk_bf16_f32 v146, v116, v117\n"
"v_cvt_pk_bf16_f32 v147, v118, v119\n"
"v_mfma_f32_16x16x32_bf16 v[128:131], a[164:167], v[68:71], v[128:131]\n"
"v_cvt_pk_bf16_f32 v148, v120, v121\n"
"v_cvt_pk_bf16_f32 v149, v122, v123\n"
"v_mfma_f32_16x16x32_bf16 v[128:131], a[168:171], v[72:75], v[128:131]\n"
"v_cvt_pk_bf16_f32 v150, v124, v125\n"
"v_cvt_pk_bf16_f32 v151, v126, v127\n"
"v_mfma_f32_16x16x32_bf16 v[128:131], a[172:175], v[76:79], v[128:131]\n"
"v_mov_b32 v20, v180\n"
"v_and_b32 v217, 15, v213\n"
"v_mfma_f32_16x16x32_bf16 v[132:135], a[188:191], v[76:79], v[176:179]\n"
"v_permlane16_swap_b32 v180, v20\n"
"v_mfma_f32_16x16x32_bf16 v[132:135], a[184:187], v[72:75], v[132:135]\n"
"v_add_f32 v180, v180, v20\n"
"v_mov_b32 v20, v180\n"
"v_mfma_f32_16x16x32_bf16 v[132:135], a[180:183], v[68:71], v[132:135]\n"
"s_nop 0\n"
"v_permlane32_swap_b32 v180, v20\n"
"v_mfma_f32_16x16x32_bf16 v[132:135], a[176:179], v[64:67], v[132:135]\n"
"v_add_f32 v180, v180, v20\n"
"v_lshrrev_b32 v214, 4, v213\n"
"v_mfma_f32_16x16x32_bf16 v[136:139], a[192:195], v[64:67], v[176:179]\n"
"v_rcp_f32 v20, v180\n"
"v_mfma_f32_16x16x32_bf16 v[136:139], a[196:199], v[68:71], v[136:139]\n"
"v_lshl_add_u32 v208, v217, 2, s89\n"
"v_lshl_add_u32 v209, v214, 4, s89\n"
"v_mfma_f32_16x16x32_bf16 v[136:139], a[200:203], v[72:75], v[136:139]\n"
"ds_write_b32 v208, v20 offset:0\n"
"v_mov_b32 v21, v181\n"
"v_mfma_f32_16x16x32_bf16 v[136:139], a[204:207], v[76:79], v[136:139]\n"
"ds_read_b128 v[0:3], v209 offset:0\n"
"v_add_f32 v183, v183, v112\n"
"v_mfma_f32_16x16x32_bf16 v[140:143], a[220:223], v[76:79], v[176:179]\n"
"v_exp_f32 v128, v128\n"
"v_mfma_f32_16x16x32_bf16 v[140:143], a[216:219], v[72:75], v[140:143]\n"
"v_exp_f32 v129, v129\n"
"v_mfma_f32_16x16x32_bf16 v[140:143], a[212:215], v[68:71], v[140:143]\n"
"v_exp_f32 v130, v130\n"
"v_mfma_f32_16x16x32_bf16 v[140:143], a[208:211], v[64:67], v[140:143]\n"
"v_exp_f32 v131, v131\n"
"v_mfma_f32_16x16x32_bf16 a[64:67], v[152:155], v[220:223], a[64:67]\n"
"v_exp_f32 v132, v132\n"
"v_mfma_f32_16x16x32_bf16 a[64:67], v[156:159], v[224:227], a[64:67]\n"
"v_exp_f32 v133, v133\n"
"v_mfma_f32_16x16x32_bf16 a[68:71], v[156:159], v[232:235], a[68:71]\n"
"v_exp_f32 v134, v134\n"
"v_mfma_f32_16x16x32_bf16 a[68:71], v[152:155], v[228:231], a[68:71]\n"
"v_exp_f32 v135, v135\n"
"v_mfma_f32_16x16x32_bf16 a[72:75], v[152:155], v[236:239], a[72:75]\n"
"v_add_f32 v183, v183, v113\n"
"v_add_f32 v183, v183, v114\n"
"v_mfma_f32_16x16x32_bf16 a[72:75], v[156:159], v[240:243], a[72:75]\n"
"v_exp_f32 v136, v136\n"
"v_mfma_f32_16x16x32_bf16 a[76:79], v[156:159], v[248:251], a[76:79]\n"
"v_exp_f32 v137, v137\n"
"v_mfma_f32_16x16x32_bf16 a[76:79], v[152:155], v[244:247], a[76:79]\n"
"v_exp_f32 v138, v138\n"
"v_mfma_f32_16x16x32_bf16 a[80:83], v[152:155], v[80:83], a[80:83]\n"
"v_exp_f32 v139, v139\n"
"v_mfma_f32_16x16x32_bf16 a[80:83], v[156:159], v[84:87], a[80:83]\n"
"v_exp_f32 v140, v140\n"
"v_mfma_f32_16x16x32_bf16 a[84:87], v[156:159], v[92:95], a[84:87]\n"
"v_exp_f32 v141, v141\n"
"v_mfma_f32_16x16x32_bf16 a[84:87], v[152:155], v[88:91], a[84:87]\n"
"v_exp_f32 v142, v142\n"
"v_mfma_f32_16x16x32_bf16 a[88:91], v[152:155], v[96:99], a[88:91]\n"
"v_exp_f32 v143, v143\n"
"v_mfma_f32_16x16x32_bf16 a[88:91], v[156:159], v[100:103], a[88:91]\n"
"v_permlane16_swap_b32 v181, v21\n"
"v_mfma_f32_16x16x32_bf16 a[92:95], v[156:159], v[108:111], a[92:95]\n"
"v_add_f32 v183, v183, v115\n"
"v_cvt_pk_bf16_f32 v156, v136, v137\n"
"v_mfma_f32_16x16x32_bf16 a[92:95], v[152:155], v[104:107], a[92:95]\n"
"v_cvt_pk_bf16_f32 v152, v128, v129\n"
"v_cvt_pk_bf16_f32 v153, v130, v131\n"
"v_mfma_f32_16x16x32_bf16 a[96:99], v[144:147], v[220:223], a[96:99]\n"
"v_cvt_pk_bf16_f32 v154, v132, v133\n"
"v_cvt_pk_bf16_f32 v155, v134, v135\n"
"v_mfma_f32_16x16x32_bf16 a[96:99], v[148:151], v[224:227], a[96:99]\n"
"v_cvt_pk_bf16_f32 v157, v138, v139\n"
"v_cvt_pk_bf16_f32 v158, v140, v141\n"
"v_mfma_f32_16x16x32_bf16 a[100:103], v[148:151], v[232:235], a[100:103]\n"
"v_cvt_pk_bf16_f32 v159, v142, v143\n"
"v_add_f32 v181, v181, v21\n"
"v_mfma_f32_16x16x32_bf16 a[100:103], v[144:147], v[228:231], a[100:103]\n"
"v_add_f32 v183, v183, v116\n"
"v_mov_b32 v21, v181\n"
"v_mfma_f32_16x16x32_bf16 a[104:107], v[144:147], v[236:239], a[104:107]\n"
"v_add_f32 v183, v183, v117\n"
"v_add_f32 v183, v183, v118\n"
"v_mfma_f32_16x16x32_bf16 a[104:107], v[148:151], v[240:243], a[104:107]\n"
"v_add_f32 v183, v183, v119\n"
"v_mov_b32 v22, v182\n"
"v_mfma_f32_16x16x32_bf16 a[108:111], v[148:151], v[248:251], a[108:111]\n"
"v_permlane32_swap_b32 v181, v21\n"
"v_mfma_f32_16x16x32_bf16 a[108:111], v[144:147], v[244:247], a[108:111]\n"
"v_add_f32 v183, v183, v120\n"
"v_add_f32 v184, v184, v128\n"
"v_mfma_f32_16x16x32_bf16 a[112:115], v[144:147], v[80:83], a[112:115]\n"
"v_add_f32 v181, v181, v21\n"
"v_add_f32 v183, v183, v121\n"
"v_mfma_f32_16x16x32_bf16 a[112:115], v[148:151], v[84:87], a[112:115]\n"
"v_add_f32 v184, v184, v129\n"
"v_add_f32 v183, v183, v122\n"
"v_mfma_f32_16x16x32_bf16 a[116:119], v[148:151], v[92:95], a[116:119]\n"
"v_rcp_f32 v21, v181\n"
"v_mfma_f32_16x16x32_bf16 a[116:119], v[144:147], v[88:91], a[116:119]\n"
"ds_write_b32 v208, v21 offset:64\n"
"v_add_f32 v184, v184, v130\n"
"v_mfma_f32_16x16x32_bf16 a[120:123], v[144:147], v[96:99], a[120:123]\n"
"ds_read_b128 v[4:7], v209 offset:64\n"
"v_add_f32 v183, v183, v123\n"
"v_mfma_f32_16x16x32_bf16 a[120:123], v[148:151], v[100:103], a[120:123]\n"
"v_permlane16_swap_b32 v182, v22\n"
"v_mfma_f32_16x16x32_bf16 a[124:127], v[148:151], v[108:111], a[124:127]\n"
"v_add_f32 v184, v184, v131\n"
"v_add_f32 v182, v182, v22\n"
"v_mfma_f32_16x16x32_bf16 a[124:127], v[144:147], v[104:107], a[124:127]\n"
"v_add_f32 v183, v183, v124\n"
"v_add_f32 v184, v184, v132\n"
"v_mfma_f32_16x16x32_bf16 a[128:131], v[152:155], v[220:223], a[128:131]\n"
"v_mov_b32 v22, v182\n"
"v_add_f32 v183, v183, v125\n"
"v_mfma_f32_16x16x32_bf16 a[128:131], v[156:159], v[224:227], a[128:131]\n"
"v_add_f32 v184, v184, v133\n"
"v_add_f32 v183, v183, v126\n"
"v_mfma_f32_16x16x32_bf16 a[132:135], v[156:159], v[232:235], a[132:135]\n"
"v_add_f32 v184, v184, v134\n"
"v_bfe_u32 v215, v213, 2, 2\n"
"v_mfma_f32_16x16x32_bf16 a[132:135], v[152:155], v[228:231], a[132:135]\n"
"v_add_f32 v183, v183, v127\n"
"v_add_f32 v184, v184, v135\n"
"v_mfma_f32_16x16x32_bf16 a[136:139], v[152:155], v[236:239], a[136:139]\n"
"v_xor_b32 v218, v214, v215\n"
"v_permlane32_swap_b32 v182, v22\n"
"v_mfma_f32_16x16x32_bf16 a[136:139], v[156:159], v[240:243], a[136:139]\n"
"v_mov_b32 v23, v183\n"
"v_add_f32 v184, v184, v136\n"
"v_mfma_f32_16x16x32_bf16 a[140:143], v[156:159], v[248:251], a[140:143]\n"
"v_lshlrev_b32 v218, 3, v218\n"
"v_add_f32 v182, v182, v22\n"
"v_mfma_f32_16x16x32_bf16 a[140:143], v[152:155], v[244:247], a[140:143]\n"
"v_add_f32 v184, v184, v137\n"
"v_lshl_add_u32 v218, v217, 5, v218\n"
"v_mfma_f32_16x16x32_bf16 a[144:147], v[152:155], v[80:83], a[144:147]\n"
"v_rcp_f32 v22, v182\n"
"v_mfma_f32_16x16x32_bf16 a[144:147], v[156:159], v[84:87], a[144:147]\n"
"ds_write_b32 v208, v22 offset:128\n"
"v_add_f32 v184, v184, v138\n"
"v_mfma_f32_16x16x32_bf16 a[148:151], v[156:159], v[92:95], a[148:151]\n"
"ds_read_b128 v[8:11], v209 offset:128\n"
"v_and_b32 v216, 3, v213\n"
"v_mfma_f32_16x16x32_bf16 a[148:151], v[152:155], v[88:91], a[148:151]\n"
"v_add_u32 v207, s90, v218\n"
"v_add3_u32 v204, s91, v218, 0\n"
"v_mfma_f32_16x16x32_bf16 a[152:155], v[152:155], v[96:99], a[152:155]\n"
"v_permlane16_swap_b32 v183, v23\n"
"v_mfma_f32_16x16x32_bf16 a[152:155], v[156:159], v[100:103], a[152:155]\n"
"v_add_f32 v184, v184, v139\n"
"v_xor_b32 v218, v216, v214\n"
"v_mfma_f32_16x16x32_bf16 a[156:159], v[156:159], v[108:111], a[156:159]\n"
"v_add_f32 v183, v183, v23\n"
"v_add_f32 v184, v184, v140\n"
"v_mfma_f32_16x16x32_bf16 a[156:159], v[152:155], v[104:107], a[156:159]\n"
"v_lshlrev_b32 v218, 3, v218\n"
"v_mov_b32 v23, v183\n"
"v_add_f32 v184, v184, v141\n"
"v_lshl_add_u32 v218, v215, 5, v218\n"
"v_add_f32 v184, v184, v142\n"
"v_lshl_add_u32 v218, v214, 7, v218\n"
"v_add_f32 v184, v184, v143\n"
"v_add_u32 v202, 0x18000, v218\n"
"v_permlane32_swap_b32 v183, v23\n"
"v_mov_b32 v24, v184\n"
"v_lshlrev_b32 v218, 3, v214\n"
"v_add_f32 v183, v183, v23\n"
"v_mad_u32_u24 v205, v217, s93, v218\n"
"v_rcp_f32 v23, v183\n"
"v_lshl_add_u32 v217, v214, 2, v215\n"
"v_permlane16_swap_b32 v184, v24\n"
"v_lshlrev_b32 v218, 7, v217\n"
"v_and_b32 v219, 6, v217\n"
"v_accvgpr_read_b32 v32, a0\n"
"v_accvgpr_read_b32 v33, a1\n"
"ds_write_b32 v208, v23 offset:192\n"
"v_add_f32 v184, v184, v24\n"
"v_and_b32 v217, 1, v216\n"
"v_accvgpr_read_b32 v34, a2\n"
"v_accvgpr_read_b32 v35, a3\n"
"s_waitcnt lgkmcnt(5)\n"
"v_mul_f32 v32, v32, v0\n"
"v_mul_f32 v33, v33, v1\n"
"v_accvgpr_read_b32 v40, a8\n"
"v_accvgpr_read_b32 v41, a9\n"
"ds_read_b128 v[12:15], v209 offset:192\n"
"v_mov_b32 v24, v184\n"
"v_lshl_add_u32 v218, v217, 3, v218\n"
"v_accvgpr_read_b32 v36, a4\n"
"v_accvgpr_read_b32 v37, a5\n"
"v_accvgpr_read_b32 v38, a6\n"
"v_accvgpr_read_b32 v39, a7\n"
"v_mul_f32 v34, v34, v2\n"
"v_mul_f32 v35, v35, v3\n"
"v_cvt_pk_bf16_f32 v32, v32, v33\n"
"v_accvgpr_read_b32 v42, a10\n"
"v_accvgpr_read_b32 v43, a11\n"
"v_mul_f32 v40, v40, v0\n"
"v_mul_f32 v41, v41, v1\n"
"v_lshrrev_b32 v217, 1, v216\n"
"v_mul_f32 v36, v36, v0\n"
"v_mul_f32 v37, v37, v1\n"
"v_mul_f32 v38, v38, v2\n"
"v_mul_f32 v39, v39, v3\n"
"v_cvt_pk_bf16_f32 v33, v34, v35\n"
"v_accvgpr_read_b32 v44, a12\n"
"v_accvgpr_read_b32 v45, a13\n"
"v_accvgpr_read_b32 v46, a14\n"
"v_accvgpr_read_b32 v47, a15\n"
"v_mul_f32 v42, v42, v2\n"
"v_mul_f32 v43, v43, v3\n"
"v_cvt_pk_bf16_f32 v40, v40, v41\n"
"v_lshlrev_b32 v203, 4, v213\n"
"v_xor_b32 v219, v219, v217\n"
"v_cvt_pk_bf16_f32 v34, v36, v37\n"
"v_cvt_pk_bf16_f32 v35, v38, v39\n"
"v_mul_f32 v44, v44, v0\n"
"v_mul_f32 v45, v45, v1\n"
"v_mul_f32 v46, v46, v2\n"
"v_mul_f32 v47, v47, v3\n"
"v_cvt_pk_bf16_f32 v41, v42, v43\n"
"v_permlane32_swap_b32 v184, v24\n"
"v_xor_b32 v219, s92, v219\n"
"buffer_store_dwordx4 v[32:35], v203, s[44:47], s79 offen offset:0 sc1\n"
"v_cvt_pk_bf16_f32 v42, v44, v45\n"
"v_cvt_pk_bf16_f32 v43, v46, v47\n"
"v_add_f32 v184, v184, v24\n"
"v_lshl_add_u32 v200, v219, 4, v218\n"
"buffer_store_dwordx4 v[40:43], v203, s[44:47], s79 offen offset:1024 sc1\n"
"v_rcp_f32 v24, v184\n"
"ds_read_b64_tr_b16 a[160:161], v200 offset:0\n"
"ds_read_b64_tr_b16 a[162:163], v200 offset:2048\n"
"ds_read_b64_tr_b16 a[164:165], v200 offset:4096\n"
"ds_read_b64_tr_b16 a[166:167], v200 offset:6144\n"
"ds_read_b64_tr_b16 a[168:169], v200 offset:8192\n"
"ds_read_b64_tr_b16 a[170:171], v200 offset:10240\n"
"ds_read_b64_tr_b16 a[172:173], v200 offset:12288\n"
"ds_read_b64_tr_b16 a[174:175], v200 offset:14336\n"
"ds_read_b64_tr_b16 a[176:177], v200 offset:16384\n"
"ds_read_b64_tr_b16 a[178:179], v200 offset:18432\n"
"ds_read_b64_tr_b16 a[180:181], v200 offset:20480\n"
"ds_read_b64_tr_b16 a[182:183], v200 offset:22528\n"
"ds_read_b64_tr_b16 a[184:185], v200 offset:24576\n"
"ds_read_b64_tr_b16 a[186:187], v200 offset:26624\n"
"ds_read_b64_tr_b16 a[188:189], v200 offset:28672\n"
"ds_read_b64_tr_b16 a[190:191], v200 offset:30720\n"
"ds_read_b64_tr_b16 a[192:193], v200 offset:32768\n"
"ds_read_b64_tr_b16 a[194:195], v200 offset:34816\n"
"ds_read_b64_tr_b16 a[196:197], v200 offset:36864\n"
"ds_read_b64_tr_b16 a[198:199], v200 offset:38912\n"
"ds_read_b64_tr_b16 a[200:201], v200 offset:40960\n"
"ds_read_b64_tr_b16 a[202:203], v200 offset:43008\n"
"ds_read_b64_tr_b16 a[204:205], v200 offset:45056\n"
"ds_read_b64_tr_b16 a[206:207], v200 offset:47104\n"
"ds_read_b64_tr_b16 a[208:209], v200 offset:49152\n"
"ds_read_b64_tr_b16 a[210:211], v200 offset:51200\n"
"ds_read_b64_tr_b16 a[212:213], v200 offset:53248\n"
"ds_read_b64_tr_b16 a[214:215], v200 offset:55296\n"
"ds_read_b64_tr_b16 a[216:217], v200 offset:57344\n"
"ds_read_b64_tr_b16 a[218:219], v200 offset:59392\n"
"ds_read_b64_tr_b16 a[220:221], v200 offset:61440\n"
"ds_read_b64_tr_b16 a[222:223], v200 offset:63488\n"
"v_accvgpr_read_b32 v32, a16\n"
"v_accvgpr_read_b32 v33, a17\n"
"ds_write_b32 v208, v24 offset:256\n"
"v_accvgpr_read_b32 v34, a18\n"
"v_accvgpr_read_b32 v35, a19\n"
"v_mul_f32 v32, v32, v0\n"
"v_mul_f32 v33, v33, v1\n"
"v_accvgpr_read_b32 v40, a24\n"
"v_accvgpr_read_b32 v41, a25\n"
"ds_read_b128 v[16:19], v209 offset:256\n"
"v_accvgpr_read_b32 v36, a20\n"
"v_accvgpr_read_b32 v37, a21\n"
"v_accvgpr_read_b32 v38, a22\n"
"v_accvgpr_read_b32 v39, a23\n"
"v_mul_f32 v34, v34, v2\n"
"v_mul_f32 v35, v35, v3\n"
"v_cvt_pk_bf16_f32 v32, v32, v33\n"
"v_accvgpr_read_b32 v42, a26\n"
"v_accvgpr_read_b32 v43, a27\n"
"v_mul_f32 v40, v40, v0\n"
"v_mul_f32 v41, v41, v1\n"
"v_mul_f32 v36, v36, v0\n"
"v_mul_f32 v37, v37, v1\n"
"v_mul_f32 v38, v38, v2\n"
"v_mul_f32 v39, v39, v3\n"
"v_cvt_pk_bf16_f32 v33, v34, v35\n"
"v_accvgpr_read_b32 v44, a28\n"
"v_accvgpr_read_b32 v45, a29\n"
"v_accvgpr_read_b32 v46, a30\n"
"v_accvgpr_read_b32 v47, a31\n"
"v_mul_f32 v42, v42, v2\n"
"v_mul_f32 v43, v43, v3\n"
"v_cvt_pk_bf16_f32 v40, v40, v41\n"
"v_cvt_pk_bf16_f32 v34, v36, v37\n"
"v_cvt_pk_bf16_f32 v35, v38, v39\n"
"v_mul_f32 v44, v44, v0\n"
"v_mul_f32 v45, v45, v1\n"
"v_mul_f32 v46, v46, v2\n"
"v_mul_f32 v47, v47, v3\n"
"v_cvt_pk_bf16_f32 v41, v42, v43\n"
"buffer_store_dwordx4 v[32:35], v203, s[44:47], s79 offen offset:2048 sc1\n"
"v_cvt_pk_bf16_f32 v42, v44, v45\n"
"v_cvt_pk_bf16_f32 v43, v46, v47\n"
"buffer_store_dwordx4 v[40:43], v203, s[44:47], s79 offen offset:3072 sc1\n"
"v_accvgpr_read_b32 v48, a32\n"
"v_accvgpr_read_b32 v49, a33\n"
"v_accvgpr_read_b32 v50, a34\n"
"v_accvgpr_read_b32 v51, a35\n"
"s_waitcnt lgkmcnt(15)\n"
"v_mul_f32 v48, v48, v4\n"
"v_mul_f32 v49, v49, v5\n"
"v_accvgpr_read_b32 v56, a40\n"
"v_accvgpr_read_b32 v57, a41\n"
"v_accvgpr_read_b32 v52, a36\n"
"v_accvgpr_read_b32 v53, a37\n"
"v_accvgpr_read_b32 v54, a38\n"
"v_accvgpr_read_b32 v55, a39\n"
"v_mul_f32 v50, v50, v6\n"
"v_mul_f32 v51, v51, v7\n"
"v_cvt_pk_bf16_f32 v48, v48, v49\n"
"v_accvgpr_read_b32 v58, a42\n"
"v_accvgpr_read_b32 v59, a43\n"
"v_mul_f32 v56, v56, v4\n"
"v_mul_f32 v57, v57, v5\n"
"v_accvgpr_read_b32 v32, a48\n"
"v_accvgpr_read_b32 v33, a49\n"
"v_mul_f32 v52, v52, v4\n"
"v_mul_f32 v53, v53, v5\n"
"v_mul_f32 v54, v54, v6\n"
"v_mul_f32 v55, v55, v7\n"
"v_cvt_pk_bf16_f32 v49, v50, v51\n"
"v_accvgpr_read_b32 v60, a44\n"
"v_accvgpr_read_b32 v61, a45\n"
"v_accvgpr_read_b32 v62, a46\n"
"v_accvgpr_read_b32 v63, a47\n"
"v_mul_f32 v58, v58, v6\n"
"v_mul_f32 v59, v59, v7\n"
"v_cvt_pk_bf16_f32 v56, v56, v57\n"
"v_accvgpr_read_b32 v34, a50\n"
"v_accvgpr_read_b32 v35, a51\n"
"v_mul_f32 v32, v32, v4\n"
"v_mul_f32 v33, v33, v5\n"
"v_accvgpr_read_b32 v40, a56\n"
"v_accvgpr_read_b32 v41, a57\n"
"s_mov_b32 s79, 0x1000\n"
"v_cvt_pk_bf16_f32 v50, v52, v53\n"
"v_cvt_pk_bf16_f32 v51, v54, v55\n"
"v_mul_f32 v60, v60, v4\n"
"v_mul_f32 v61, v61, v5\n"
"v_mul_f32 v62, v62, v6\n"
"v_mul_f32 v63, v63, v7\n"
"v_cvt_pk_bf16_f32 v57, v58, v59\n"
"v_accvgpr_read_b32 v36, a52\n"
"v_accvgpr_read_b32 v37, a53\n"
"v_accvgpr_read_b32 v38, a54\n"
"v_accvgpr_read_b32 v39, a55\n"
"v_mul_f32 v34, v34, v6\n"
"v_mul_f32 v35, v35, v7\n"
"v_cvt_pk_bf16_f32 v32, v32, v33\n"
"v_accvgpr_read_b32 v42, a58\n"
"v_accvgpr_read_b32 v43, a59\n"
"v_mul_f32 v40, v40, v4\n"
"v_mul_f32 v41, v41, v5\n"
"buffer_store_dwordx4 v[48:51], v203, s[44:47], s79 offen offset:0 sc1\n"
"v_cvt_pk_bf16_f32 v58, v60, v61\n"
"v_cvt_pk_bf16_f32 v59, v62, v63\n"
"v_mul_f32 v36, v36, v4\n"
"v_mul_f32 v37, v37, v5\n"
"v_mul_f32 v38, v38, v6\n"
"v_mul_f32 v39, v39, v7\n"
"v_cvt_pk_bf16_f32 v33, v34, v35\n"
"v_accvgpr_read_b32 v44, a60\n"
"v_accvgpr_read_b32 v45, a61\n"
"v_accvgpr_read_b32 v46, a62\n"
"v_accvgpr_read_b32 v47, a63\n"
"v_mul_f32 v42, v42, v6\n"
"v_mul_f32 v43, v43, v7\n"
"v_cvt_pk_bf16_f32 v40, v40, v41\n"
"buffer_store_dwordx4 v[56:59], v203, s[44:47], s79 offen offset:1024 sc1\n"
"v_cvt_pk_bf16_f32 v34, v36, v37\n"
"v_cvt_pk_bf16_f32 v35, v38, v39\n"
"v_mul_f32 v44, v44, v4\n"
"v_mul_f32 v45, v45, v5\n"
"v_mul_f32 v46, v46, v6\n"
"v_mul_f32 v47, v47, v7\n"
"v_cvt_pk_bf16_f32 v41, v42, v43\n"
"buffer_store_dwordx4 v[32:35], v203, s[44:47], s79 offen offset:2048 sc1\n"
"v_cvt_pk_bf16_f32 v42, v44, v45\n"
"v_cvt_pk_bf16_f32 v43, v46, v47\n"
"buffer_store_dwordx4 v[40:43], v203, s[44:47], s79 offen offset:3072 sc1\n"
"v_accvgpr_read_b32 v48, a64\n"
"v_accvgpr_read_b32 v49, a65\n"
"v_accvgpr_read_b32 v50, a66\n"
"v_accvgpr_read_b32 v51, a67\n"
"v_mul_f32 v48, v48, v8\n"
"v_mul_f32 v49, v49, v9\n"
"v_accvgpr_read_b32 v56, a72\n"
"v_accvgpr_read_b32 v57, a73\n"
"v_accvgpr_read_b32 v52, a68\n"
"v_accvgpr_read_b32 v53, a69\n"
"v_accvgpr_read_b32 v54, a70\n"
"v_accvgpr_read_b32 v55, a71\n"
"v_mul_f32 v50, v50, v10\n"
"v_mul_f32 v51, v51, v11\n"
"v_cvt_pk_bf16_f32 v48, v48, v49\n"
"v_accvgpr_read_b32 v58, a74\n"
"v_accvgpr_read_b32 v59, a75\n"
"v_mul_f32 v56, v56, v8\n"
"v_mul_f32 v57, v57, v9\n"
"v_accvgpr_read_b32 v32, a80\n"
"v_accvgpr_read_b32 v33, a81\n"
"v_mul_f32 v52, v52, v8\n"
"v_mul_f32 v53, v53, v9\n"
"v_mul_f32 v54, v54, v10\n"
"v_mul_f32 v55, v55, v11\n"
"v_cvt_pk_bf16_f32 v49, v50, v51\n"
"v_accvgpr_read_b32 v60, a76\n"
"v_accvgpr_read_b32 v61, a77\n"
"v_accvgpr_read_b32 v62, a78\n"
"v_accvgpr_read_b32 v63, a79\n"
"v_mul_f32 v58, v58, v10\n"
"v_mul_f32 v59, v59, v11\n"
"v_cvt_pk_bf16_f32 v56, v56, v57\n"
"v_accvgpr_read_b32 v34, a82\n"
"v_accvgpr_read_b32 v35, a83\n"
"v_mul_f32 v32, v32, v8\n"
"v_mul_f32 v33, v33, v9\n"
"v_accvgpr_read_b32 v40, a88\n"
"v_accvgpr_read_b32 v41, a89\n"
"s_mov_b32 s79, 0x2000\n"
"v_cvt_pk_bf16_f32 v50, v52, v53\n"
"v_cvt_pk_bf16_f32 v51, v54, v55\n"
"v_mul_f32 v60, v60, v8\n"
"v_mul_f32 v61, v61, v9\n"
"v_mul_f32 v62, v62, v10\n"
"v_mul_f32 v63, v63, v11\n"
"v_cvt_pk_bf16_f32 v57, v58, v59\n"
"v_accvgpr_read_b32 v36, a84\n"
"v_accvgpr_read_b32 v37, a85\n"
"v_accvgpr_read_b32 v38, a86\n"
"v_accvgpr_read_b32 v39, a87\n"
"v_mul_f32 v34, v34, v10\n"
"v_mul_f32 v35, v35, v11\n"
"v_cvt_pk_bf16_f32 v32, v32, v33\n"
"v_accvgpr_read_b32 v42, a90\n"
"v_accvgpr_read_b32 v43, a91\n"
"v_mul_f32 v40, v40, v8\n"
"v_mul_f32 v41, v41, v9\n"
"buffer_store_dwordx4 v[48:51], v203, s[44:47], s79 offen offset:0 sc1\n"
"v_cvt_pk_bf16_f32 v58, v60, v61\n"
"v_cvt_pk_bf16_f32 v59, v62, v63\n"
"v_mul_f32 v36, v36, v8\n"
"v_mul_f32 v37, v37, v9\n"
"v_mul_f32 v38, v38, v10\n"
"v_mul_f32 v39, v39, v11\n"
"v_cvt_pk_bf16_f32 v33, v34, v35\n"
"v_accvgpr_read_b32 v44, a92\n"
"v_accvgpr_read_b32 v45, a93\n"
"v_accvgpr_read_b32 v46, a94\n"
"v_accvgpr_read_b32 v47, a95\n"
"v_mul_f32 v42, v42, v10\n"
"v_mul_f32 v43, v43, v11\n"
"v_cvt_pk_bf16_f32 v40, v40, v41\n"
"buffer_store_dwordx4 v[56:59], v203, s[44:47], s79 offen offset:1024 sc1\n"
"v_cvt_pk_bf16_f32 v34, v36, v37\n"
"v_cvt_pk_bf16_f32 v35, v38, v39\n"
"v_mul_f32 v44, v44, v8\n"
"v_mul_f32 v45, v45, v9\n"
"v_mul_f32 v46, v46, v10\n"
"v_mul_f32 v47, v47, v11\n"
"v_cvt_pk_bf16_f32 v41, v42, v43\n"
"buffer_store_dwordx4 v[32:35], v203, s[44:47], s79 offen offset:2048 sc1\n"
"v_cvt_pk_bf16_f32 v42, v44, v45\n"
"v_cvt_pk_bf16_f32 v43, v46, v47\n"
"buffer_store_dwordx4 v[40:43], v203, s[44:47], s79 offen offset:3072 sc1\n"
"v_accvgpr_read_b32 v48, a96\n"
"v_accvgpr_read_b32 v49, a97\n"
"v_accvgpr_read_b32 v50, a98\n"
"v_accvgpr_read_b32 v51, a99\n"
"v_pk_mul_f32 v[48:49], v[48:49], v[12:13]\n"
"v_accvgpr_read_b32 v56, a104\n"
"v_accvgpr_read_b32 v57, a105\n"
"v_accvgpr_read_b32 v52, a100\n"
"v_accvgpr_read_b32 v53, a101\n"
"v_accvgpr_read_b32 v54, a102\n"
"v_accvgpr_read_b32 v55, a103\n"
"v_pk_mul_f32 v[50:51], v[50:51], v[14:15]\n"
"v_cvt_pk_bf16_f32 v48, v48, v49\n"
"v_accvgpr_read_b32 v58, a106\n"
"v_accvgpr_read_b32 v59, a107\n"
"v_pk_mul_f32 v[56:57], v[56:57], v[12:13]\n"
"v_accvgpr_read_b32 v32, a112\n"
"v_accvgpr_read_b32 v33, a113\n"
"v_max3_f32 v25, v180, v181, v182\n"
"v_pk_mul_f32 v[52:53], v[52:53], v[12:13]\n"
"v_pk_mul_f32 v[54:55], v[54:55], v[14:15]\n"
"v_cvt_pk_bf16_f32 v49, v50, v51\n"
"v_accvgpr_read_b32 v60, a108\n"
"v_accvgpr_read_b32 v61, a109\n"
"v_accvgpr_read_b32 v62, a110\n"
"v_accvgpr_read_b32 v63, a111\n"
"v_pk_mul_f32 v[58:59], v[58:59], v[14:15]\n"
"v_cvt_pk_bf16_f32 v56, v56, v57\n"
"v_accvgpr_read_b32 v34, a114\n"
"v_accvgpr_read_b32 v35, a115\n"
"v_pk_mul_f32 v[32:33], v[32:33], v[12:13]\n"
"v_accvgpr_read_b32 v40, a120\n"
"v_accvgpr_read_b32 v41, a121\n"
"v_max3_f32 v25, v25, v183, v184\n"
"s_mov_b32 s79, 0x3000\n"
"v_cvt_pk_bf16_f32 v50, v52, v53\n"
"v_cvt_pk_bf16_f32 v51, v54, v55\n"
"v_pk_mul_f32 v[60:61], v[60:61], v[12:13]\n"
"v_pk_mul_f32 v[62:63], v[62:63], v[14:15]\n"
"v_cvt_pk_bf16_f32 v57, v58, v59\n"
"v_accvgpr_read_b32 v36, a116\n"
"v_accvgpr_read_b32 v37, a117\n"
"v_accvgpr_read_b32 v38, a118\n"
"v_accvgpr_read_b32 v39, a119\n"
"v_pk_mul_f32 v[34:35], v[34:35], v[14:15]\n"
"v_cvt_pk_bf16_f32 v32, v32, v33\n"
"v_accvgpr_read_b32 v42, a122\n"
"v_accvgpr_read_b32 v43, a123\n"
"v_pk_mul_f32 v[40:41], v[40:41], v[12:13]\n"
"v_fma_f32 v25, v180, 0, v25\n"
"buffer_store_dwordx4 v[48:51], v203, s[44:47], s79 offen offset:0 sc1\n"
"v_cvt_pk_bf16_f32 v58, v60, v61\n"
"v_cvt_pk_bf16_f32 v59, v62, v63\n"
"v_pk_mul_f32 v[36:37], v[36:37], v[12:13]\n"
"v_pk_mul_f32 v[38:39], v[38:39], v[14:15]\n"
"v_cvt_pk_bf16_f32 v33, v34, v35\n"
"v_accvgpr_read_b32 v44, a124\n"
"v_accvgpr_read_b32 v45, a125\n"
"v_accvgpr_read_b32 v46, a126\n"
"v_accvgpr_read_b32 v47, a127\n"
"v_pk_mul_f32 v[42:43], v[42:43], v[14:15]\n"
"v_cvt_pk_bf16_f32 v40, v40, v41\n"
"v_fma_f32 v25, v181, 0, v25\n"
"buffer_store_dwordx4 v[56:59], v203, s[44:47], s79 offen offset:1024 sc1\n"
"v_cvt_pk_bf16_f32 v34, v36, v37\n"
"v_cvt_pk_bf16_f32 v35, v38, v39\n"
"v_pk_mul_f32 v[44:45], v[44:45], v[12:13]\n"
"v_pk_mul_f32 v[46:47], v[46:47], v[14:15]\n"
"v_cvt_pk_bf16_f32 v41, v42, v43\n"
"v_fma_f32 v25, v182, 0, v25\n"
"buffer_store_dwordx4 v[32:35], v203, s[44:47], s79 offen offset:2048 sc1\n"
"v_cvt_pk_bf16_f32 v42, v44, v45\n"
"v_cvt_pk_bf16_f32 v43, v46, v47\n"
"v_fma_f32 v25, v183, 0, v25\n"
"buffer_store_dwordx4 v[40:43], v203, s[44:47], s79 offen offset:3072 sc1\n"
"v_accvgpr_read_b32 v48, a128\n"
"v_accvgpr_read_b32 v49, a129\n"
"v_fma_f32 v25, v184, 0, v25\n"
"v_accvgpr_read_b32 v50, a130\n"
"v_accvgpr_read_b32 v51, a131\n"
"s_waitcnt lgkmcnt(0)\n"
"v_pk_mul_f32 v[48:49], v[48:49], v[16:17]\n"
"v_accvgpr_read_b32 v56, a136\n"
"v_accvgpr_read_b32 v57, a137\n"
"v_cmp_ngt_f32 vcc, 0x5d800000, v25\n"
"v_accvgpr_read_b32 v52, a132\n"
"v_accvgpr_read_b32 v53, a133\n"
"v_accvgpr_read_b32 v54, a134\n"
"v_accvgpr_read_b32 v55, a135\n"
"v_pk_mul_f32 v[50:51], v[50:51], v[18:19]\n"
"v_cvt_pk_bf16_f32 v48, v48, v49\n"
"v_accvgpr_read_b32 v58, a138\n"
"v_accvgpr_read_b32 v59, a139\n"
"v_pk_mul_f32 v[56:57], v[56:57], v[16:17]\n"
"v_accvgpr_read_b32 v32, a144\n"
"v_accvgpr_read_b32 v33, a145\n"
"v_pk_mul_f32 v[52:53], v[52:53], v[16:17]\n"
"v_pk_mul_f32 v[54:55], v[54:55], v[18:19]\n"
"v_cvt_pk_bf16_f32 v49, v50, v51\n"
"v_accvgpr_read_b32 v60, a140\n"
"v_accvgpr_read_b32 v61, a141\n"
"v_accvgpr_read_b32 v62, a142\n"
"v_accvgpr_read_b32 v63, a143\n"
"v_pk_mul_f32 v[58:59], v[58:59], v[18:19]\n"
"v_cvt_pk_bf16_f32 v56, v56, v57\n"
"v_accvgpr_read_b32 v34, a146\n"
"v_accvgpr_read_b32 v35, a147\n"
"v_pk_mul_f32 v[32:33], v[32:33], v[16:17]\n"
"v_accvgpr_read_b32 v40, a152\n"
"v_accvgpr_read_b32 v41, a153\n"
"s_mov_b32 s79, 0x4000\n"
"v_cvt_pk_bf16_f32 v50, v52, v53\n"
"v_cvt_pk_bf16_f32 v51, v54, v55\n"
"v_pk_mul_f32 v[60:61], v[60:61], v[16:17]\n"
"v_pk_mul_f32 v[62:63], v[62:63], v[18:19]\n"
"v_cvt_pk_bf16_f32 v57, v58, v59\n"
"v_accvgpr_read_b32 v36, a148\n"
"v_accvgpr_read_b32 v37, a149\n"
"v_accvgpr_read_b32 v38, a150\n"
"v_accvgpr_read_b32 v39, a151\n"
"v_pk_mul_f32 v[34:35], v[34:35], v[18:19]\n"
"v_cvt_pk_bf16_f32 v32, v32, v33\n"
"v_accvgpr_read_b32 v42, a154\n"
"v_accvgpr_read_b32 v43, a155\n"
"v_pk_mul_f32 v[40:41], v[40:41], v[16:17]\n"
"buffer_store_dwordx4 v[48:51], v203, s[44:47], s79 offen offset:0 sc1\n"
"v_cvt_pk_bf16_f32 v58, v60, v61\n"
"v_cvt_pk_bf16_f32 v59, v62, v63\n"
"v_pk_mul_f32 v[36:37], v[36:37], v[16:17]\n"
"v_pk_mul_f32 v[38:39], v[38:39], v[18:19]\n"
"v_cvt_pk_bf16_f32 v33, v34, v35\n"
"v_accvgpr_read_b32 v44, a156\n"
"v_accvgpr_read_b32 v45, a157\n"
"v_accvgpr_read_b32 v46, a158\n"
"v_accvgpr_read_b32 v47, a159\n"
"v_pk_mul_f32 v[42:43], v[42:43], v[18:19]\n"
"v_cvt_pk_bf16_f32 v40, v40, v41\n"
"buffer_store_dwordx4 v[56:59], v203, s[44:47], s79 offen offset:1024 sc1\n"
"v_cvt_pk_bf16_f32 v34, v36, v37\n"
"v_cvt_pk_bf16_f32 v35, v38, v39\n"
"v_pk_mul_f32 v[44:45], v[44:45], v[16:17]\n"
"v_pk_mul_f32 v[46:47], v[46:47], v[18:19]\n"
"v_cvt_pk_bf16_f32 v41, v42, v43\n"
"s_cmp_lg_u64 vcc, 0\ns_cselect_b32 s22, 1, 0\ns_andn2_b32 s22, s22, s85\ns_lshl_b32 s23, s19, 2\ns_add_u32 s23, s23, 161792\n"
"v_add_u32 v210, s97, v204\n"
"v_min_u32 v218, 1, v213\n"
"buffer_store_dwordx4 v[32:35], v203, s[44:47], s79 offen offset:2048 sc1\n"
"v_cvt_pk_bf16_f32 v42, v44, v45\n"
"v_cvt_pk_bf16_f32 v43, v46, v47\n"
"v_mov_b32 v25, s22\n"
"v_mov_b32 v26, s23\n"
"v_add_u32 v204, s96, v204\n"
"v_add_u32 v201, 0x10000, v200\n"
"v_mad_u32_u24 v206, v218, s94, v252\n"
"buffer_store_dwordx4 v[40:43], v203, s[44:47], s79 offen offset:3072 sc1\n"
"ds_write_b32 v26, v25 offset:0\n"
"s_waitcnt lgkmcnt(0)\n"
"s_waitcnt vmcnt(8)\n"
"s_nop 7\n"
"s_branch L_join_%=\n"
"L_fb_%=:\n"
"s_waitcnt vmcnt(0)\n"
"s_waitcnt lgkmcnt(0)\n"
"s_barrier\n"
"s_mov_b32 s85, 1\n"
"s_branch L_start_%=\n"
"L_fbm_%=:\n"
"s_waitcnt vmcnt(0)\n"
"s_waitcnt lgkmcnt(0)\n"
"s_mov_b32 s75, s78\n"
"s_mov_b32 s77, 16\n"
"v_mov_b32 v180, 0xff800000\n"
"v_mov_b32 v181, 0xff800000\n"
"v_mov_b32 v182, 0xff800000\n"
"v_mov_b32 v183, 0xff800000\n"
"v_mov_b32 v184, 0xff800000\n"
"L_fbl_%=:\n"
"s_barrier\n"
"s_add_u32 m0, s78, 0\n"
"s_nop 0\n"
"buffer_load_dwordx4 v198, s[52:55], s75 offen offset:0 nt lds\n"
"buffer_load_dwordx4 v198, s[52:55], s75 offen offset:2048 nt lds\n"
"s_add_u32 m0, s78, 0x380\n"
"s_nop 0\n"
"buffer_load_dwordx4 v198, s[52:55], s75 offen offset:128 nt lds\n"
"buffer_load_dwordx4 v198, s[52:55], s75 offen offset:2176 nt lds\n"
"s_add_u32 s75, s75, 0x4000\n"
"s_waitcnt vmcnt(0)\n"
"s_barrier\n"
"ds_read_b128 a[160:163], v186 offset:0\n"
"ds_read_b128 a[164:167], v187 offset:0\n"
"ds_read_b128 a[168:171], v186 offset:1024\n"
"ds_read_b128 a[172:175], v187 offset:1024\n"
"ds_read_b128 a[176:179], v186 offset:4096\n"
"ds_read_b128 a[180:183], v187 offset:4096\n"
"ds_read_b128 a[184:187], v186 offset:5120\n"
"ds_read_b128 a[188:191], v187 offset:5120\n"
"ds_read_b128 a[192:195], v186 offset:8192\n"
"ds_read_b128 a[196:199], v187 offset:8192\n"
"ds_read_b128 a[200:203], v186 offset:9216\n"
"ds_read_b128 a[204:207], v187 offset:9216\n"
"ds_read_b128 a[208:211], v186 offset:12288\n"
"ds_read_b128 a[212:215], v187 offset:12288\n"
"ds_read_b128 a[216:219], v186 offset:13312\n"
"ds_read_b128 a[220:223], v187 offset:13312\n"
"s_waitcnt lgkmcnt(0)\n"
"v_mfma_f32_16x16x32_bf16 v[112:115], a[160:163], v[0:3], 0\n"
"v_mfma_f32_16x16x32_bf16 v[112:115], a[164:167], v[4:7], v[112:115]\n"
"v_mfma_f32_16x16x32_bf16 v[112:115], a[168:171], v[8:11], v[112:115]\n"
"v_mfma_f32_16x16x32_bf16 v[112:115], a[172:175], v[12:15], v[112:115]\n"
"v_mfma_f32_16x16x32_bf16 v[116:119], a[176:179], v[0:3], 0\n"
"v_mfma_f32_16x16x32_bf16 v[116:119], a[180:183], v[4:7], v[116:119]\n"
"v_mfma_f32_16x16x32_bf16 v[116:119], a[184:187], v[8:11], v[116:119]\n"
"v_mfma_f32_16x16x32_bf16 v[116:119], a[188:191], v[12:15], v[116:119]\n"
"v_mfma_f32_16x16x32_bf16 v[120:123], a[192:195], v[0:3], 0\n"
"v_mfma_f32_16x16x32_bf16 v[120:123], a[196:199], v[4:7], v[120:123]\n"
"v_mfma_f32_16x16x32_bf16 v[120:123], a[200:203], v[8:11], v[120:123]\n"
"v_mfma_f32_16x16x32_bf16 v[120:123], a[204:207], v[12:15], v[120:123]\n"
"v_mfma_f32_16x16x32_bf16 v[124:127], a[208:211], v[0:3], 0\n"
"v_mfma_f32_16x16x32_bf16 v[124:127], a[212:215], v[4:7], v[124:127]\n"
"v_mfma_f32_16x16x32_bf16 v[124:127], a[216:219], v[8:11], v[124:127]\n"
"v_mfma_f32_16x16x32_bf16 v[124:127], a[220:223], v[12:15], v[124:127]\n"
"v_max3_f32 v180, v180, v112, v113\n"
"v_max3_f32 v180, v180, v114, v115\n"
"v_max3_f32 v180, v180, v116, v117\n"
"v_max3_f32 v180, v180, v118, v119\n"
"v_max3_f32 v180, v180, v120, v121\n"
"v_max3_f32 v180, v180, v122, v123\n"
"s_nop 1\n"
"v_max3_f32 v180, v180, v124, v125\n"
"v_max3_f32 v180, v180, v126, v127\n"
"v_mfma_f32_16x16x32_bf16 v[112:115], a[160:163], v[16:19], 0\n"
"v_mfma_f32_16x16x32_bf16 v[112:115], a[164:167], v[20:23], v[112:115]\n"
"v_mfma_f32_16x16x32_bf16 v[112:115], a[168:171], v[24:27], v[112:115]\n"
"v_mfma_f32_16x16x32_bf16 v[112:115], a[172:175], v[28:31], v[112:115]\n"
"v_mfma_f32_16x16x32_bf16 v[116:119], a[176:179], v[16:19], 0\n"
"v_mfma_f32_16x16x32_bf16 v[116:119], a[180:183], v[20:23], v[116:119]\n"
"v_mfma_f32_16x16x32_bf16 v[116:119], a[184:187], v[24:27], v[116:119]\n"
"v_mfma_f32_16x16x32_bf16 v[116:119], a[188:191], v[28:31], v[116:119]\n"
"v_mfma_f32_16x16x32_bf16 v[120:123], a[192:195], v[16:19], 0\n"
"v_mfma_f32_16x16x32_bf16 v[120:123], a[196:199], v[20:23], v[120:123]\n"
"v_mfma_f32_16x16x32_bf16 v[120:123], a[200:203], v[24:27], v[120:123]\n"
"v_mfma_f32_16x16x32_bf16 v[120:123], a[204:207], v[28:31], v[120:123]\n"
"v_mfma_f32_16x16x32_bf16 v[124:127], a[208:211], v[16:19], 0\n"
"v_mfma_f32_16x16x32_bf16 v[124:127], a[212:215], v[20:23], v[124:127]\n"
"v_mfma_f32_16x16x32_bf16 v[124:127], a[216:219], v[24:27], v[124:127]\n"
"v_mfma_f32_16x16x32_bf16 v[124:127], a[220:223], v[28:31], v[124:127]\n"
"v_max3_f32 v181, v181, v112, v113\n"
"v_max3_f32 v181, v181, v114, v115\n"
"v_max3_f32 v181, v181, v116, v117\n"
"v_max3_f32 v181, v181, v118, v119\n"
"v_max3_f32 v181, v181, v120, v121\n"
"v_max3_f32 v181, v181, v122, v123\n"
"s_nop 1\n"
"v_max3_f32 v181, v181, v124, v125\n"
"v_max3_f32 v181, v181, v126, v127\n"
"v_mfma_f32_16x16x32_bf16 v[112:115], a[160:163], v[32:35], 0\n"
"v_mfma_f32_16x16x32_bf16 v[112:115], a[164:167], v[36:39], v[112:115]\n"
"v_mfma_f32_16x16x32_bf16 v[112:115], a[168:171], v[40:43], v[112:115]\n"
"v_mfma_f32_16x16x32_bf16 v[112:115], a[172:175], v[44:47], v[112:115]\n"
"v_mfma_f32_16x16x32_bf16 v[116:119], a[176:179], v[32:35], 0\n"
"v_mfma_f32_16x16x32_bf16 v[116:119], a[180:183], v[36:39], v[116:119]\n"
"v_mfma_f32_16x16x32_bf16 v[116:119], a[184:187], v[40:43], v[116:119]\n"
"v_mfma_f32_16x16x32_bf16 v[116:119], a[188:191], v[44:47], v[116:119]\n"
"v_mfma_f32_16x16x32_bf16 v[120:123], a[192:195], v[32:35], 0\n"
"v_mfma_f32_16x16x32_bf16 v[120:123], a[196:199], v[36:39], v[120:123]\n"
"v_mfma_f32_16x16x32_bf16 v[120:123], a[200:203], v[40:43], v[120:123]\n"
"v_mfma_f32_16x16x32_bf16 v[120:123], a[204:207], v[44:47], v[120:123]\n"
"v_mfma_f32_16x16x32_bf16 v[124:127], a[208:211], v[32:35], 0\n"
"v_mfma_f32_16x16x32_bf16 v[124:127], a[212:215], v[36:39], v[124:127]\n"
"v_mfma_f32_16x16x32_bf16 v[124:127], a[216:219], v[40:43], v[124:127]\n"
"v_mfma_f32_16x16x32_bf16 v[124:127], a[220:223], v[44:47], v[124:127]\n"
"v_max3_f32 v182, v182, v112, v113\n"
"v_max3_f32 v182, v182, v114, v115\n"
"v_max3_f32 v182, v182, v116, v117\n"
"v_max3_f32 v182, v182, v118, v119\n"
"v_max3_f32 v182, v182, v120, v121\n"
"v_max3_f32 v182, v182, v122, v123\n"
"s_nop 1\n"
"v_max3_f32 v182, v182, v124, v125\n"
"v_max3_f32 v182, v182, v126, v127\n"
"v_mfma_f32_16x16x32_bf16 v[112:115], a[160:163], v[48:51], 0\n"
"v_mfma_f32_16x16x32_bf16 v[112:115], a[164:167], v[52:55], v[112:115]\n"
"v_mfma_f32_16x16x32_bf16 v[112:115], a[168:171], v[56:59], v[112:115]\n"
"v_mfma_f32_16x16x32_bf16 v[112:115], a[172:175], v[60:63], v[112:115]\n"
"v_mfma_f32_16x16x32_bf16 v[116:119], a[176:179], v[48:51], 0\n"
"v_mfma_f32_16x16x32_bf16 v[116:119], a[180:183], v[52:55], v[116:119]\n"
"v_mfma_f32_16x16x32_bf16 v[116:119], a[184:187], v[56:59], v[116:119]\n"
"v_mfma_f32_16x16x32_bf16 v[116:119], a[188:191], v[60:63], v[116:119]\n"
"v_mfma_f32_16x16x32_bf16 v[120:123], a[192:195], v[48:51], 0\n"
"v_mfma_f32_16x16x32_bf16 v[120:123], a[196:199], v[52:55], v[120:123]\n"
"v_mfma_f32_16x16x32_bf16 v[120:123], a[200:203], v[56:59], v[120:123]\n"
"v_mfma_f32_16x16x32_bf16 v[120:123], a[204:207], v[60:63], v[120:123]\n"
"v_mfma_f32_16x16x32_bf16 v[124:127], a[208:211], v[48:51], 0\n"
"v_mfma_f32_16x16x32_bf16 v[124:127], a[212:215], v[52:55], v[124:127]\n"
"v_mfma_f32_16x16x32_bf16 v[124:127], a[216:219], v[56:59], v[124:127]\n"
"v_mfma_f32_16x16x32_bf16 v[124:127], a[220:223], v[60:63], v[124:127]\n"
"v_max3_f32 v183, v183, v112, v113\n"
"v_max3_f32 v183, v183, v114, v115\n"
"v_max3_f32 v183, v183, v116, v117\n"
"v_max3_f32 v183, v183, v118, v119\n"
"v_max3_f32 v183, v183, v120, v121\n"
"v_max3_f32 v183, v183, v122, v123\n"
"s_nop 1\n"
"v_max3_f32 v183, v183, v124, v125\n"
"v_max3_f32 v183, v183, v126, v127\n"
"v_mfma_f32_16x16x32_bf16 v[112:115], a[160:163], v[64:67], 0\n"
"v_mfma_f32_16x16x32_bf16 v[112:115], a[164:167], v[68:71], v[112:115]\n"
"v_mfma_f32_16x16x32_bf16 v[112:115], a[168:171], v[72:75], v[112:115]\n"
"v_mfma_f32_16x16x32_bf16 v[112:115], a[172:175], v[76:79], v[112:115]\n"
"v_mfma_f32_16x16x32_bf16 v[116:119], a[176:179], v[64:67], 0\n"
"v_mfma_f32_16x16x32_bf16 v[116:119], a[180:183], v[68:71], v[116:119]\n"
"v_mfma_f32_16x16x32_bf16 v[116:119], a[184:187], v[72:75], v[116:119]\n"
"v_mfma_f32_16x16x32_bf16 v[116:119], a[188:191], v[76:79], v[116:119]\n"
"v_mfma_f32_16x16x32_bf16 v[120:123], a[192:195], v[64:67], 0\n"
"v_mfma_f32_16x16x32_bf16 v[120:123], a[196:199], v[68:71], v[120:123]\n"
"v_mfma_f32_16x16x32_bf16 v[120:123], a[200:203], v[72:75], v[120:123]\n"
"v_mfma_f32_16x16x32_bf16 v[120:123], a[204:207], v[76:79], v[120:123]\n"
"v_mfma_f32_16x16x32_bf16 v[124:127], a[208:211], v[64:67], 0\n"
"v_mfma_f32_16x16x32_bf16 v[124:127], a[212:215], v[68:71], v[124:127]\n"
"v_mfma_f32_16x16x32_bf16 v[124:127], a[216:219], v[72:75], v[124:127]\n"
"v_mfma_f32_16x16x32_bf16 v[124:127], a[220:223], v[76:79], v[124:127]\n"
"v_max3_f32 v184, v184, v112, v113\n"
"v_max3_f32 v184, v184, v114, v115\n"
"v_max3_f32 v184, v184, v116, v117\n"
"v_max3_f32 v184, v184, v118, v119\n"
"v_max3_f32 v184, v184, v120, v121\n"
"v_max3_f32 v184, v184, v122, v123\n"
"s_nop 1\n"
"v_max3_f32 v184, v184, v124, v125\n"
"v_max3_f32 v184, v184, v126, v127\n"
"s_sub_u32 s77, s77, 1\n"
"s_cmp_lg_u32 s77, 0\n"
"s_cbranch_scc1 L_fbl_%=\n"
"v_mov_b32 v140, v180\n"
"s_nop 1\n"
"v_permlane16_swap_b32 v180, v140\n"
"v_max_f32 v180, v180, v140\n"
"v_mov_b32 v140, v180\n"
"s_nop 1\n"
"v_permlane32_swap_b32 v180, v140\n"
"v_min_f32_e64 v160, -v180, -v140\n"
"v_min_f32_e64 v161, -v180, -v140\n"
"v_min_f32_e64 v162, -v180, -v140\n"
"v_min_f32_e64 v163, -v180, -v140\n"
"v_mov_b32 v180, 0\n"
"v_mov_b32 v140, v181\n"
"s_nop 1\n"
"v_permlane16_swap_b32 v181, v140\n"
"v_max_f32 v181, v181, v140\n"
"v_mov_b32 v140, v181\n"
"s_nop 1\n"
"v_permlane32_swap_b32 v181, v140\n"
"v_min_f32_e64 v164, -v181, -v140\n"
"v_min_f32_e64 v165, -v181, -v140\n"
"v_min_f32_e64 v166, -v181, -v140\n"
"v_min_f32_e64 v167, -v181, -v140\n"
"v_mov_b32 v181, 0\n"
"v_mov_b32 v140, v182\n"
"s_nop 1\n"
"v_permlane16_swap_b32 v182, v140\n"
"v_max_f32 v182, v182, v140\n"
"v_mov_b32 v140, v182\n"
"s_nop 1\n"
"v_permlane32_swap_b32 v182, v140\n"
"v_min_f32_e64 v168, -v182, -v140\n"
"v_min_f32_e64 v169, -v182, -v140\n"
"v_min_f32_e64 v170, -v182, -v140\n"
"v_min_f32_e64 v171, -v182, -v140\n"
"v_mov_b32 v182, 0\n"
"v_mov_b32 v140, v183\n"
"s_nop 1\n"
"v_permlane16_swap_b32 v183, v140\n"
"v_max_f32 v183, v183, v140\n"
"v_mov_b32 v140, v183\n"
"s_nop 1\n"
"v_permlane32_swap_b32 v183, v140\n"
"v_min_f32_e64 v172, -v183, -v140\n"
"v_min_f32_e64 v173, -v183, -v140\n"
"v_min_f32_e64 v174, -v183, -v140\n"
"v_min_f32_e64 v175, -v183, -v140\n"
"v_mov_b32 v183, 0\n"
"v_mov_b32 v140, v184\n"
"s_nop 1\n"
"v_permlane16_swap_b32 v184, v140\n"
"v_max_f32 v184, v184, v140\n"
"v_mov_b32 v140, v184\n"
"s_nop 1\n"
"v_permlane32_swap_b32 v184, v140\n"
"v_min_f32_e64 v176, -v184, -v140\n"
"v_min_f32_e64 v177, -v184, -v140\n"
"v_min_f32_e64 v178, -v184, -v140\n"
"v_min_f32_e64 v179, -v184, -v140\n"
"v_mov_b32 v184, 0\n"
"s_barrier\n"
"s_mov_b32 s75, s78\n"
"s_add_u32 m0, s78, 0\n"
"s_nop 0\n"
"buffer_load_dwordx4 v198, s[52:55], s75 offen offset:0 nt lds\n"
"buffer_load_dwordx4 v198, s[52:55], s75 offen offset:2048 nt lds\n"
"s_add_u32 m0, s78, 0x380\n"
"s_nop 0\n"
"buffer_load_dwordx4 v198, s[52:55], s75 offen offset:128 nt lds\n"
"buffer_load_dwordx4 v198, s[52:55], s75 offen offset:2176 nt lds\n"
"s_add_u32 m0, s78, 0x4000\n"
"s_nop 0\n"
"buffer_load_dwordx4 v198, s[56:59], s75 offen offset:0 nt lds\n"
"buffer_load_dwordx4 v198, s[56:59], s75 offen offset:2048 nt lds\n"
"s_add_u32 m0, s78, 0x4380\n"
"s_nop 0\n"
"buffer_load_dwordx4 v198, s[56:59], s75 offen offset:128 nt lds\n"
"buffer_load_dwordx4 v198, s[56:59], s75 offen offset:2176 nt lds\n"
"s_add_u32 s75, s75, 0x4000\n"
"s_add_u32 m0, s78, 0x8000\n"
"s_nop 0\n"
"buffer_load_dwordx4 v198, s[52:55], s75 offen offset:0 nt lds\n"
"buffer_load_dwordx4 v198, s[52:55], s75 offen offset:2048 nt lds\n"
"s_add_u32 m0, s78, 0x8380\n"
"s_nop 0\n"
"buffer_load_dwordx4 v198, s[52:55], s75 offen offset:128 nt lds\n"
"buffer_load_dwordx4 v198, s[52:55], s75 offen offset:2176 nt lds\n"
"s_add_u32 m0, s78, 0xc000\n"
"s_nop 0\n"
"buffer_load_dwordx4 v198, s[56:59], s75 offen offset:0 nt lds\n"
"buffer_load_dwordx4 v198, s[56:59], s75 offen offset:2048 nt lds\n"
"s_add_u32 m0, s78, 0xc380\n"
"s_nop 0\n"
"buffer_load_dwordx4 v198, s[56:59], s75 offen offset:128 nt lds\n"
"buffer_load_dwordx4 v198, s[56:59], s75 offen offset:2176 nt lds\n"
"s_add_u32 s75, s75, 0x4000\n"
"s_waitcnt vmcnt(0)\n"
"s_barrier\n"
"ds_read_b128 a[160:163], v186 offset:0\n"
"ds_read_b128 a[164:167], v187 offset:0\n"
"ds_read_b128 a[168:171], v186 offset:1024\n"
"ds_read_b128 a[172:175], v187 offset:1024\n"
"ds_read_b128 a[176:179], v186 offset:4096\n"
"ds_read_b128 a[180:183], v187 offset:4096\n"
"ds_read_b128 a[184:187], v186 offset:5120\n"
"ds_read_b128 a[188:191], v187 offset:5120\n"
"ds_read_b128 a[192:195], v186 offset:8192\n"
"ds_read_b128 a[196:199], v187 offset:8192\n"
"ds_read_b128 a[200:203], v186 offset:9216\n"
"ds_read_b128 a[204:207], v187 offset:9216\n"
"ds_read_b128 a[208:211], v186 offset:12288\n"
"ds_read_b128 a[212:215], v187 offset:12288\n"
"ds_read_b128 a[216:219], v186 offset:13312\n"
"ds_read_b128 a[220:223], v187 offset:13312\n"
"s_waitcnt lgkmcnt(0)\n"
"s_nop 12\n"
"s_branch L_attn_%=\n"
"L_sp3_%=:\ns_mov_b32 s39, 0x400000\nL_sq3_%=:\ns_sleep 1\nbuffer_load_dword v212, v254, s[68:71], 0 offen sc1\ns_waitcnt vmcnt(0)\nv_cmp_eq_u32 vcc, s80, v212\ns_nop 4\ns_and_b32 s20, vcc_lo, 0x1\ns_cmp_eq_u32 s20, 0x1\ns_cbranch_scc1 L_bk3_%=\ns_sub_u32 s39, s39, 1\ns_cmp_lg_u32 s39, 0\ns_cbranch_scc1 L_sq3_%=\ns_branch L_bk3_%=\n"
"L_fx4_%=:\nv_mov_b32 v253, v248\ns_cmp_lg_u32 s19, 0\ns_cbranch_scc1 L_sg5_%=\ns_mov_b64 exec, 1\nbuffer_store_dword v253, v206, s[68:71], 0 offen offset:0 sc1\ns_mov_b64 exec, -1\nL_sg5_%=:\ns_cmp_lg_u32 s19, 0\ns_cbranch_scc1 L_sg6_%=\ns_mov_b64 exec, 1\nbuffer_store_dword v253, v206, s[68:71], 0 offen offset:4 sc1\ns_mov_b64 exec, -1\nL_sg6_%=:\ns_branch L_bk4_%=\n"
"L_sp7_%=:\ns_mov_b32 s39, 0x400000\nL_sq7_%=:\ns_sleep 1\nbuffer_load_dword v212, v254, s[68:71], 0 offen sc1\ns_waitcnt vmcnt(0)\nv_cmp_eq_u32 vcc, s80, v212\ns_nop 4\ns_and_b32 s20, vcc_lo, 0x2\ns_cmp_eq_u32 s20, 0x2\ns_cbranch_scc1 L_bk7_%=\ns_sub_u32 s39, s39, 1\ns_cmp_lg_u32 s39, 0\ns_cbranch_scc1 L_sq7_%=\ns_branch L_bk7_%=\n"
"L_sp10_%=:\ns_mov_b32 s39, 0x400000\nL_sq10_%=:\ns_sleep 1\nbuffer_load_dword v212, v254, s[68:71], 0 offen sc1\ns_waitcnt vmcnt(0)\nv_cmp_eq_u32 vcc, s80, v212\ns_nop 4\ns_and_b32 s20, vcc_lo, 0xff00\ns_cmp_eq_u32 s20, 0xff00\ns_cbranch_scc1 L_bk10_%=\ns_sub_u32 s39, s39, 1\ns_cmp_lg_u32 s39, 0\ns_cbranch_scc1 L_sq10_%=\ns_branch L_bk10_%=\n"
"L_fx11_%=:\nv_mov_b32 v253, v248\ns_cmp_lg_u32 s19, 0\ns_cbranch_scc1 L_sg12_%=\ns_mov_b64 exec, 0x1fe\nbuffer_store_dword v253, v206, s[68:71], 0 offen offset:0 sc1\ns_mov_b64 exec, -1\nL_sg12_%=:\ns_cmp_lg_u32 s19, 0\ns_cbranch_scc1 L_sg13_%=\ns_mov_b64 exec, 0x1fe\nbuffer_store_dword v253, v206, s[68:71], 0 offen offset:32 sc1\ns_mov_b64 exec, -1\nL_sg13_%=:\ns_branch L_bk11_%=\n"
"L_sp14_%=:\ns_mov_b32 s39, 0x400000\nL_sq14_%=:\ns_sleep 1\nbuffer_load_dword v212, v254, s[68:71], 0 offen sc1\ns_waitcnt vmcnt(0)\nv_cmp_eq_u32 vcc, s80, v212\ns_nop 4\ns_and_b32 s20, vcc_lo, 0xff0000\ns_cmp_eq_u32 s20, 0xff0000\ns_cbranch_scc1 L_bk14_%=\ns_sub_u32 s39, s39, 1\ns_cmp_lg_u32 s39, 0\ns_cbranch_scc1 L_sq14_%=\ns_branch L_bk14_%=\n"
"L_sp16_%=:\ns_mov_b32 s39, 0x400000\nL_sq16_%=:\ns_sleep 1\nbuffer_load_dword v212, v254, s[68:71], 0 offen sc1\ns_waitcnt vmcnt(0)\nv_cmp_eq_u32 vcc, s80, v212\ns_nop 4\ns_and_b32 s20, vcc_lo, 0xff000000\ns_cmp_eq_u32 s20, 0xff000000\ns_cbranch_scc1 L_bk16_%=\ns_sub_u32 s39, s39, 1\ns_cmp_lg_u32 s39, 0\ns_cbranch_scc1 L_sq16_%=\ns_branch L_bk16_%=\n"
"L_end_%=:\n"
      :
      : "s"(__builtin_amdgcn_kernarg_segment_ptr()), "s"(blockIdx.x), "s"(__builtin_amdgcn_readfirstlane(threadIdx.x >> 6))
      : "v0","v1","v2","v3","v4","v5","v6","v7","v8","v9","v10","v11","v12","v13","v14","v15","v16","v17","v18","v19","v20","v21","v22","v23","v24","v25","v26","v27","v28","v29","v30","v31","v32","v33","v34","v35","v36","v37","v38","v39","v40","v41","v42","v43","v44","v45","v46","v47","v48","v49","v50","v51","v52","v53","v54","v55","v56","v57","v58","v59","v60","v61","v62","v63","v64","v65","v66","v67","v68","v69","v70","v71","v72","v73","v74","v75","v76","v77","v78","v79","v80","v81","v82","v83","v84","v85","v86","v87","v88","v89","v90","v91","v92","v93","v94","v95","v96","v97","v98","v99","v100","v101","v102","v103","v104","v105","v106","v107","v108","v109","v110","v111","v112","v113","v114","v115","v116","v117","v118","v119","v120","v121","v122","v123","v124","v125","v126","v127","v128","v129","v130","v131","v132","v133","v134","v135","v136","v137","v138","v139","v140","v141","v142","v143","v144","v145","v146","v147","v148","v149","v150","v151","v152","v153","v154","v155","v156","v157","v158","v159","v160","v161","v162","v163","v164","v165","v166","v167","v168","v169","v170","v171","v172","v173","v174","v175","v176","v177","v178","v179","v180","v181","v182","v183","v184","v185","v186","v187","v188","v189","v190","v191","v192","v193","v194","v195","v196","v197","v198","v199","v200","v201","v202","v203","v204","v205","v206","v207","v208","v209","v210","v211","v212","v213","v214","v215","v216","v217","v218","v219","v220","v221","v222","v223","v224","v225","v226","v227","v228","v229","v230","v231","v232","v233","v234","v235","v236","v237","v238","v239","v240","v241","v242","v243","v244","v245","v246","v247","v248","v249","v250","v251","v252","v253","v254","v255","a0","a1","a2","a3","a4","a5","a6","a7","a8","a9","a10","a11","a12","a13","a14","a15","a16","a17","a18","a19","a20","a21","a22","a23","a24","a25","a26","a27","a28","a29","a30","a31","a32","a33","a34","a35","a36","a37","a38","a39","a40","a41","a42","a43","a44","a45","a46","a47","a48","a49","a50","a51","a52","a53","a54","a55","a56","a57","a58","a59","a60","a61","a62","a63","a64","a65","a66","a67","a68","a69","a70","a71","a72","a73","a74","a75","a76","a77","a78","a79","a80","a81","a82","a83","a84","a85","a86","a87","a88","a89","a90","a91","a92","a93","a94","a95","a96","a97","a98","a99","a100","a101","a102","a103","a104","a105","a106","a107","a108","a109","a110","a111","a112","a113","a114","a115","a116","a117","a118","a119","a120","a121","a122","a123","a124","a125","a126","a127","a128","a129","a130","a131","a132","a133","a134","a135","a136","a137","a138","a139","a140","a141","a142","a143","a144","a145","a146","a147","a148","a149","a150","a151","a152","a153","a154","a155","a156","a157","a158","a159","a160","a161","a162","a163","a164","a165","a166","a167","a168","a169","a170","a171","a172","a173","a174","a175","a176","a177","a178","a179","a180","a181","a182","a183","a184","a185","a186","a187","a188","a189","a190","a191","a192","a193","a194","a195","a196","a197","a198","a199","a200","a201","a202","a203","a204","a205","a206","a207","a208","a209","a210","a211","a212","a213","a214","a215","a216","a217","a218","a219","a220","a221","a222","a223","a224","a225","a226","a227","a228","a229","a230","a231","a232","a233","a234","a235","a236","a237","a238","a239","a240","a241","a242","a243","a244","a245","a246","a247","a248","a249","a250","a251","a252","a253","a254","a255","s16","s17","s18","s19","s20","s21","s22","s23","s24","s25","s26","s27","s28","s29","s30","s31","s33","s34","s35","s36","s37","s38","s39","s40","s41","s42","s43","s44","s45","s46","s47","s48","s49","s50","s51","s52","s53","s54","s55","s56","s57","s58","s59","s60","s61","s62","s63","s64","s65","s66","s67","s68","s69","s70","s71","s72","s73","s74","s75","s76","s77","s78","s79","s80","s81","s82","s83","s84","s85","s86","s87","s88","s89","s90","s91","s92","s93","s94","s95","s96","s97","s98","s99","vcc","scc","memory");
}

extern "C" void kernel_launch(void* const* d_in, const int* in_sizes, int n_in, void* d_out, int out_size, void* d_ws, size_t ws_size,
                              hipStream_t stream) {
  (void)hipFuncSetAttribute((const void*)gqa_fused, hipFuncAttributeMaxDynamicSharedMemorySize, 163840);
  hipLaunchKernelGGL(gqa_fused, dim3(256), dim3(256), 163840, stream, d_in[0], d_in[1], d_in[2], d_in[3], d_in[4], d_in[5], d_in[6], d_in[7],
                     d_in[8], d_in[9], d_out, d_ws);
}
```
